```python
import torch

def _softmax(x, axis=-1):
    return torch.softmax(x, dim=axis)

def _gelu(x):
    return torch.nn.functional.gelu(x, approximate='tanh')

def _silu(x):
    return torch.nn.functional.silu(x)

def _sigmoid(x):
    return torch.sigmoid(x)

def _layernorm(x, axis=-1, eps=1e-05):
    m = x.mean(dim=axis, keepdim=True)
    v = x.var(dim=axis, keepdim=True, unbiased=False)
    return (x - m) / torch.sqrt(v + eps)

def _scatter_add(tgt, idx, src, axis=0):
    return tgt.clone().index_add_(axis, idx, src)

def _segment_sum(src, seg, n):
    out = torch.zeros((n,) + src.shape[1:], dtype=src.dtype)
    return out.index_add_(0, seg, src)

def _pad(x, pw):
    flat = tuple((v for pair in reversed(pw) for v in pair))
    return torch.nn.functional.pad(x, flat)
kernel_name = 'recurrent_scan__ssm_conv_h3__mixer_gmlp__prompt'

def setup_inputs():

    _w = lambda a: (lambda t: t.to(torch.bfloat16) if t.is_floating_point() else t)(torch.as_tensor(a))

    def _A_setup():
        B, S, D = (1, 2048, 576)
        ks = 4
        return {'q': _w((lambda *s: torch.randn(*s, dtype=torch.bfloat16) * 0.05)(B, S, D)), 'k': _w((lambda *s: torch.randn(*s, dtype=torch.bfloat16) * 0.2)(B, S, D)), 'v': _w((lambda *s: torch.randn(*s, dtype=torch.bfloat16) * 0.2)(B, S, D)), 'shift_k': _w((lambda *s: torch.randn(*s, dtype=torch.bfloat16) * 0.2)(D, ks) * 0.5), 'ssm_k': _w((lambda *s: torch.randn(*s, dtype=torch.bfloat16) * 0.2)(D, S) * (1.0 / S ** 0.5)), 'ssm_D': _w((lambda *s: torch.randn(*s, dtype=torch.bfloat16) * 0.2)(D))}
    _A_in = _A_setup()

    def _B_setup():
        B, S, D, F = (1, 2048, 576, 4608)
        Fh = F // 2
        return {'x': _w((lambda *s: torch.randn(*s, dtype=torch.bfloat16) * 0.05)(B, S, D)), 'w_in': _w((lambda *s: torch.randn(*s, dtype=torch.bfloat16) * 0.4)(D, 2 * Fh)), 'w_sp': _w(0.01 * (lambda *s: torch.randn(*s, dtype=torch.bfloat16) * 0.4)(S, S)), 'b_sp': _w(1.0 + 0.01 * (lambda *s: torch.randn(*s, dtype=torch.bfloat16) * 0.1)(S)), 'w_out': _w((lambda *s: torch.randn(*s, dtype=torch.bfloat16) * 0.1)(Fh, D))}
    _B_in = _B_setup()
    return {'A_q': _A_in['q'], 'A_k': _A_in['k'], 'A_v': _A_in['v'], 'A_shift_k': _A_in['shift_k'], 'A_ssm_k': _A_in['ssm_k'], 'A_ssm_D': _A_in['ssm_D'], 'B_x': _B_in['x'], 'B_w_in': _B_in['w_in'], 'B_w_sp': _B_in['w_sp'], 'B_b_sp': _B_in['b_sp'], 'B_w_out': _B_in['w_out']}

def reference(A_q, A_k, A_v, A_shift_k, A_ssm_k, A_ssm_D, B_x, B_w_in, B_w_sp, B_b_sp, B_w_out):

    def _A_ref(q, k, v, shift_k, ssm_k, ssm_D):
        B, S, D = q.shape
        ks = shift_k.shape[-1]
        k_pad = _pad(k, ((0, 0), (ks - 1, 0), (0, 0)))
        k_shift = torch.zeros_like(k)
        for t in range(ks):
            k_shift = k_shift + k_pad[:, t:t + S, :] * shift_k[:, ks - 1 - t][None, None, :]
        kv = k_shift * v
        L = 2 * S
        kv_pad = _pad(kv, ((0, 0), (0, S), (0, 0)))
        kf_pad = _pad(ssm_k, ((0, 0), (0, S)))
        kv_f = (lambda x, axes=(-2, -1): torch.fft.rfft2(x.float(), dim=axes))(kv_pad[:, None, :, :], axes=(1, 2))
        kf_f = (lambda x, axes=(-2, -1): torch.fft.rfft2(x.float(), dim=axes))(kf_pad[:, None, :], axes=(1, 2))
        kf_f = (lambda x, *d: x.permute(*d))(kf_f, 1, 2, 0)
        y_f = kv_f * kf_f[None, :, :, :]
        y = (lambda x, s, axes=(-2, -1): torch.fft.irfft2(x, s=s, dim=axes).to(torch.bfloat16))(y_f, (1, L), axes=(1, 2))[:, 0, :S, :]
        y = y + kv * ssm_D[None, None, :]
        return q * y

    def _B_ref(x, w_in, w_sp, b_sp, w_out):
        B, S, D = x.shape
        F = w_in.shape[-1]
        Fh = F // 2
        z = _gelu(torch.einsum('bsd,df->bsf', x, w_in))
        z1 = z[..., :Fh].clone()
        z2 = _layernorm(z[..., Fh:], axis=-1)
        g = torch.einsum('ts,bsf->btf', w_sp, z2) + b_sp[None, :, None]
        return torch.einsum('bsf,fd->bsd', z1 * g, w_out)
    x = A_q
    xn = _layernorm(x, axis=-1)
    s = _A_ref(xn, A_k, A_v, A_shift_k, A_ssm_k, A_ssm_D)
    g = _silu(_B_ref(xn, B_w_in, B_w_sp, B_b_sp, B_w_out))
    return x + s * g
```

```cpp
#include <hip/hip_runtime.h>
#include <hip/hip_bf16.h>
#include <stdint.h>

typedef unsigned short u16;
typedef __attribute__((ext_vector_type(8))) short bf16x8;
typedef __attribute__((ext_vector_type(4))) float f32x4;

constexpr int S = 2048, D = 576, F = 4608, FH = 2304;

__device__ __forceinline__ float bf2f(u16 v) { return __uint_as_float(((unsigned)v) << 16); }
__device__ __forceinline__ u16 f2bf(float f) { __hip_bfloat16 b = __float2bfloat16(f); return *reinterpret_cast<u16*>(&b); }
__device__ __forceinline__ float rbf(float f) { return bf2f(f2bf(f)); }

__global__ void k_ln(const u16* __restrict__ in, int ldin, u16* __restrict__ out, int ldout, int n) {
    __shared__ float red[8];
    int row = blockIdx.x, t = threadIdx.x;
    const u16* p = in + (size_t)row * ldin;
    float s = 0.f;
    for (int i = t; i < n; i += 256) s += bf2f(p[i]);
    for (int o = 32; o >= 1; o >>= 1) s += __shfl_xor(s, o);
    if ((t & 63) == 0) red[t >> 6] = s;
    __syncthreads();
    float mean = (red[0] + red[1] + red[2] + red[3]) / n;
    float q = 0.f;
    for (int i = t; i < n; i += 256) { float d = bf2f(p[i]) - mean; q += d * d; }
    for (int o = 32; o >= 1; o >>= 1) q += __shfl_xor(q, o);
    if ((t & 63) == 0) red[4 + (t >> 6)] = q;
    __syncthreads();
    float var = (red[4] + red[5] + red[6] + red[7]) / n;
    float mb = rbf(mean), vb = rbf(var);
    float den = rbf(sqrtf(rbf(vb + 1e-5f)));
    for (int i = t; i < n; i += 256) out[(size_t)row * ldout + i] = f2bf(rbf(bf2f(p[i]) - mb) / den);
}

__global__ void k_kv(const u16* __restrict__ k, const u16* __restrict__ v, const u16* __restrict__ sk, u16* __restrict__ kv) {
    int idx = blockIdx.x * 256 + threadIdx.x;
    if (idx >= S * D) return;
    int s = idx / D, d = idx % D;
    float acc = 0.f;
    for (int t = 0; t < 4; ++t) {
        int ss = s + t - 3;
        float kk = ss >= 0 ? bf2f(k[(size_t)ss * D + d]) : 0.f;
        acc = rbf(acc + rbf(kk * bf2f(sk[d * 4 + 3 - t])));
    }
    kv[idx] = f2bf(acc * bf2f(v[idx]));
}

__global__ void k_conv(const u16* __restrict__ kv, const u16* __restrict__ h, const u16* __restrict__ Dv, const u16* __restrict__ xn, u16* __restrict__ sout) {
    int idx = blockIdx.x * 256 + threadIdx.x;
    if (idx >= S * D) return;
    int t = idx / D, d = idx % D;
    float acc = 0.f;
    for (int s = 0; s <= t; ++s) acc += bf2f(kv[(size_t)s * D + d]) * bf2f(h[(size_t)d * S + (t - s)]);
    float y = rbf(acc);
    y = rbf(y + rbf(bf2f(kv[idx]) * bf2f(Dv[d])));
    sout[idx] = f2bf(bf2f(xn[idx]) * y);
}

template <int MODE>
__global__ void k_gemm(const u16* __restrict__ A, int lda, const u16* __restrict__ B, int ldb, int M, int N, int K,
                       u16* __restrict__ C, int ldc, const u16* __restrict__ e0, const u16* __restrict__ e1, int lde) {
    int wave = (blockIdx.x * 256 + threadIdx.x) >> 6, lane = threadIdx.x & 63;
    int nt = N / 16;
    int tm = wave / nt, tn = wave % nt;
    if (tm * 16 >= M) return;
    int fr = lane & 15, fq = lane >> 4;
    f32x4 acc = {0, 0, 0, 0};
    for (int k0 = 0; k0 < K; k0 += 32) {
        bf16x8 a = *reinterpret_cast<const bf16x8*>(A + (size_t)(tm * 16 + fr) * lda + k0 + fq * 8);
        bf16x8 b;
        for (int j = 0; j < 8; ++j) b[j] = (short)B[(size_t)(k0 + fq * 8 + j) * ldb + tn * 16 + fr];
        acc = __builtin_amdgcn_mfma_f32_16x16x32_bf16(a, b, acc, 0, 0, 0);
    }
    for (int j = 0; j < 4; ++j) {
        int r = tm * 16 + fq * 4 + j, c = tn * 16 + fr;
        float v = rbf(acc[j]);
        if (MODE == 0) {
            float u = 0.7978845608028654f * (v + 0.044715f * v * v * v);
            float g = 0.5f * v * (1.f + tanhf(u));
            C[(size_t)r * ldc + c] = f2bf(g);
        } else if (MODE == 1) {
            float g = rbf(v + bf2f(e0[r]));
            C[(size_t)r * ldc + c] = f2bf(bf2f(e1[(size_t)r * lde + c]) * g);
        } else {
            float g = rbf(v / (1.f + expf(-v)));
            float sg = rbf(bf2f(e1[(size_t)r * lde + c]) * g);
            C[(size_t)r * ldc + c] = f2bf(bf2f(e0[(size_t)r * lde + c]) + sg);
        }
    }
}

extern "C" void kernel_launch(void* const* d_in, const int* in_sizes, int n_in,
                              void* d_out, int out_size, void* d_ws, size_t ws_size,
                              hipStream_t stream) {
    const u16* q = (const u16*)d_in[0];
    const u16* k = (const u16*)d_in[1];
    const u16* v = (const u16*)d_in[2];
    const u16* sk = (const u16*)d_in[3];
    const u16* h = (const u16*)d_in[4];
    const u16* Dv = (const u16*)d_in[5];
    const u16* w_in = (const u16*)d_in[7];
    const u16* w_sp = (const u16*)d_in[8];
    const u16* b_sp = (const u16*)d_in[9];
    const u16* w_out = (const u16*)d_in[10];
    u16* out = (u16*)d_out;
    char* ws = (char*)d_ws;
    u16* xn = (u16*)ws; ws += (size_t)S * D * 2;
    u16* kv = (u16*)ws; ws += (size_t)S * D * 2;
    u16* sb = (u16*)ws; ws += (size_t)S * D * 2;
    u16* z = (u16*)ws; ws += (size_t)S * F * 2;
    u16* z2n = (u16*)ws; ws += (size_t)S * FH * 2;
    u16* P = (u16*)ws; ws += (size_t)S * FH * 2;
    k_ln<<<S, 256, 0, stream>>>(q, D, xn, D, D);
    k_kv<<<(S * D + 255) / 256, 256, 0, stream>>>(k, v, sk, kv);
    k_conv<<<(S * D + 255) / 256, 256, 0, stream>>>(kv, h, Dv, xn, sb);
    k_gemm<0><<<(S / 16) * (F / 16) / 4, 256, 0, stream>>>(xn, D, w_in, F, S, F, D, z, F, nullptr, nullptr, 0);
    k_ln<<<S, 256, 0, stream>>>(z + FH, F, z2n, FH, FH);
    k_gemm<1><<<(S / 16) * (FH / 16) / 4, 256, 0, stream>>>(w_sp, S, z2n, FH, S, FH, S, P, FH, b_sp, z, F);
    k_gemm<2><<<(S / 16) * (D / 16) / 4, 256, 0, stream>>>(P, FH, w_out, D, S, D, FH, out, D, q, sb, D);
}
```

```cpp
#include <hip/hip_runtime.h>
#include <stdint.h>

#define LAS __attribute__((address_space(3)))
typedef unsigned short u16;
typedef short bf16x8 __attribute__((ext_vector_type(8)));
typedef short bf16x4 __attribute__((ext_vector_type(4)));
typedef float f32x4 __attribute__((ext_vector_type(4)));
typedef float f32x2 __attribute__((ext_vector_type(2)));
typedef unsigned u32x4 __attribute__((ext_vector_type(4)));
typedef unsigned u32x2 __attribute__((ext_vector_type(2)));
typedef __bf16 bf16x2_t __attribute__((ext_vector_type(2)));

constexpr int S = 2048, D = 576, F = 4608, FH = 2304;

__device__ __forceinline__ float bflo(unsigned w) { return __uint_as_float(w << 16); }
__device__ __forceinline__ float bfhi(unsigned w) { return __uint_as_float(w & 0xffff0000u); }
__device__ __forceinline__ unsigned pk2(float lo, float hi) { f32x2 p = {lo, hi}; return __builtin_bit_cast(unsigned, __builtin_convertvector(p, bf16x2_t)); }
__device__ __forceinline__ float rbf(float x) { return bflo(pk2(x, x)); }
__device__ __forceinline__ f32x4 mfma16(bf16x8 a, bf16x8 b, f32x4 c) { return __builtin_amdgcn_mfma_f32_16x16x32_bf16(a, b, c, 0, 0, 0); }
__device__ __forceinline__ void glds16(const void* g, LAS char* l) { __builtin_amdgcn_global_load_lds((const unsigned*)g, (LAS unsigned*)l, 16, 0, 0); }
__device__ __forceinline__ bf16x4 trread(LAS char* p) { return __builtin_bit_cast(bf16x4, __builtin_amdgcn_ds_read_tr16_b64_v4i16((LAS bf16x4*)p)); }
__device__ __forceinline__ bf16x8 cat8(bf16x4 a, bf16x4 b) { return __builtin_shufflevector(a, b, 0, 1, 2, 3, 4, 5, 6, 7); }
#define WAIT_V(n) asm volatile("s_waitcnt vmcnt(%0)" ::"n"(n) : "memory")
#define WAIT_L0() asm volatile("s_waitcnt lgkmcnt(0)" ::: "memory")
#define BAR() do { asm volatile("" ::: "memory"); __builtin_amdgcn_s_barrier(); asm volatile("" ::: "memory"); } while (0)

__device__ __forceinline__ float gelu_tanh(float v) {
    const float c1 = -2.0f * 0.7978845608028654f * 1.4426950408889634f;
    const float c2 = c1 * 0.044715f;
    float e = __builtin_amdgcn_exp2f(v * __builtin_fmaf(c2, v * v, c1));
    return v * __builtin_amdgcn_rcpf(1.0f + e);
}

constexpr size_t WS_Z1 = 0;
constexpr size_t WS_Z2 = WS_Z1 + (size_t)S * FH * 2;
constexpr size_t WS_P = WS_Z2 + (size_t)S * FH * 2;
constexpr size_t WS_KVT = WS_P + (size_t)S * FH * 2;
constexpr size_t WS_YT = WS_KVT + (size_t)S * D * 2;
constexpr size_t WS_ST2 = WS_YT + (size_t)S * D * 2;
constexpr size_t WS_ST1 = WS_ST2 + (size_t)16 * S * 8;

constexpr int K1_A = 16384, K1_B = 36864, K1_STAGE = K1_A + K1_B, K1_NBUF = 3;
constexpr int K1_LDS = K1_STAGE * K1_NBUF + 4096;

__global__ __launch_bounds__(512, 2) void k1_gemm1(const u16* __restrict__ x, const u16* __restrict__ w_in,
                                                   const u16* __restrict__ kin, const u16* __restrict__ vin, const u16* __restrict__ shk,
                                                   char* __restrict__ ws) {
    extern __shared__ __attribute__((aligned(16))) char smem_[];
    LAS char* lds = (LAS char*)smem_;
    const int b = blockIdx.x, xcd = b & 7, r = b >> 3;
    const int gi = xcd & 1, gj = xcd >> 1;
    const int pi = gi * 8 + (r & 7);
    const int cq = r >> 3;
    const int isz2 = cq >> 1;
    const int ct = 2 * gj + (cq & 1);
    const int col0 = isz2 * FH + ct * 288;
    const int tid = threadIdx.x, wid = __builtin_amdgcn_readfirstlane(tid >> 6), lane = tid & 63;
    const int wm = wid & 3, wn = wid >> 2;
    const int fr = lane & 15, g = lane >> 4;

    const char* src[7];
#pragma unroll
    for (int i = 0; i < 7; ++i) {
        int p = wid + 8 * i;
        if (i < 2) {
            int lam = p * 8 + (lane >> 3);
            int rho = lam & 15, mu = (lam >> 4) & 1;
            int grow = isz2 ? ((lam & ~31) + 8 * (rho >> 2) + 4 * mu + (rho & 3)) : lam;
            int ch = (lane & 7) ^ ((lam >> 1) & 7);
            src[i] = (const char*)x + (size_t)(pi * 128 + grow) * (D * 2) + ch * 16;
        } else {
            int q = p - 16; if (q >= 36) q = 35;
            int c = q * 64 + lane, row = c / 36, cc = c - row * 36;
            cc ^= ((row >> 3) & 1) << 1;
            src[i] = (const char*)w_in + (size_t)row * (F * 2) + col0 * 2 + cc * 16;
        }
    }
    auto stage = [&](int t, int buf) {
        LAS char* base = lds + buf * K1_STAGE + wid * 1024;
#pragma unroll
        for (int i = 0; i < 2; ++i) glds16(src[i] + t * 128, base + i * 8192);
#pragma unroll
        for (int i = 2; i < 6; ++i) glds16(src[i] + (size_t)t * (64 * F * 2), base + i * 8192);
        if (wid < 4) glds16(src[6] + (size_t)t * (64 * F * 2), base + 6 * 8192);
    };
    stage(0, 0);
    stage(1, 1);

    const int aoff = (wm * 32 + fr) * 128 + ((g ^ ((fr >> 1) & 7)) << 4);
    const int q4 = (lane >> 2) & 3, p4 = lane & 3;
    const int brow = K1_A + (8 * g + q4) * 576 + p4 * 8;
    const int sw = (g & 1) * 32;
    const int be = brow + wn * 320 + sw, bo = brow + wn * 320 - sw;
    const int b8 = brow + (wn ? (9 * 32 - sw) : (8 * 32 + sw));

    f32x4 acc[2][9];
#pragma unroll
    for (int m = 0; m < 2; ++m)
#pragma unroll
        for (int n = 0; n < 9; ++n) acc[m][n] = (f32x4){0.f, 0.f, 0.f, 0.f};
    f32x4 csum[3], rs, rq;
#pragma unroll
    for (int n = 0; n < 3; ++n) csum[n] = (f32x4){0.f, 0.f, 0.f, 0.f};
    rs = (f32x4){0.f, 0.f, 0.f, 0.f}; rq = rs;
    const bf16x8 ones = {0x3f80, 0x3f80, 0x3f80, 0x3f80, 0x3f80, 0x3f80, 0x3f80, 0x3f80};

    int buf = 0;
    for (int t = 0; t < 9; ++t) {
        if (t < 8) { if (wid < 4) WAIT_V(7); else WAIT_V(6); } else WAIT_V(0);
        BAR();
        if (t + 2 < 9) { int nb = buf + 2; if (nb >= 3) nb -= 3; stage(t + 2, nb); }
        LAS char* sb = lds + buf * K1_STAGE;
#pragma unroll
        for (int ks = 0; ks < 2; ++ks) {
            bf16x8 a[2], bb[9];
#pragma unroll
            for (int m = 0; m < 2; ++m) a[m] = *(LAS bf16x8*)(sb + ((aoff + m * 2048) ^ (ks * 64)));
#pragma unroll
            for (int l = 0; l < 9; ++l) {
                int o = (l == 8 ? b8 : ((l & 1) ? bo : be) + l * 32) + ks * (32 * 576);
                bb[l] = cat8(trread(sb + o), trread(sb + o + 4 * 576));
            }
            if (isz2) {
#pragma unroll
                for (int l = 0; l < 9; ++l)
#pragma unroll
                    for (int m = 0; m < 2; ++m) acc[m][l] = mfma16(a[m], bb[l], acc[m][l]);
            } else {
#pragma unroll
                for (int l = 0; l < 9; ++l)
#pragma unroll
                    for (int m = 0; m < 2; ++m) acc[m][l] = mfma16(bb[l], a[m], acc[m][l]);
            }
            bf16x8 am = wn ? a[1] : a[0];
            rs = mfma16(ones, am, rs);
            rq = mfma16(am, am, rq);
            bf16x8 c0 = wm == 0 ? bb[0] : wm == 1 ? bb[1] : wm == 2 ? bb[2] : bb[3];
            bf16x8 c1 = wm == 0 ? bb[4] : wm == 1 ? bb[5] : wm == 2 ? bb[6] : bb[7];
            csum[0] = mfma16(ones, c0, csum[0]);
            csum[1] = mfma16(ones, c1, csum[1]);
            if (wm == 0) csum[2] = mfma16(ones, bb[8], csum[2]);
        }
        buf = buf + 1 == 3 ? 0 : buf + 1;
    }
    LAS f32x2* rowst = (LAS f32x2*)(lds + K1_STAGE * K1_NBUF);
    LAS float* cs = (LAS float*)(lds + K1_STAGE * K1_NBUF + 1024);
    {
        if ((fr >> 2) == g) {
            float sum = rs[0];
            float sq = (fr & 3) == 0 ? rq[0] : (fr & 3) == 1 ? rq[1] : (fr & 3) == 2 ? rq[2] : rq[3];
            float mean = sum * (1.0f / D);
            float var = fmaxf(sq * (1.0f / D) - mean * mean, 0.f);
            float mb = rbf(mean), vb = rbf(var);
            float den = rbf(__builtin_sqrtf(rbf(vb + 1e-5f)));
            float rd = 1.0f / den;
            int lam = wm * 32 + wn * 16 + fr;
            rowst[lam] = (f32x2){mb, rd};
            if (!isz2 && ct == 0) ((f32x2*)(ws + WS_ST1))[pi * 128 + lam] = (f32x2){mb, den};
        }
        if (g == 0) {
            cs[(wn * 9 + wm) * 16 + fr] = csum[0][0];
            cs[(wn * 9 + wm + 4) * 16 + fr] = csum[1][0];
            if (wm == 0) cs[(wn * 9 + 8) * 16 + fr] = csum[2][0];
        }
    }
    WAIT_L0();
    BAR();
    const int fp = 2 * ct + wn;
    if (isz2) {
        f32x2 st[2][4];
#pragma unroll
        for (int m = 0; m < 2; ++m)
#pragma unroll
            for (int rr = 0; rr < 4; ++rr) st[m][rr] = rowst[wm * 32 + m * 16 + 4 * g + rr];
        float sum[8], sq[8];
#pragma unroll
        for (int j = 0; j < 8; ++j) { sum[j] = 0.f; sq[j] = 0.f; }
        const int kt = pi * 2 + (wm >> 1), ks = wm & 1;
        char* dst = ws + WS_Z2 + ((size_t)(fp * 32 + kt) * 18 + ks) * 1024 + lane * 16;
#pragma unroll
        for (int l = 0; l < 9; ++l) {
            const float c = cs[(wn * 9 + l) * 16 + fr];
            float z[8];
#pragma unroll
            for (int m = 0; m < 2; ++m)
#pragma unroll
                for (int rr = 0; rr < 4; ++rr) z[m * 4 + rr] = gelu_tanh(rbf((acc[m][l][rr] - st[m][rr].x * c) * st[m][rr].y));
            u32x4 w;
#pragma unroll
            for (int j = 0; j < 4; ++j) {
                w[j] = pk2(z[2 * j], z[2 * j + 1]);
                float lo = bflo(w[j]), hi = bfhi(w[j]);
                sum[2 * j] += lo; sq[2 * j] = __builtin_fmaf(lo, lo, sq[2 * j]);
                sum[2 * j + 1] += hi; sq[2 * j + 1] = __builtin_fmaf(hi, hi, sq[2 * j + 1]);
            }
            const int n = l == 8 ? (wn ? 0 : 8) : l + wn;
            *(u32x4*)(dst + n * 2048) = w;
        }
#pragma unroll
        for (int j = 0; j < 8; ++j) {
#pragma unroll
            for (int o = 1; o < 16; o <<= 1) { sum[j] += __shfl_xor(sum[j], o); sq[j] += __shfl_xor(sq[j], o); }
        }
        if (fr < 8) {
            float a = sum[0], c = sq[0];
#pragma unroll
            for (int j = 1; j < 8; ++j) { a = fr == j ? sum[j] : a; c = fr == j ? sq[j] : c; }
            ((f32x2*)(ws + WS_ST2))[fp * S + pi * 128 + wm * 32 + 8 * g + fr] = (f32x2){a, c};
        }
    } else {
        f32x2 st[2];
#pragma unroll
        for (int m = 0; m < 2; ++m) st[m] = rowst[wm * 32 + m * 16 + fr];
        const int kap0 = (9 * fp + wn) >> 1;
        const int kap8 = wn ? (9 * fp) >> 1 : (9 * fp + 8) >> 1;
#pragma unroll
        for (int m = 0; m < 2; ++m) {
            const int tf = pi * 8 + wm * 2 + m;
            char* dst = ws + WS_Z1 + (size_t)tf * 72 * 1024 + lane * 16;
#pragma unroll
            for (int lp = 0; lp < 4; ++lp) {
                u32x4 w;
#pragma unroll
                for (int h = 0; h < 2; ++h) {
                    const int l = 2 * lp + h;
                    const f32x4 c = *(LAS f32x4*)(cs + (wn * 9 + l) * 16 + 4 * g);
                    float z[4];
#pragma unroll
                    for (int rr = 0; rr < 4; ++rr) z[rr] = gelu_tanh(rbf((acc[m][l][rr] - st[m].x * c[rr]) * st[m].y));
                    w[2 * h] = pk2(z[0], z[1]); w[2 * h + 1] = pk2(z[2], z[3]);
                }
                *(u32x4*)(dst + (kap0 + lp) * 1024) = w;
            }
            {
                const f32x4 c = *(LAS f32x4*)(cs + (wn * 9 + 8) * 16 + 4 * g);
                float z[4];
#pragma unroll
                for (int rr = 0; rr < 4; ++rr) z[rr] = gelu_tanh(rbf((acc[m][8][rr] - st[m].x * c[rr]) * st[m].y));
                u32x2 w = {pk2(z[0], z[1]), pk2(z[2], z[3])};
                *(u32x2*)(dst + kap8 * 1024 + wn * 8) = w;
            }
        }
    }
    if (tid < 288) {
        const int st_ = b >> 3, dt = b & 7;
        const int sg = tid / 36, dc = tid - sg * 36;
        const int d0 = dt * 72 + dc * 2, s0 = st_ * 64 + sg * 8;
        unsigned kw[11], vw[8];
#pragma unroll
        for (int i = 0; i < 11; ++i) { int s = s0 - 3 + i; kw[i] = s >= 0 ? *(const unsigned*)(kin + (size_t)s * D + d0) : 0u; }
#pragma unroll
        for (int i = 0; i < 8; ++i) vw[i] = *(const unsigned*)(vin + (size_t)(s0 + i) * D + d0);
        const u32x4 sk = *(const u32x4*)(shk + d0 * 4);
        float o0[8], o1[8];
#pragma unroll
        for (int i = 0; i < 8; ++i) {
            float a0 = rbf(bflo(kw[i]) * bfhi(sk[1])), a1 = rbf(bfhi(kw[i]) * bfhi(sk[3]));
            a0 = rbf(a0 + rbf(bflo(kw[i + 1]) * bflo(sk[1]))); a1 = rbf(a1 + rbf(bfhi(kw[i + 1]) * bflo(sk[3])));
            a0 = rbf(a0 + rbf(bflo(kw[i + 2]) * bfhi(sk[0]))); a1 = rbf(a1 + rbf(bfhi(kw[i + 2]) * bfhi(sk[2])));
            a0 = rbf(a0 + rbf(bflo(kw[i + 3]) * bflo(sk[0]))); a1 = rbf(a1 + rbf(bfhi(kw[i + 3]) * bflo(sk[2])));
            o0[i] = a0 * bflo(vw[i]); o1[i] = a1 * bfhi(vw[i]);
        }
        u32x4 w0 = {pk2(o0[7], o0[6]), pk2(o0[5], o0[4]), pk2(o0[3], o0[2]), pk2(o0[1], o0[0])};
        u32x4 w1 = {pk2(o1[7], o1[6]), pk2(o1[5], o1[4]), pk2(o1[3], o1[2]), pk2(o1[1], o1[0])};
        *(u32x4*)(ws + WS_KVT + ((size_t)d0 * S + s0) * 2) = w0;
        *(u32x4*)(ws + WS_KVT + ((size_t)(d0 + 1) * S + s0) * 2) = w1;
    }
}

constexpr int CV_U = 512 + 4096;
constexpr int CV_HS = 4096 + 64 + 64;
constexpr int CV_HSTRIDE = 4160;
constexpr int CV_WAVE = CV_U + 4 * CV_HSTRIDE;
constexpr int K15_LDS = 4 * CV_WAVE;

__global__ __launch_bounds__(256) void k15_norm_conv(const u16* __restrict__ hk, const u16* __restrict__ dv, char* __restrict__ ws) {
    extern __shared__ __attribute__((aligned(16))) char smem_[];
    LAS char* lds = (LAS char*)smem_;
    const int b = blockIdx.x, tid = threadIdx.x, wid = __builtin_amdgcn_readfirstlane(tid >> 6), lane = tid & 63;
    const int fr = lane & 15, g = lane >> 4;
    if (b < 256) {
        const int xcd = b & 7, r = b >> 3, gi = xcd & 1, gj = xcd >> 1;
        const int kt = gi * 16 + (r & 15), ct = 2 * gj + (r >> 4);
        char* base = ws + WS_Z2 + lane * 16;
        u32x4 v[9];
#pragma unroll
        for (int i = 0; i < 9; ++i) {
            int c = wid * 9 + i;
            int fp = 2 * ct + (c >= 18), cc = c >= 18 ? c - 18 : c;
            v[i] = *(const u32x4*)(base + ((size_t)(fp * 32 + kt) * 18 + cc) * 1024);
        }
        LAS f32x2* part = (LAS f32x2*)lds;
        LAS f32x2* fin = (LAS f32x2*)(lds + 2048);
        {
            const f32x2* st = (const f32x2*)(ws + WS_ST2) + kt * 64 + lane;
            f32x2 a = st[(wid * 4 + 0) * S], c = st[(wid * 4 + 1) * S], d = st[(wid * 4 + 2) * S], e = st[(wid * 4 + 3) * S];
            part[wid * 64 + lane] = (a + c) + (d + e);
        }
        __syncthreads();
        if (wid == 0) {
            f32x2 t = (part[lane] + part[64 + lane]) + (part[128 + lane] + part[192 + lane]);
            float mean = t.x * (1.0f / FH);
            float var = fmaxf(t.y * (1.0f / FH) - mean * mean, 0.f);
            float mb = rbf(mean), vb = rbf(var);
            float den = rbf(__builtin_sqrtf(rbf(vb + 1e-5f)));
            fin[lane] = (f32x2){mb, 1.0f / den};
        }
        __syncthreads();
        f32x2 st[2][8];
#pragma unroll
        for (int ks = 0; ks < 2; ++ks)
#pragma unroll
            for (int j = 0; j < 8; ++j) st[ks][j] = fin[ks * 32 + 8 * g + j];
#pragma unroll
        for (int i = 0; i < 9; ++i) {
            int c = wid * 9 + i;
            int fp = 2 * ct + (c >= 18), cc = c >= 18 ? c - 18 : c;
            const bool k1 = cc & 1;
            u32x4 w;
#pragma unroll
            for (int j = 0; j < 4; ++j) {
                f32x2 s0 = k1 ? st[1][2 * j] : st[0][2 * j], s1 = k1 ? st[1][2 * j + 1] : st[0][2 * j + 1];
                w[j] = pk2(rbf(bflo(v[i][j]) - s0.x) * s0.y, rbf(bfhi(v[i][j]) - s1.x) * s1.y);
            }
            *(u32x4*)(base + ((size_t)(fp * 32 + kt) * 18 + cc) * 1024) = w;
        }
        return;
    }
    const int d = (b - 256) * 4 + wid;
    LAS char* U = lds + wid * CV_WAVE;
    LAS char* H = U + CV_U;
    {
        const char* usrc = ws + WS_KVT + (size_t)d * S * 2 + lane * 16;
        const char* hsrc = (const char*)hk + (size_t)d * S * 2 + lane * 16;
        u32x4 uu[4], hh[4]; u32x2 hn[4];
#pragma unroll
        for (int i = 0; i < 4; ++i) { uu[i] = *(const u32x4*)(usrc + i * 1024); hh[i] = *(const u32x4*)(hsrc + i * 1024); }
#pragma unroll
        for (int i = 0; i < 4; ++i) { bool last = (i == 3 && lane == 63); hn[i] = last ? (u32x2){0u, 0u} : *(const u32x2*)(hsrc + i * 1024 + 16); }
        if (lane < 32) *(LAS u32x4*)(U + lane * 16) = (u32x4){0u, 0u, 0u, 0u};
        if (lane < 8) {
            int c = lane >> 1, half = lane & 1;
            u32x4 z = {0u, 0u, 0u, 0u};
            if (half) {
                unsigned h0 = *(const unsigned*)((const char*)hk + (size_t)d * S * 2), h1 = *(const unsigned*)((const char*)hk + (size_t)d * S * 2 + 4);
                if (c == 1) z[3] = h0 << 16;
                if (c == 2) z[3] = h0;
                if (c == 3) { z[2] = h0 << 16; z[3] = __builtin_amdgcn_alignbit(h1, h0, 16); }
            }
            *(LAS u32x4*)(H + c * CV_HSTRIDE + half * 16) = z;
        }
#pragma unroll
        for (int i = 0; i < 4; ++i) {
            *(LAS u32x4*)(U + 512 + i * 1024 + lane * 16) = uu[i];
            unsigned w0 = hh[i][0], w1 = hh[i][1], w2 = hh[i][2], w3 = hh[i][3], w4 = hn[i][0], w5 = hn[i][1];
            unsigned a01 = __builtin_amdgcn_alignbit(w1, w0, 16), a12 = __builtin_amdgcn_alignbit(w2, w1, 16), a23 = __builtin_amdgcn_alignbit(w3, w2, 16),
                     a34 = __builtin_amdgcn_alignbit(w4, w3, 16), a45 = __builtin_amdgcn_alignbit(w5, w4, 16);
            LAS char* p = H + 32 + i * 1024 + lane * 16;
            *(LAS u32x4*)(p) = hh[i];
            *(LAS u32x4*)(p + CV_HSTRIDE) = (u32x4){a01, a12, a23, a34};
            *(LAS u32x4*)(p + 2 * CV_HSTRIDE) = (u32x4){w1, w2, w3, w4};
            *(LAS u32x4*)(p + 3 * CV_HSTRIDE) = (u32x4){a12, a23, a34, a45};
        }
    }
    WAIT_L0();
    __builtin_amdgcn_sched_barrier(0);
    const int e0 = 16 * (g >> 1) - 8 * (g & 1) - 7 + fr + 16;
    const int ac = e0 & 3;
    LAS char* ap = H + ac * CV_HSTRIDE + (e0 - ac) * 2;
    LAS char* bp = U + 512 + 32 * (fr - (g >> 1)) + 16 * (g & 1);
    f32x4 acc[8];
#pragma unroll
    for (int n = 0; n < 8; ++n) acc[n] = (f32x4){0.f, 0.f, 0.f, 0.f};
#pragma unroll 2
    for (int bb = 0; bb < 8; ++bb) {
        bf16x8 A[8], B[8];
#pragma unroll
        for (int a = 0; a < 8; ++a) {
            LAS char* p = ap + (8 * a + bb) * 64;
            A[a] = cat8(*(LAS bf16x4*)p, *(LAS bf16x4*)(p + 8));
            B[a] = *(LAS bf16x8*)(bp + a * 512 - bb * 64);
        }
#pragma unroll
        for (int a = 0; a < 8; ++a)
#pragma unroll
            for (int c = 0; c + a < 8; ++c) acc[a + c] = mfma16(A[a], B[c], acc[a + c]);
    }
    const float dd = bflo((unsigned)dv[d]);
    char* dst = ws + WS_YT + (size_t)d * S * 2 + (16 * fr + 4 * g) * 2;
#pragma unroll
    for (int nu = 0; nu < 8; ++nu) {
        u32x2 uw = *(LAS u32x2*)(U + 512 + (256 * nu + 16 * fr + 8 * (g >> 1)) * 2 + ((g & 1) ? 0 : 8));
        float u0 = bfhi(uw[1]), u1 = bflo(uw[1]), u2 = bfhi(uw[0]), u3 = bflo(uw[0]);
        float y0 = rbf(acc[nu][0]) + rbf(u0 * dd), y1 = rbf(acc[nu][1]) + rbf(u1 * dd), y2 = rbf(acc[nu][2]) + rbf(u2 * dd), y3 = rbf(acc[nu][3]) + rbf(u3 * dd);
        *(u32x2*)(dst + nu * 512) = (u32x2){pk2(y0, y1), pk2(y2, y3)};
    }
}

constexpr int K2_A = 16384, K2_B = 18432, K2_STAGE = K2_A + K2_B, K2_NBUF = 4, K2_LDS = K2_STAGE * K2_NBUF;

__global__ __launch_bounds__(512, 2) void k2_gemm2(const u16* __restrict__ w_sp, const u16* __restrict__ b_sp, char* __restrict__ ws) {
    extern __shared__ __attribute__((aligned(16))) char smem_[];
    LAS char* lds = (LAS char*)smem_;
    const int b = blockIdx.x, xcd = b & 7, r = b >> 3;
    const int gi = xcd & 1, gj = xcd >> 1;
    const int pi = gi * 8 + (r & 7), pj = gj * 4 + (r >> 3);
    const int tid = threadIdx.x, wid = __builtin_amdgcn_readfirstlane(tid >> 6), lane = tid & 63;
    const int wm = wid & 3, big = wid >> 2;
    const int fr = lane & 15, g = lane >> 4;
    const char* srcA[2];
#pragma unroll
    for (int i = 0; i < 2; ++i) {
        int lam = (wid + 8 * i) * 8 + (lane >> 3);
        int ch = (lane & 7) ^ ((lam >> 1) & 7);
        srcA[i] = (const char*)w_sp + (size_t)(pi * 128 + lam) * (S * 2) + ch * 16;
    }
    const char* srcB = ws + WS_Z2 + (size_t)pj * 32 * K2_B + wid * 1024 + lane * 16;
    auto stage = [&](int t, int buf) {
        LAS char* base = lds + buf * K2_STAGE + wid * 1024;
        glds16(srcA[0] + t * 128, base);
        glds16(srcA[1] + t * 128, base + 8192);
        glds16(srcB + t * K2_B, base + K2_A);
        glds16(srcB + t * K2_B + 8192, base + K2_A + 8192);
        if (wid < 2) glds16(srcB + t * K2_B + 16384, base + K2_A + 16384);
    };
    stage(0, 0); stage(1, 1); stage(2, 2);
    const int odd = pj & 1;
    const int nb = big ? (odd ? 1 : 4) : (odd ? 5 : 0);
    const int n4 = odd ? 0 : 8;
    const int aoff = (wm * 32 + fr) * 128 + ((g ^ ((fr >> 1) & 7)) << 4);
    const int boff = K2_A + nb * 2048 + lane * 16, boff4 = K2_A + n4 * 2048 + lane * 16;
    f32x4 acc[2][5];
#pragma unroll
    for (int m = 0; m < 2; ++m)
#pragma unroll
        for (int n = 0; n < 5; ++n) acc[m][n] = (f32x4){0.f, 0.f, 0.f, 0.f};
    for (int t = 0; t < 32; ++t) {
        if (t < 30) { if (wid < 2) WAIT_V(10); else WAIT_V(8); }
        else if (t == 30) { if (wid < 2) WAIT_V(5); else WAIT_V(4); }
        else WAIT_V(0);
        BAR();
        if (t + 3 < 32) stage(t + 3, (t + 3) & 3);
        LAS char* sb = lds + (t & 3) * K2_STAGE;
#pragma unroll
        for (int ks = 0; ks < 2; ++ks) {
            bf16x8 a[2], bb[5];
#pragma unroll
            for (int m = 0; m < 2; ++m) a[m] = *(LAS bf16x8*)(sb + ((aoff + m * 2048) ^ (ks * 64)));
#pragma unroll
            for (int l = 0; l < 4; ++l) bb[l] = *(LAS bf16x8*)(sb + boff + l * 2048 + ks * 1024);
            if (big) bb[4] = *(LAS bf16x8*)(sb + boff4 + ks * 1024);
#pragma unroll
            for (int l = 0; l < 4; ++l)
#pragma unroll
                for (int m = 0; m < 2; ++m) acc[m][l] = mfma16(bb[l], a[m], acc[m][l]);
            if (big) {
#pragma unroll
                for (int m = 0; m < 2; ++m) acc[m][4] = mfma16(bb[4], a[m], acc[m][4]);
            }
        }
    }
    const int kap0 = (9 * pj + nb) >> 1, kap4 = (9 * pj + n4) >> 1;
#pragma unroll
    for (int m = 0; m < 2; ++m) {
        const int tf = pi * 8 + wm * 2 + m;
        const float bias = bflo((unsigned)b_sp[tf * 16 + fr]);
        const size_t off = (size_t)tf * 72 * 1024 + lane * 16;
#pragma unroll
        for (int lp = 0; lp < 2; ++lp) {
            const u32x4 z = *(const u32x4*)(ws + WS_Z1 + off + (kap0 + lp) * 1024);
            u32x4 w;
#pragma unroll
            for (int h = 0; h < 2; ++h) {
                const f32x4 a = acc[m][2 * lp + h];
                float g0 = rbf(rbf(a[0]) + bias), g1 = rbf(rbf(a[1]) + bias), g2 = rbf(rbf(a[2]) + bias), g3 = rbf(rbf(a[3]) + bias);
                w[2 * h] = pk2(bflo(z[2 * h]) * g0, bfhi(z[2 * h]) * g1);
                w[2 * h + 1] = pk2(bflo(z[2 * h + 1]) * g2, bfhi(z[2 * h + 1]) * g3);
            }
            *(u32x4*)(ws + WS_P + off + (kap0 + lp) * 1024) = w;
        }
        if (big) {
            const u32x2 z = *(const u32x2*)(ws + WS_Z1 + off + kap4 * 1024 + odd * 8);
            const f32x4 a = acc[m][4];
            float g0 = rbf(rbf(a[0]) + bias), g1 = rbf(rbf(a[1]) + bias), g2 = rbf(rbf(a[2]) + bias), g3 = rbf(rbf(a[3]) + bias);
            *(u32x2*)(ws + WS_P + off + kap4 * 1024 + odd * 8) = (u32x2){pk2(bflo(z[0]) * g0, bfhi(z[0]) * g1), pk2(bflo(z[1]) * g2, bfhi(z[1]) * g3)};
        }
    }
}

constexpr int K3_SLOT = 9 * 1024, K3_WAVE = 2 * K3_SLOT, K3_LDS = 8 * K3_WAVE;
constexpr int K3_NBLK = 26 * 9;

__global__ __launch_bounds__(512, 2) void k3_gemm3(const u16* __restrict__ w_out, const u16* __restrict__ x, char* __restrict__ ws, u16* __restrict__ out) {
    extern __shared__ __attribute__((aligned(16))) char smem_[];
    LAS char* lds = (LAS char*)smem_;
    int L; { const int q = K3_NBLK / 8, rr = K3_NBLK % 8, xc = blockIdx.x & 7, off = blockIdx.x >> 3; L = (xc < rr ? xc * (q + 1) : rr * (q + 1) + (xc - rr) * q) + off; }
    const int rt = L / 9, ctile = L - rt * 9;
    const int tid = threadIdx.x, wid = __builtin_amdgcn_readfirstlane(tid >> 6), lane = tid & 63;
    const int fr = lane & 15, g = lane >> 4;
    LAS char* my = lds + wid * K3_WAVE;
    const int kap0 = wid * 9;
    const char* srcA = ws + WS_P + (size_t)kap0 * 1024 + lane * 16;
    int tfo[5];
#pragma unroll
    for (int m = 0; m < 5; ++m) { int tf = rt * 5 + m; if (tf > 127) tf = 127; tfo[m] = tf * 72 * 1024; }
    const int rowl = lane >> 3;
    const char* srcB = (const char*)w_out + (size_t)(kap0 * 32 + rowl) * (D * 2) + ctile * 128 + (((lane & 7) ^ (((rowl >> 1) & 3) << 1)) << 4);
    auto stage = [&](int i) {
        LAS char* base = my + (i & 1) * K3_SLOT;
#pragma unroll
        for (int m = 0; m < 5; ++m) glds16(srcA + tfo[m] + i * 1024, base + m * 1024);
#pragma unroll
        for (int p = 0; p < 4; ++p) glds16(srcB + (size_t)(i * 32 + p * 8) * (D * 2), base + 5120 + p * 1024);
    };
    stage(0); stage(1);
    f32x4 acc[5][4];
#pragma unroll
    for (int m = 0; m < 5; ++m)
#pragma unroll
        for (int n = 0; n < 4; ++n) acc[m][n] = (f32x4){0.f, 0.f, 0.f, 0.f};
    const int q4 = (lane >> 2) & 3, p4 = lane & 3;
    const int sig = (2 * g + (q4 >> 1)) & 3;
    int bo[4];
#pragma unroll
    for (int n = 0; n < 4; ++n) bo[n] = 5120 + (4 * g + q4) * 128 + ((n ^ sig) << 5) + p4 * 8;
    for (int i = 0; i < 9; ++i) {
        if (i < 8) WAIT_V(9); else WAIT_V(0);
        LAS char* sb = my + (i & 1) * K3_SLOT;
        bf16x8 a[5], bb[4];
#pragma unroll
        for (int m = 0; m < 5; ++m) a[m] = *(LAS bf16x8*)(sb + m * 1024 + lane * 16);
#pragma unroll
        for (int n = 0; n < 4; ++n) bb[n] = cat8(trread(sb + bo[n]), trread(sb + bo[n] + 2048));
        WAIT_L0();
        __builtin_amdgcn_sched_barrier(0);
        if (i + 2 < 9) stage(i + 2);
#pragma unroll
        for (int m = 0; m < 5; ++m)
#pragma unroll
            for (int n = 0; n < 4; ++n) acc[m][n] = mfma16(bb[n], a[m], acc[m][n]);
    }
    BAR();
    if (wid >= 4) {
#pragma unroll
        for (int m = 0; m < 5; ++m)
#pragma unroll
            for (int n = 0; n < 4; ++n) *(LAS f32x4*)(lds + (wid - 4) * 20480 + (m * 4 + n) * 1024 + lane * 16) = acc[m][n];
    }
    WAIT_L0();
    BAR();
    constexpr int RS = 272, R2 = 81920 - 20480 * 4 + 0;
    if (wid < 4) {
#pragma unroll
        for (int m = 0; m < 5; ++m)
#pragma unroll
            for (int n = 0; n < 4; ++n) acc[m][n] += *(LAS f32x4*)(lds + wid * 20480 + (m * 4 + n) * 1024 + lane * 16);
    }
    WAIT_L0();
    BAR();
    if (wid < 4) {
#pragma unroll
        for (int m = 0; m < 5; ++m)
#pragma unroll
            for (int n = 0; n < 4; ++n) *(LAS f32x4*)(lds + R2 + wid * (80 * RS) + (m * 16 + fr) * RS + (n * 16 + 4 * g) * 4) = acc[m][n];
    } else {
        for (int c = tid - 256; c < 640; c += 256) {
            int dd = c / 10, tc = c - dd * 10;
            int t0 = rt * 80 + tc * 8; if (t0 > S - 8) t0 = S - 8;
            u32x4 v = *(const u32x4*)(ws + WS_YT + ((size_t)(ctile * 64 + dd) * S + t0) * 2);
            *(LAS u32x4*)(lds + 90112 + dd * 176 + tc * 16) = v;
        }
    }
    WAIT_L0();
    BAR();
    for (int c = tid; c < 640; c += 512) {
        const int tl = c >> 3, dc = c & 7, t = rt * 80 + tl;
        if (t < S) {
            f32x4 s0 = {0.f, 0.f, 0.f, 0.f}, s1 = s0;
#pragma unroll
            for (int w = 0; w < 4; ++w) {
                s0 += *(LAS f32x4*)(lds + R2 + w * (80 * RS) + tl * RS + dc * 32);
                s1 += *(LAS f32x4*)(lds + R2 + w * (80 * RS) + tl * RS + dc * 32 + 16);
            }
            const u32x4 xv = *(const u32x4*)(x + (size_t)t * D + ctile * 64 + dc * 8);
            const f32x2 st = ((const f32x2*)(ws + WS_ST1))[t];
            float res[8];
#pragma unroll
            for (int j = 0; j < 8; ++j) {
                float o = rbf(j < 4 ? s0[j] : s1[j - 4]);
                float sg = rbf(o * __builtin_amdgcn_rcpf(1.0f + __builtin_amdgcn_exp2f(-1.4426950408889634f * o)));
                float xx = (j & 1) ? bfhi(xv[j >> 1]) : bflo(xv[j >> 1]);
                float xn = rbf(rbf(xx - st.x) / st.y);
                float y = bflo((unsigned)*(LAS u16*)(lds + 90112 + (dc * 8 + j) * 176 + tl * 2));
                float s = rbf(xn * y);
                res[j] = xx + rbf(s * sg);
            }
            *(u32x4*)(out + (size_t)t * D + ctile * 64 + dc * 8) = (u32x4){pk2(res[0], res[1]), pk2(res[2], res[3]), pk2(res[4], res[5]), pk2(res[6], res[7])};
        }
    }
}

extern "C" void kernel_launch(void* const* d_in, const int* in_sizes, int n_in,
                              void* d_out, int out_size, void* d_ws, size_t ws_size,
                              hipStream_t stream) {
    const u16* q = (const u16*)d_in[0];
    const u16* k = (const u16*)d_in[1];
    const u16* v = (const u16*)d_in[2];
    const u16* sk = (const u16*)d_in[3];
    const u16* h = (const u16*)d_in[4];
    const u16* Dv = (const u16*)d_in[5];
    const u16* w_in = (const u16*)d_in[7];
    const u16* w_sp = (const u16*)d_in[8];
    const u16* b_sp = (const u16*)d_in[9];
    const u16* w_out = (const u16*)d_in[10];
    char* ws = (char*)d_ws;
    hipFuncSetAttribute((const void*)k1_gemm1, hipFuncAttributeMaxDynamicSharedMemorySize, K1_LDS);
    hipFuncSetAttribute((const void*)k15_norm_conv, hipFuncAttributeMaxDynamicSharedMemorySize, K15_LDS);
    hipFuncSetAttribute((const void*)k2_gemm2, hipFuncAttributeMaxDynamicSharedMemorySize, K2_LDS);
    hipFuncSetAttribute((const void*)k3_gemm3, hipFuncAttributeMaxDynamicSharedMemorySize, K3_LDS);
    k1_gemm1<<<256, 512, K1_LDS, stream>>>(q, w_in, k, v, sk, ws);
    k15_norm_conv<<<256 + 144, 256, K15_LDS, stream>>>(h, Dv, ws);
    k2_gemm2<<<256, 512, K2_LDS, stream>>>(w_sp, b_sp, ws);
    k3_gemm3<<<K3_NBLK, 512, K3_LDS, stream>>>(w_out, q, ws, (u16*)d_out);
}
```

```cpp
#include <hip/hip_runtime.h>
#include <stdint.h>

#define LAS __attribute__((address_space(3)))
typedef unsigned short u16;
typedef short bf16x8 __attribute__((ext_vector_type(8)));
typedef short bf16x4 __attribute__((ext_vector_type(4)));
typedef float f32x4 __attribute__((ext_vector_type(4)));
typedef float f32x2 __attribute__((ext_vector_type(2)));
typedef unsigned u32x4 __attribute__((ext_vector_type(4)));
typedef unsigned u32x2 __attribute__((ext_vector_type(2)));
typedef __bf16 bf16x2_t __attribute__((ext_vector_type(2)));

constexpr int S = 2048, D = 576, F = 4608, FH = 2304;

__device__ __forceinline__ float bflo(unsigned w) { return __uint_as_float(w << 16); }
__device__ __forceinline__ float bfhi(unsigned w) { return __uint_as_float(w & 0xffff0000u); }
__device__ __forceinline__ unsigned pk2(float lo, float hi) { f32x2 p = {lo, hi}; return __builtin_bit_cast(unsigned, __builtin_convertvector(p, bf16x2_t)); }
__device__ __forceinline__ float rbf(float x) { return bflo(pk2(x, x)); }
__device__ __forceinline__ f32x4 mfma16(bf16x8 a, bf16x8 b, f32x4 c) { return __builtin_amdgcn_mfma_f32_16x16x32_bf16(a, b, c, 0, 0, 0); }
__device__ __forceinline__ void glds16(const void* g, LAS char* l) { __builtin_amdgcn_global_load_lds((const unsigned*)g, (LAS unsigned*)l, 16, 0, 0); }
__device__ __forceinline__ bf16x4 trread(LAS char* p) { return __builtin_bit_cast(bf16x4, __builtin_amdgcn_ds_read_tr16_b64_v4i16((LAS bf16x4*)p)); }
__device__ __forceinline__ bf16x8 cat8(bf16x4 a, bf16x4 b) { return __builtin_shufflevector(a, b, 0, 1, 2, 3, 4, 5, 6, 7); }
#define WAIT_V(n) asm volatile("s_waitcnt vmcnt(%0)" ::"n"(n) : "memory")
#define WAIT_L0() asm volatile("s_waitcnt lgkmcnt(0)" ::: "memory")
#define BAR() do { asm volatile("" ::: "memory"); __builtin_amdgcn_s_barrier(); asm volatile("" ::: "memory"); } while (0)

#define STAMP(kid, i) do { } while (0)
#define STAMPC(kid, i) do { } while (0)
__device__ __forceinline__ float gelu_tanh(float v) {
    const float c1 = -2.0f * 0.7978845608028654f * 1.4426950408889634f;
    const float c2 = c1 * 0.044715f;
    float e = __builtin_amdgcn_exp2f(v * __builtin_fmaf(c2, v * v, c1));
    return v * __builtin_amdgcn_rcpf(1.0f + e);
}

constexpr size_t WS_Z1 = 0;
constexpr size_t WS_Z2 = WS_Z1 + (size_t)S * FH * 2;
constexpr size_t WS_P = WS_Z2 + (size_t)S * FH * 2;
constexpr size_t WS_KVT = WS_P + (size_t)S * FH * 2;
constexpr size_t WS_YT = WS_KVT + (size_t)S * D * 2;
constexpr size_t WS_ST2 = WS_YT + (size_t)S * D * 2;
constexpr size_t WS_ST1 = WS_ST2 + (size_t)16 * S * 8;

constexpr int K1_A = 16384, K1_B = 36864, K1_STAGE = K1_A + K1_B, K1_NBUF = 3;
constexpr int K1_LDS = K1_STAGE * K1_NBUF + 4096;

__global__ __launch_bounds__(512, 2) void k1_gemm1(const u16* __restrict__ x, const u16* __restrict__ w_in,
                                                   const u16* __restrict__ kin, const u16* __restrict__ vin, const u16* __restrict__ shk,
                                                   char* __restrict__ ws) {
    extern __shared__ __attribute__((aligned(16))) char smem_[];
    LAS char* lds = (LAS char*)smem_;
    const int b = blockIdx.x, xcd = b & 7, r = b >> 3;
    const int gi = xcd & 1, gj = xcd >> 1;
    const int pi = gi * 8 + (r & 7);
    const int cq = r >> 3;
    const int isz2 = cq >> 1;
    const int ct = 2 * gj + (cq & 1);
    const int col0 = isz2 * FH + ct * 288;
    const int tid = threadIdx.x, wid = __builtin_amdgcn_readfirstlane(tid >> 6), lane = tid & 63;
    const int wm = wid & 3, wn = wid >> 2;
    const int fr = lane & 15, g = lane >> 4;
    if (wid == 0) STAMP(0, 0);

    const char* src[7];
#pragma unroll
    for (int i = 0; i < 7; ++i) {
        int p = wid + 8 * i;
        if (i < 2) {
            int lam = p * 8 + (lane >> 3);
            int rho = lam & 15, mu = (lam >> 4) & 1;
            int grow = isz2 ? ((lam & ~31) + 8 * (rho >> 2) + 4 * mu + (rho & 3)) : lam;
            int ch = (lane & 7) ^ ((lam >> 1) & 7);
            src[i] = (const char*)x + (size_t)(pi * 128 + grow) * (D * 2) + ch * 16;
        } else {
            int q = p - 16; if (q >= 36) q = 35;
            int c = q * 64 + lane, row = c / 36, cc = c - row * 36;
            cc ^= ((row >> 3) & 1) << 1;
            src[i] = (const char*)w_in + (size_t)row * (F * 2) + col0 * 2 + cc * 16;
        }
    }
    auto stage = [&](int t, int buf) {
        LAS char* base = lds + buf * K1_STAGE + wid * 1024;
#pragma unroll
        for (int i = 0; i < 2; ++i) glds16(src[i] + t * 128, base + i * 8192);
#pragma unroll
        for (int i = 2; i < 6; ++i) glds16(src[i] + (size_t)t * (64 * F * 2), base + i * 8192);
        if (wid < 4) glds16(src[6] + (size_t)t * (64 * F * 2), base + 6 * 8192);
    };
    stage(0, 0);
    stage(1, 1);

    const int aoff = (wm * 32 + fr) * 128 + ((g ^ ((fr >> 1) & 7)) << 4);
    const int q4 = (lane >> 2) & 3, p4 = lane & 3;
    const int brow = K1_A + (8 * g + q4) * 576 + p4 * 8;
    const int sw = (g & 1) * 32;
    const int be = brow + wn * 320 + sw, bo = brow + wn * 320 - sw;
    const int b8 = brow + (wn ? (9 * 32 - sw) : (8 * 32 + sw));

    f32x4 acc[2][9];
#pragma unroll
    for (int m = 0; m < 2; ++m)
#pragma unroll
        for (int n = 0; n < 9; ++n) acc[m][n] = (f32x4){0.f, 0.f, 0.f, 0.f};
    f32x4 csum[3], rs, rq;
#pragma unroll
    for (int n = 0; n < 3; ++n) csum[n] = (f32x4){0.f, 0.f, 0.f, 0.f};
    rs = (f32x4){0.f, 0.f, 0.f, 0.f}; rq = rs;
    const bf16x8 ones = {0x3f80, 0x3f80, 0x3f80, 0x3f80, 0x3f80, 0x3f80, 0x3f80, 0x3f80};

    int buf = 0;
    for (int t = 0; t < 9; ++t) {
        if (t < 8) { if (wid < 4) WAIT_V(7); else WAIT_V(6); } else WAIT_V(0);
        BAR();
        if (t + 2 < 9) { int nb = buf + 2; if (nb >= 3) nb -= 3; stage(t + 2, nb); }
        LAS char* sb = lds + buf * K1_STAGE;
#pragma unroll
        for (int ks = 0; ks < 2; ++ks) {
            bf16x8 a[2], bb[9];
#pragma unroll
            for (int m = 0; m < 2; ++m) a[m] = *(LAS bf16x8*)(sb + ((aoff + m * 2048) ^ (ks * 64)));
#pragma unroll
            for (int l = 0; l < 9; ++l) {
                int o = (l == 8 ? b8 : ((l & 1) ? bo : be) + l * 32) + ks * (32 * 576);
                bb[l] = cat8(trread(sb + o), trread(sb + o + 4 * 576));
            }
            if (isz2) {
#pragma unroll
                for (int l = 0; l < 9; ++l)
#pragma unroll
                    for (int m = 0; m < 2; ++m) acc[m][l] = mfma16(a[m], bb[l], acc[m][l]);
            } else {
#pragma unroll
                for (int l = 0; l < 9; ++l)
#pragma unroll
                    for (int m = 0; m < 2; ++m) acc[m][l] = mfma16(bb[l], a[m], acc[m][l]);
            }
            bf16x8 am = wn ? a[1] : a[0];
            rs = mfma16(ones, am, rs);
            rq = mfma16(am, am, rq);
            bf16x8 c0 = wm == 0 ? bb[0] : wm == 1 ? bb[1] : wm == 2 ? bb[2] : bb[3];
            bf16x8 c1 = wm == 0 ? bb[4] : wm == 1 ? bb[5] : wm == 2 ? bb[6] : bb[7];
            csum[0] = mfma16(ones, c0, csum[0]);
            csum[1] = mfma16(ones, c1, csum[1]);
            if (wm == 0) csum[2] = mfma16(ones, bb[8], csum[2]);
        }
        buf = buf + 1 == 3 ? 0 : buf + 1;
    }
    LAS f32x2* rowst = (LAS f32x2*)(lds + K1_STAGE * K1_NBUF);
    LAS float* cs = (LAS float*)(lds + K1_STAGE * K1_NBUF + 1024);
    {
        if ((fr >> 2) == g) {
            float sum = rs[0];
            float sq = (fr & 3) == 0 ? rq[0] : (fr & 3) == 1 ? rq[1] : (fr & 3) == 2 ? rq[2] : rq[3];
            float mean = sum * (1.0f / D);
            float var = fmaxf(sq * (1.0f / D) - mean * mean, 0.f);
            float mb = rbf(mean), vb = rbf(var);
            float den = rbf(__builtin_sqrtf(rbf(vb + 1e-5f)));
            float rd = 1.0f / den;
            int lam = wm * 32 + wn * 16 + fr;
            rowst[lam] = (f32x2){mb, rd};
            if (!isz2 && ct == 0) ((f32x2*)(ws + WS_ST1))[pi * 128 + lam] = (f32x2){mb, den};
        }
        if (g == 0) {
            cs[(wn * 9 + wm) * 16 + fr] = csum[0][0];
            cs[(wn * 9 + wm + 4) * 16 + fr] = csum[1][0];
            if (wm == 0) cs[(wn * 9 + 8) * 16 + fr] = csum[2][0];
        }
    }
    WAIT_L0();
    BAR();
    const int fp = 2 * ct + wn;
    if (isz2) {
        f32x2 st[2][4];
#pragma unroll
        for (int m = 0; m < 2; ++m)
#pragma unroll
            for (int rr = 0; rr < 4; ++rr) st[m][rr] = rowst[wm * 32 + m * 16 + 4 * g + rr];
        float sum[8], sq[8];
#pragma unroll
        for (int j = 0; j < 8; ++j) { sum[j] = 0.f; sq[j] = 0.f; }
        const int kt = pi * 2 + (wm >> 1), ks = wm & 1;
        char* dst = ws + WS_Z2 + ((size_t)(fp * 32 + kt) * 18 + ks) * 1024 + lane * 16;
#pragma unroll
        for (int l = 0; l < 9; ++l) {
            const float c = cs[(wn * 9 + l) * 16 + fr];
            float z[8];
#pragma unroll
            for (int m = 0; m < 2; ++m)
#pragma unroll
                for (int rr = 0; rr < 4; ++rr) z[m * 4 + rr] = gelu_tanh(rbf((acc[m][l][rr] - st[m][rr].x * c) * st[m][rr].y));
            u32x4 w;
#pragma unroll
            for (int j = 0; j < 4; ++j) {
                w[j] = pk2(z[2 * j], z[2 * j + 1]);
                float lo = bflo(w[j]), hi = bfhi(w[j]);
                sum[2 * j] += lo; sq[2 * j] = __builtin_fmaf(lo, lo, sq[2 * j]);
                sum[2 * j + 1] += hi; sq[2 * j + 1] = __builtin_fmaf(hi, hi, sq[2 * j + 1]);
            }
            const int n = l == 8 ? (wn ? 0 : 8) : l + wn;
            *(u32x4*)(dst + n * 2048) = w;
        }
#pragma unroll
        for (int j = 0; j < 8; ++j) {
#pragma unroll
            for (int o = 1; o < 16; o <<= 1) { sum[j] += __shfl_xor(sum[j], o); sq[j] += __shfl_xor(sq[j], o); }
        }
        if (fr < 8) {
            float a = sum[0], c = sq[0];
#pragma unroll
            for (int j = 1; j < 8; ++j) { a = fr == j ? sum[j] : a; c = fr == j ? sq[j] : c; }
            ((f32x2*)(ws + WS_ST2))[fp * S + pi * 128 + wm * 32 + 8 * g + fr] = (f32x2){a, c};
        }
    } else {
        f32x2 st[2];
#pragma unroll
        for (int m = 0; m < 2; ++m) st[m] = rowst[wm * 32 + m * 16 + fr];
        const int kap0 = (9 * fp + wn) >> 1;
        const int kap8 = wn ? (9 * fp) >> 1 : (9 * fp + 8) >> 1;
#pragma unroll
        for (int m = 0; m < 2; ++m) {
            const int tf = pi * 8 + wm * 2 + m;
            char* dst = ws + WS_Z1 + (size_t)tf * 72 * 1024 + lane * 16;
#pragma unroll
            for (int lp = 0; lp < 4; ++lp) {
                u32x4 w;
#pragma unroll
                for (int h = 0; h < 2; ++h) {
                    const int l = 2 * lp + h;
                    const f32x4 c = *(LAS f32x4*)(cs + (wn * 9 + l) * 16 + 4 * g);
                    float z[4];
#pragma unroll
                    for (int rr = 0; rr < 4; ++rr) z[rr] = gelu_tanh(rbf((acc[m][l][rr] - st[m].x * c[rr]) * st[m].y));
                    w[2 * h] = pk2(z[0], z[1]); w[2 * h + 1] = pk2(z[2], z[3]);
                }
                *(u32x4*)(dst + (kap0 + lp) * 1024) = w;
            }
            {
                const f32x4 c = *(LAS f32x4*)(cs + (wn * 9 + 8) * 16 + 4 * g);
                float z[4];
#pragma unroll
                for (int rr = 0; rr < 4; ++rr) z[rr] = gelu_tanh(rbf((acc[m][8][rr] - st[m].x * c[rr]) * st[m].y));
                u32x2 w = {pk2(z[0], z[1]), pk2(z[2], z[3])};
                *(u32x2*)(dst + kap8 * 1024 + wn * 8) = w;
            }
        }
    }
    if (tid < 288) {
        const int st_ = b >> 3, dt = b & 7;
        const int sg = tid / 36, dc = tid - sg * 36;
        const int d0 = dt * 72 + dc * 2, s0 = st_ * 64 + sg * 8;
        unsigned kw[11], vw[8];
#pragma unroll
        for (int i = 0; i < 11; ++i) { int s = s0 - 3 + i; kw[i] = s >= 0 ? *(const unsigned*)(kin + (size_t)s * D + d0) : 0u; }
#pragma unroll
        for (int i = 0; i < 8; ++i) vw[i] = *(const unsigned*)(vin + (size_t)(s0 + i) * D + d0);
        const u32x4 sk = *(const u32x4*)(shk + d0 * 4);
        float o0[8], o1[8];
#pragma unroll
        for (int i = 0; i < 8; ++i) {
            float a0 = rbf(bflo(kw[i]) * bfhi(sk[1])), a1 = rbf(bfhi(kw[i]) * bfhi(sk[3]));
            a0 = rbf(a0 + rbf(bflo(kw[i + 1]) * bflo(sk[1]))); a1 = rbf(a1 + rbf(bfhi(kw[i + 1]) * bflo(sk[3])));
            a0 = rbf(a0 + rbf(bflo(kw[i + 2]) * bfhi(sk[0]))); a1 = rbf(a1 + rbf(bfhi(kw[i + 2]) * bfhi(sk[2])));
            a0 = rbf(a0 + rbf(bflo(kw[i + 3]) * bflo(sk[0]))); a1 = rbf(a1 + rbf(bfhi(kw[i + 3]) * bflo(sk[2])));
            o0[i] = a0 * bflo(vw[i]); o1[i] = a1 * bfhi(vw[i]);
        }
        u32x4 w0 = {pk2(o0[7], o0[6]), pk2(o0[5], o0[4]), pk2(o0[3], o0[2]), pk2(o0[1], o0[0])};
        u32x4 w1 = {pk2(o1[7], o1[6]), pk2(o1[5], o1[4]), pk2(o1[3], o1[2]), pk2(o1[1], o1[0])};
        *(u32x4*)(ws + WS_KVT + ((size_t)d0 * S + s0) * 2) = w0;
        *(u32x4*)(ws + WS_KVT + ((size_t)(d0 + 1) * S + s0) * 2) = w1;
    }
}

constexpr int CV_U = 512 + 4096;
constexpr int CV_HS = 4096 + 64 + 64;
constexpr int CV_HSTRIDE = 4160;
constexpr int CV_WAVE = CV_U + 4 * CV_HSTRIDE;
constexpr int K15_LDS = 4 * CV_WAVE;

__global__ __launch_bounds__(256) void k15_norm_conv(const u16* __restrict__ hk, const u16* __restrict__ dv, char* __restrict__ ws) {
    extern __shared__ __attribute__((aligned(16))) char smem_[];
    LAS char* lds = (LAS char*)smem_;
    const int b = blockIdx.x, tid = threadIdx.x, wid = __builtin_amdgcn_readfirstlane(tid >> 6), lane = tid & 63;
    const int fr = lane & 15, g = lane >> 4;
    if (wid == 0) STAMP(1, 0);
    if (b < 256) {
        const int xcd = b & 7, r = b >> 3, gi = xcd & 1, gj = xcd >> 1;
        const int kt = gi * 16 + (r & 15), ct = 2 * gj + (r >> 4);
        char* base = ws + WS_Z2 + lane * 16;
        u32x4 v[9];
#pragma unroll
        for (int i = 0; i < 9; ++i) {
            int c = wid * 9 + i;
            int fp = 2 * ct + (c >= 18), cc = c >= 18 ? c - 18 : c;
            v[i] = *(const u32x4*)(base + ((size_t)(fp * 32 + kt) * 18 + cc) * 1024);
        }
        LAS f32x2* part = (LAS f32x2*)lds;
        LAS f32x2* fin = (LAS f32x2*)(lds + 2048);
        {
            const f32x2* st = (const f32x2*)(ws + WS_ST2) + kt * 64 + lane;
            f32x2 a = st[(wid * 4 + 0) * S], c = st[(wid * 4 + 1) * S], d = st[(wid * 4 + 2) * S], e = st[(wid * 4 + 3) * S];
            part[wid * 64 + lane] = (a + c) + (d + e);
        }
        __syncthreads();
        if (wid == 0) {
            f32x2 t = (part[lane] + part[64 + lane]) + (part[128 + lane] + part[192 + lane]);
            float mean = t.x * (1.0f / FH);
            float var = fmaxf(t.y * (1.0f / FH) - mean * mean, 0.f);
            float mb = rbf(mean), vb = rbf(var);
            float den = rbf(__builtin_sqrtf(rbf(vb + 1e-5f)));
            fin[lane] = (f32x2){mb, 1.0f / den};
        }
        __syncthreads();
        f32x2 st[2][8];
#pragma unroll
        for (int ks = 0; ks < 2; ++ks)
#pragma unroll
            for (int j = 0; j < 8; ++j) st[ks][j] = fin[ks * 32 + 8 * g + j];
#pragma unroll
        for (int i = 0; i < 9; ++i) {
            int c = wid * 9 + i;
            int fp = 2 * ct + (c >= 18), cc = c >= 18 ? c - 18 : c;
            const bool k1 = cc & 1;
            u32x4 w;
#pragma unroll
            for (int j = 0; j < 4; ++j) {
                f32x2 s0 = k1 ? st[1][2 * j] : st[0][2 * j], s1 = k1 ? st[1][2 * j + 1] : st[0][2 * j + 1];
                w[j] = pk2(rbf(bflo(v[i][j]) - s0.x) * s0.y, rbf(bfhi(v[i][j]) - s1.x) * s1.y);
            }
            *(u32x4*)(base + ((size_t)(fp * 32 + kt) * 18 + cc) * 1024) = w;
        }
        return;
    }
    const int d = (b - 256) * 4 + wid;
    LAS char* U = lds + wid * CV_WAVE;
    LAS char* H = U + CV_U;
    {
        const char* usrc = ws + WS_KVT + (size_t)d * S * 2 + lane * 16;
        const char* hsrc = (const char*)hk + (size_t)d * S * 2 + lane * 16;
        u32x4 uu[4], hh[4]; u32x2 hn[4];
#pragma unroll
        for (int i = 0; i < 4; ++i) { uu[i] = *(const u32x4*)(usrc + i * 1024); hh[i] = *(const u32x4*)(hsrc + i * 1024); }
#pragma unroll
        for (int i = 0; i < 4; ++i) { bool last = (i == 3 && lane == 63); hn[i] = last ? (u32x2){0u, 0u} : *(const u32x2*)(hsrc + i * 1024 + 16); }
        if (lane < 32) *(LAS u32x4*)(U + lane * 16) = (u32x4){0u, 0u, 0u, 0u};
        if (lane < 8) {
            int c = lane >> 1, half = lane & 1;
            u32x4 z = {0u, 0u, 0u, 0u};
            if (half) {
                unsigned h0 = *(const unsigned*)((const char*)hk + (size_t)d * S * 2), h1 = *(const unsigned*)((const char*)hk + (size_t)d * S * 2 + 4);
                if (c == 1) z[3] = h0 << 16;
                if (c == 2) z[3] = h0;
                if (c == 3) { z[2] = h0 << 16; z[3] = __builtin_amdgcn_alignbit(h1, h0, 16); }
            }
            *(LAS u32x4*)(H + c * CV_HSTRIDE + half * 16) = z;
        }
#pragma unroll
        for (int i = 0; i < 4; ++i) {
            *(LAS u32x4*)(U + 512 + i * 1024 + lane * 16) = uu[i];
            unsigned w0 = hh[i][0], w1 = hh[i][1], w2 = hh[i][2], w3 = hh[i][3], w4 = hn[i][0], w5 = hn[i][1];
            unsigned a01 = __builtin_amdgcn_alignbit(w1, w0, 16), a12 = __builtin_amdgcn_alignbit(w2, w1, 16), a23 = __builtin_amdgcn_alignbit(w3, w2, 16),
                     a34 = __builtin_amdgcn_alignbit(w4, w3, 16), a45 = __builtin_amdgcn_alignbit(w5, w4, 16);
            LAS char* p = H + 32 + i * 1024 + lane * 16;
            *(LAS u32x4*)(p) = hh[i];
            *(LAS u32x4*)(p + CV_HSTRIDE) = (u32x4){a01, a12, a23, a34};
            *(LAS u32x4*)(p + 2 * CV_HSTRIDE) = (u32x4){w1, w2, w3, w4};
            *(LAS u32x4*)(p + 3 * CV_HSTRIDE) = (u32x4){a12, a23, a34, a45};
        }
    }
    WAIT_L0();
    __builtin_amdgcn_sched_barrier(0);
    const int e0 = 16 * (g >> 1) - 8 * (g & 1) - 7 + fr + 16;
    const int ac = e0 & 3;
    LAS char* ap = H + ac * CV_HSTRIDE + (e0 - ac) * 2;
    LAS char* bp = U + 512 + 32 * (fr - (g >> 1)) + 16 * (g & 1);
    f32x4 acc[8];
#pragma unroll
    for (int n = 0; n < 8; ++n) acc[n] = (f32x4){0.f, 0.f, 0.f, 0.f};
#pragma unroll 2
    for (int bb = 0; bb < 8; ++bb) {
        bf16x8 A[8], B[8];
#pragma unroll
        for (int a = 0; a < 8; ++a) {
            LAS char* p = ap + (8 * a + bb) * 64;
            A[a] = cat8(*(LAS bf16x4*)p, *(LAS bf16x4*)(p + 8));
            B[a] = *(LAS bf16x8*)(bp + a * 512 - bb * 64);
        }
#pragma unroll
        for (int a = 0; a < 8; ++a)
#pragma unroll
            for (int c = 0; c + a < 8; ++c) acc[a + c] = mfma16(A[a], B[c], acc[a + c]);
    }
    const float dd = bflo((unsigned)dv[d]);
    char* dst = ws + WS_YT + (size_t)d * S * 2 + (16 * fr + 4 * g) * 2;
#pragma unroll
    for (int nu = 0; nu < 8; ++nu) {
        u32x2 uw = *(LAS u32x2*)(U + 512 + (256 * nu + 16 * fr + 8 * (g >> 1)) * 2 + ((g & 1) ? 0 : 8));
        float u0 = bfhi(uw[1]), u1 = bflo(uw[1]), u2 = bfhi(uw[0]), u3 = bflo(uw[0]);
        float y0 = rbf(acc[nu][0]) + rbf(u0 * dd), y1 = rbf(acc[nu][1]) + rbf(u1 * dd), y2 = rbf(acc[nu][2]) + rbf(u2 * dd), y3 = rbf(acc[nu][3]) + rbf(u3 * dd);
        *(u32x2*)(dst + nu * 512) = (u32x2){pk2(y0, y1), pk2(y2, y3)};
    }
}

constexpr int K2_A = 16384, K2_B = 18432, K2_STAGE = K2_A + K2_B, K2_NBUF = 4, K2_LDS = K2_STAGE * K2_NBUF;

template <int NB>
__device__ __forceinline__ void k2_loader(const char* srcA0, const char* srcA1, const char* srcB, LAS char* ldsA, LAS char* ldsB) {
    constexpr int PPW = 4 + NB;
    auto stage = [&](int t) {
        const int so = (t & 3) * K2_STAGE;
#pragma unroll
        for (int i = 0; i < 4; ++i) glds16(((i & 1) ? srcA1 : srcA0) + (size_t)i * (8 * S * 2) + t * 128, ldsA + so + i * 1024);
#pragma unroll
        for (int i = 0; i < NB; ++i) glds16(srcB + t * K2_B + i * 4096, ldsB + so + i * 4096);
    };
    stage(0); stage(1); stage(2);
#pragma unroll 1
    for (int t = 0; t < 29; ++t) {
        WAIT_V(2 * PPW);
        BAR();
        stage(t + 3);
    }
    WAIT_V(2 * PPW); BAR();
    WAIT_V(PPW); BAR();
    WAIT_V(0); BAR();
}

__global__ __launch_bounds__(512, 2) void k2_gemm2(const u16* __restrict__ w_sp, const u16* __restrict__ b_sp, char* __restrict__ ws) {
    extern __shared__ __attribute__((aligned(16))) char smem_[];
    LAS char* lds = (LAS char*)smem_;
    const int b = blockIdx.x, xcd = b & 7, r = b >> 3;
    const int gi = xcd & 1, gj = xcd >> 1;
    const int pi = gi * 8 + (r & 7), pj = gj * 4 + (r >> 3);
    const int tid = threadIdx.x, wid = __builtin_amdgcn_readfirstlane(tid >> 6), lane = tid & 63;
    const int fr = lane & 15, g = lane >> 4;
    if (wid >= 4) {
        const int w = wid - 4;
        const int lam = w * 32 + (lane >> 3);
        const int ch = (lane & 7) ^ ((lam >> 1) & 3);
        const char* srcA0 = (const char*)w_sp + (size_t)(pi * 128 + lam) * (S * 2) + ch * 16;
        const char* srcA1 = (const char*)w_sp + (size_t)(pi * 128 + lam) * (S * 2) + (ch ^ 4) * 16;
        const char* srcB = ws + WS_Z2 + (size_t)pj * 32 * K2_B + w * 1024 + lane * 16;
        if (w < 2) k2_loader<5>(srcA0, srcA1, srcB, lds + w * 4096, lds + K2_A + w * 1024);
        else k2_loader<4>(srcA0, srcA1, srcB, lds + w * 4096, lds + K2_A + w * 1024);
        return;
    }
    const int wm = wid;
    if (wid == 0) STAMP(2, 0);
    const int aoff = (wm * 32 + fr) * 128 + ((g ^ ((fr >> 1) & 7)) << 4);
    const int boff = K2_A + lane * 16;
    f32x4 acc[2][9];
#pragma unroll
    for (int m = 0; m < 2; ++m)
#pragma unroll
        for (int n = 0; n < 9; ++n) acc[m][n] = (f32x4){0.f, 0.f, 0.f, 0.f};
    bf16x8 a0[2], b0[9], a1[2], b1[9];
#define K2_READ(A, B, sb, ks) do { _Pragma("unroll") for (int m = 0; m < 2; ++m) A[m] = *(LAS bf16x8*)((sb) + (aoff ^ ((ks) * 64)) + m * 2048); \
        _Pragma("unroll") for (int l = 0; l < 9; ++l) B[l] = *(LAS bf16x8*)((sb) + boff + l * 2048 + (ks) * 1024); } while (0)
#define K2_MMA(A, B) do { _Pragma("unroll") for (int l = 0; l < 9; ++l) _Pragma("unroll") for (int m = 0; m < 2; ++m) acc[m][l] = mfma16(B[l], A[m], acc[m][l]); } while (0)
#define K2_SCHED() do { _Pragma("unroll") for (int i = 0; i < 11; ++i) { __builtin_amdgcn_sched_group_barrier(0x008, 1, 0); __builtin_amdgcn_sched_group_barrier(0x100, 1, 0); } \
        __builtin_amdgcn_sched_group_barrier(0x008, 7, 0); } while (0)
    BAR();
    if (wid == 0) { STAMP(2, 1); STAMPC(2, 14); }
    K2_READ(a0, b0, lds, 0);
    for (int t = 0; t < 31; ++t) {
        LAS char* sb = lds + (t & 3) * K2_STAGE;
        LAS char* sn = lds + ((t + 1) & 3) * K2_STAGE;
        K2_READ(a1, b1, sb, 1);
        K2_MMA(a0, b0);
        K2_SCHED();
        __builtin_amdgcn_sched_barrier(0);
        WAIT_L0();
        BAR();
        K2_READ(a0, b0, sn, 0);
        K2_MMA(a1, b1);
        K2_SCHED();
        __builtin_amdgcn_sched_barrier(0);
    }
    {
        LAS char* sb = lds + 3 * K2_STAGE;
        K2_READ(a1, b1, sb, 1);
        K2_MMA(a0, b0);
        K2_SCHED();
        __builtin_amdgcn_sched_barrier(0);
        K2_MMA(a1, b1);
    }
    if (wid == 0) { STAMP(2, 5); STAMPC(2, 15); }
    const int odd = pj & 1;
#pragma unroll
    for (int m = 0; m < 2; ++m) {
        const int tf = pi * 8 + wm * 2 + m;
        const float bias = bflo((unsigned)b_sp[tf * 16 + fr]);
        const size_t off = (size_t)tf * 72 * 1024 + lane * 16 + (size_t)((9 * pj) >> 1) * 1024;
        u32x2 gq[9];
#pragma unroll
        for (int n = 0; n < 9; ++n) {
            const f32x4 a = acc[m][n];
            gq[n] = (u32x2){pk2(rbf(a[0]) + bias, rbf(a[1]) + bias), pk2(rbf(a[2]) + bias, rbf(a[3]) + bias)};
        }
        auto mul2 = [](unsigned z, unsigned gg) { return pk2(bflo(z) * bflo(gg), bfhi(z) * bfhi(gg)); };
        if (odd) {
            const u32x2 z = *(const u32x2*)(ws + WS_Z1 + off + 8);
            *(u32x2*)(ws + WS_P + off + 8) = (u32x2){mul2(z[0], gq[0][0]), mul2(z[1], gq[0][1])};
#pragma unroll
            for (int lp = 0; lp < 4; ++lp) {
                const u32x4 zz = *(const u32x4*)(ws + WS_Z1 + off + (lp + 1) * 1024);
                *(u32x4*)(ws + WS_P + off + (lp + 1) * 1024) = (u32x4){mul2(zz[0], gq[2 * lp + 1][0]), mul2(zz[1], gq[2 * lp + 1][1]), mul2(zz[2], gq[2 * lp + 2][0]), mul2(zz[3], gq[2 * lp + 2][1])};
            }
        } else {
#pragma unroll
            for (int lp = 0; lp < 4; ++lp) {
                const u32x4 zz = *(const u32x4*)(ws + WS_Z1 + off + lp * 1024);
                *(u32x4*)(ws + WS_P + off + lp * 1024) = (u32x4){mul2(zz[0], gq[2 * lp][0]), mul2(zz[1], gq[2 * lp][1]), mul2(zz[2], gq[2 * lp + 1][0]), mul2(zz[3], gq[2 * lp + 1][1])};
            }
            const u32x2 z = *(const u32x2*)(ws + WS_Z1 + off + 4 * 1024);
            *(u32x2*)(ws + WS_P + off + 4 * 1024) = (u32x2){mul2(z[0], gq[8][0]), mul2(z[1], gq[8][1])};
        }
    }
    WAIT_V(0);
    if (wid == 0) STAMP(2, 6);
}

constexpr int K3_SLOT = 9 * 1024, K3_WAVE = 2 * K3_SLOT, K3_LDS = 8 * K3_WAVE;
constexpr int K3_NBLK = 26 * 9;

__global__ __launch_bounds__(512, 2) void k3_gemm3(const u16* __restrict__ w_out, const u16* __restrict__ x, char* __restrict__ ws, u16* __restrict__ out) {
    extern __shared__ __attribute__((aligned(16))) char smem_[];
    LAS char* lds = (LAS char*)smem_;
    int L; { const int q = K3_NBLK / 8, rr = K3_NBLK % 8, xc = blockIdx.x & 7, off = blockIdx.x >> 3; L = (xc < rr ? xc * (q + 1) : rr * (q + 1) + (xc - rr) * q) + off; }
    const int rt = L / 9, ctile = L - rt * 9;
    const int tid = threadIdx.x, wid = __builtin_amdgcn_readfirstlane(tid >> 6), lane = tid & 63;
    const int fr = lane & 15, g = lane >> 4;
    LAS char* my = lds + wid * K3_WAVE;
    if (wid == 0) STAMP(3, 0);
    const int kap0 = wid * 9;
    const char* srcA = ws + WS_P + (size_t)kap0 * 1024 + lane * 16;
    int tfo[5];
#pragma unroll
    for (int m = 0; m < 5; ++m) { int tf = rt * 5 + m; if (tf > 127) tf = 127; tfo[m] = tf * 72 * 1024; }
    const int rowl = lane >> 3;
    const char* srcB = (const char*)w_out + (size_t)(kap0 * 32 + rowl) * (D * 2) + ctile * 128 + (((lane & 7) ^ (((rowl >> 1) & 3) << 1)) << 4);
    auto stage = [&](int i) {
        LAS char* base = my + (i & 1) * K3_SLOT;
#pragma unroll
        for (int m = 0; m < 5; ++m) glds16(srcA + tfo[m] + i * 1024, base + m * 1024);
#pragma unroll
        for (int p = 0; p < 4; ++p) glds16(srcB + (size_t)(i * 32 + p * 8) * (D * 2), base + 5120 + p * 1024);
    };
    stage(0); stage(1);
    f32x4 acc[5][4];
#pragma unroll
    for (int m = 0; m < 5; ++m)
#pragma unroll
        for (int n = 0; n < 4; ++n) acc[m][n] = (f32x4){0.f, 0.f, 0.f, 0.f};
    const int q4 = (lane >> 2) & 3, p4 = lane & 3;
    const int sig = (2 * g + (q4 >> 1)) & 3;
    int bo[4];
#pragma unroll
    for (int n = 0; n < 4; ++n) bo[n] = 5120 + (4 * g + q4) * 128 + ((n ^ sig) << 5) + p4 * 8;
    for (int i = 0; i < 9; ++i) {
        if (i < 8) WAIT_V(9); else WAIT_V(0);
        LAS char* sb = my + (i & 1) * K3_SLOT;
        bf16x8 a[5], bb[4];
#pragma unroll
        for (int m = 0; m < 5; ++m) a[m] = *(LAS bf16x8*)(sb + m * 1024 + lane * 16);
#pragma unroll
        for (int n = 0; n < 4; ++n) bb[n] = cat8(trread(sb + bo[n]), trread(sb + bo[n] + 2048));
        WAIT_L0();
        __builtin_amdgcn_sched_barrier(0);
        if (i + 2 < 9) stage(i + 2);
#pragma unroll
        for (int m = 0; m < 5; ++m)
#pragma unroll
            for (int n = 0; n < 4; ++n) acc[m][n] = mfma16(bb[n], a[m], acc[m][n]);
    }
    BAR();
    if (wid >= 4) {
#pragma unroll
        for (int m = 0; m < 5; ++m)
#pragma unroll
            for (int n = 0; n < 4; ++n) *(LAS f32x4*)(lds + (wid - 4) * 20480 + (m * 4 + n) * 1024 + lane * 16) = acc[m][n];
    }
    WAIT_L0();
    BAR();
    constexpr int RS = 272, R2 = 81920 - 20480 * 4 + 0;
    if (wid < 4) {
#pragma unroll
        for (int m = 0; m < 5; ++m)
#pragma unroll
            for (int n = 0; n < 4; ++n) acc[m][n] += *(LAS f32x4*)(lds + wid * 20480 + (m * 4 + n) * 1024 + lane * 16);
    }
    WAIT_L0();
    BAR();
    if (wid < 4) {
#pragma unroll
        for (int m = 0; m < 5; ++m)
#pragma unroll
            for (int n = 0; n < 4; ++n) *(LAS f32x4*)(lds + R2 + wid * (80 * RS) + (m * 16 + fr) * RS + (n * 16 + 4 * g) * 4) = acc[m][n];
    } else {
        for (int c = tid - 256; c < 640; c += 256) {
            int dd = c / 10, tc = c - dd * 10;
            int t0 = rt * 80 + tc * 8; if (t0 > S - 8) t0 = S - 8;
            u32x4 v = *(const u32x4*)(ws + WS_YT + ((size_t)(ctile * 64 + dd) * S + t0) * 2);
            *(LAS u32x4*)(lds + 90112 + dd * 176 + tc * 16) = v;
        }
    }
    WAIT_L0();
    BAR();
    for (int c = tid; c < 640; c += 512) {
        const int tl = c >> 3, dc = c & 7, t = rt * 80 + tl;
        if (t < S) {
            f32x4 s0 = {0.f, 0.f, 0.f, 0.f}, s1 = s0;
#pragma unroll
            for (int w = 0; w < 4; ++w) {
                s0 += *(LAS f32x4*)(lds + R2 + w * (80 * RS) + tl * RS + dc * 32);
                s1 += *(LAS f32x4*)(lds + R2 + w * (80 * RS) + tl * RS + dc * 32 + 16);
            }
            const u32x4 xv = *(const u32x4*)(x + (size_t)t * D + ctile * 64 + dc * 8);
            const f32x2 st = ((const f32x2*)(ws + WS_ST1))[t];
            float res[8];
#pragma unroll
            for (int j = 0; j < 8; ++j) {
                float o = rbf(j < 4 ? s0[j] : s1[j - 4]);
                float sg = rbf(o * __builtin_amdgcn_rcpf(1.0f + __builtin_amdgcn_exp2f(-1.4426950408889634f * o)));
                float xx = (j & 1) ? bfhi(xv[j >> 1]) : bflo(xv[j >> 1]);
                float xn = rbf(rbf(xx - st.x) / st.y);
                float y = bflo((unsigned)*(LAS u16*)(lds + 90112 + (dc * 8 + j) * 176 + tl * 2));
                float s = rbf(xn * y);
                res[j] = xx + rbf(s * sg);
            }
            *(u32x4*)(out + (size_t)t * D + ctile * 64 + dc * 8) = (u32x4){pk2(res[0], res[1]), pk2(res[2], res[3]), pk2(res[4], res[5]), pk2(res[6], res[7])};
        }
    }
}

extern "C" void kernel_launch(void* const* d_in, const int* in_sizes, int n_in,
                              void* d_out, int out_size, void* d_ws, size_t ws_size,
                              hipStream_t stream) {
    const u16* q = (const u16*)d_in[0];
    const u16* k = (const u16*)d_in[1];
    const u16* v = (const u16*)d_in[2];
    const u16* sk = (const u16*)d_in[3];
    const u16* h = (const u16*)d_in[4];
    const u16* Dv = (const u16*)d_in[5];
    const u16* w_in = (const u16*)d_in[7];
    const u16* w_sp = (const u16*)d_in[8];
    const u16* b_sp = (const u16*)d_in[9];
    const u16* w_out = (const u16*)d_in[10];
    char* ws = (char*)d_ws;
    hipFuncSetAttribute((const void*)k1_gemm1, hipFuncAttributeMaxDynamicSharedMemorySize, K1_LDS);
    hipFuncSetAttribute((const void*)k15_norm_conv, hipFuncAttributeMaxDynamicSharedMemorySize, K15_LDS);
    hipFuncSetAttribute((const void*)k2_gemm2, hipFuncAttributeMaxDynamicSharedMemorySize, K2_LDS);
    hipFuncSetAttribute((const void*)k3_gemm3, hipFuncAttributeMaxDynamicSharedMemorySize, K3_LDS);
    k1_gemm1<<<256, 512, K1_LDS, stream>>>(q, w_in, k, v, sk, ws);
    k15_norm_conv<<<256 + 144, 256, K15_LDS, stream>>>(h, Dv, ws);
    k2_gemm2<<<256, 512, K2_LDS, stream>>>(w_sp, b_sp, ws);
    k3_gemm3<<<K3_NBLK, 512, K3_LDS, stream>>>(w_out, q, ws, (u16*)d_out);
}
```

```cpp
#include <hip/hip_runtime.h>
#include <stdint.h>

#define LAS __attribute__((address_space(3)))
#define GAS __attribute__((address_space(1)))
typedef unsigned short u16;
typedef short bf16x8 __attribute__((ext_vector_type(8)));
typedef short bf16x4 __attribute__((ext_vector_type(4)));
typedef float f32x4 __attribute__((ext_vector_type(4)));
typedef float f32x2 __attribute__((ext_vector_type(2)));
typedef unsigned u32x4 __attribute__((ext_vector_type(4)));
typedef unsigned u32x2 __attribute__((ext_vector_type(2)));
typedef __bf16 bf16x2_t __attribute__((ext_vector_type(2)));

constexpr int S = 2048, D = 576, F = 4608, FH = 2304;

__device__ __forceinline__ float bflo(unsigned w) { return __uint_as_float(w << 16); }
__device__ __forceinline__ float bfhi(unsigned w) { return __uint_as_float(w & 0xffff0000u); }
__device__ __forceinline__ unsigned pk2v(f32x2 p) { return __builtin_bit_cast(unsigned, __builtin_convertvector(p, bf16x2_t)); }
__device__ __forceinline__ unsigned pk2(float lo, float hi) { return pk2v((f32x2){lo, hi}); }
__device__ __forceinline__ f32x2 up2(unsigned w) { return (f32x2){bflo(w), bfhi(w)}; }
__device__ __forceinline__ float rbf(float x) { return bflo(pk2(x, x)); }
__device__ __forceinline__ f32x2 rbf2(f32x2 x) { return up2(pk2v(x)); }
__device__ __forceinline__ f32x4 mfma16(bf16x8 a, bf16x8 b, f32x4 c) { return __builtin_amdgcn_mfma_f32_16x16x32_bf16(a, b, c, 0, 0, 0); }
__device__ __forceinline__ void glds16(const void* g, LAS char* l) { __builtin_amdgcn_global_load_lds((const unsigned*)g, (LAS unsigned*)l, 16, 0, 0); }
__device__ __forceinline__ void glds16c(const void* g, LAS char* l) { __builtin_amdgcn_global_load_lds((const unsigned*)g, (LAS unsigned*)l, 16, 0, 16); }
__device__ __forceinline__ bf16x4 trread(LAS char* p) { return __builtin_bit_cast(bf16x4, __builtin_amdgcn_ds_read_tr16_b64_v4i16((LAS bf16x4*)p)); }
__device__ __forceinline__ bf16x8 cat8(bf16x4 a, bf16x4 b) { return __builtin_shufflevector(a, b, 0, 1, 2, 3, 4, 5, 6, 7); }
#define WAIT_V(n) asm volatile("s_waitcnt vmcnt(%0)" ::"n"(n) : "memory")
#define WAIT_L0() asm volatile("s_waitcnt lgkmcnt(0)" ::: "memory")
#define BAR() do { asm volatile("" ::: "memory"); __builtin_amdgcn_s_barrier(); asm volatile("" ::: "memory"); } while (0)
#define SCHED0() __builtin_amdgcn_sched_barrier(0)
#define GLD2(dst, base, off) asm volatile("global_load_dwordx2 %0, %1, %2" : "=v"(dst) : "v"(off), "s"(base) : "memory")
#define GLD4(dst, base, off) asm volatile("global_load_dwordx4 %0, %1, %2" : "=v"(dst) : "v"(off), "s"(base) : "memory")
#define RLX_AGENT __ATOMIC_RELAXED, __HIP_MEMORY_SCOPE_AGENT

#define STAMP(kid, i) do { } while (0)
#define STAMPC(kid, i) do { } while (0)

__device__ __forceinline__ f32x2 gelu2(f32x2 v) {
    const float c1 = -2.0f * 0.7978845608028654f * 1.4426950408889634f;
    const float c2 = c1 * 0.044715f;
    f32x2 a = v * __builtin_elementwise_fma(v * v, (f32x2){c2, c2}, (f32x2){c1, c1});
    f32x2 d = (f32x2){__builtin_amdgcn_exp2f(a.x), __builtin_amdgcn_exp2f(a.y)} + (f32x2){1.0f, 1.0f};
    return v * (f32x2){__builtin_amdgcn_rcpf(d.x), __builtin_amdgcn_rcpf(d.y)};
}
template <int CTRL> __device__ __forceinline__ float dppf(float v) {
    return __builtin_bit_cast(float, __builtin_amdgcn_update_dpp(0, __builtin_bit_cast(int, v), CTRL, 0xf, 0xf, true));
}
__device__ __forceinline__ float rowsum16(float v) {
    v += dppf<0x128>(v);
    v += dppf<0x124>(v);
    v += dppf<0x122>(v);
    v += dppf<0x121>(v);
    return v;
}

constexpr size_t SZ_Z = (size_t)S * FH * 2, SZ_X = (size_t)S * D * 2;
constexpr size_t WS_Z2R = 0;
constexpr size_t WS_Z2N = WS_Z2R + SZ_Z;
constexpr size_t WS_P = WS_Z2N + SZ_Z;
constexpr size_t WS_XN = WS_P + SZ_Z;
constexpr size_t WS_KVT = WS_XN + SZ_X;
constexpr size_t WS_YT = WS_KVT + SZ_X;
constexpr size_t WS_WT = WS_YT + SZ_X;
constexpr size_t WS_ST2 = WS_WT + (size_t)FH * D * 2;
constexpr size_t WS_FZ = WS_ST2 + (size_t)S * 16 * 8;
constexpr size_t WS_FY = WS_FZ + 16 * 256;
constexpr size_t WS_END = WS_FY + 4096;

__device__ __forceinline__ __amdgpu_buffer_rsrc_t ws_rsrc(char* ws) { return __builtin_amdgcn_make_buffer_rsrc(ws, 0, (int)WS_END, 0x00020000); }
template <int AUX> __device__ __forceinline__ void st16(__amdgpu_buffer_rsrc_t r, unsigned off, u32x4 v) { __builtin_amdgcn_raw_buffer_store_b128(v, r, off, 0, AUX); }
template <int AUX> __device__ __forceinline__ void st8(__amdgpu_buffer_rsrc_t r, unsigned off, u32x2 v) { __builtin_amdgcn_raw_buffer_store_b64(v, r, off, 0, AUX); }
template <int AUX> __device__ __forceinline__ u32x4 ld16(__amdgpu_buffer_rsrc_t r, unsigned off) { return __builtin_amdgcn_raw_buffer_load_b128(r, off, 0, AUX); }

constexpr int SL_A = 16384, SL_B = 18432, SLOT = SL_A + SL_B;
constexpr unsigned SPIN_CAP = 1u << 22;

#define SCHED_IL(nr, nm) do { _Pragma("unroll") for (int i_ = 0; i_ < (nr); ++i_) { __builtin_amdgcn_sched_group_barrier(0x008, 1, 0); __builtin_amdgcn_sched_group_barrier(0x100, 1, 0); } \
        __builtin_amdgcn_sched_group_barrier(0x008, (nm) - (nr), 0); SCHED0(); } while (0)

#define K1_ROT 1
#define KB_ROT 1
#define KB_ROT2 (2*pj)
#define KB_PUBT 4

#define K1_AUX 0
#define K1_AUXS 0
#define KB_AUX 0
#define K1_SIDET 1

#define KB_NSPLIT 4

constexpr int K1_NBUF = 4, K1_RING = K1_NBUF * SLOT;
constexpr int K1_ROWST = K1_RING, K1_CS = K1_ROWST + 1024, K1_PST = K1_CS + 1024, K1_FLG = K1_PST + 1024, K1_SCR = K1_RING + 4096;
constexpr int K1_LDS = K1_SCR + 16384;

template <int NB>
__device__ __forceinline__ void k1_loader(const u16* __restrict__ x, const u16* __restrict__ w_in, const u16* __restrict__ kin, const u16* __restrict__ vin,
                                          const u16* __restrict__ shk, const u16* __restrict__ w_out, char* __restrict__ ws, LAS char* lds,
                                          int b, int pi, int pj, int rot, int w, int lane) {
    constexpr int PPW = 4 + NB;
    const int l3 = lane >> 3, lt = w * 64 + lane;
    const unsigned ch = (lane & 7) ^ (l3 >> 1);
    const unsigned srow0 = pi * 128 + 32 * w + 8 * (l3 >> 2) + (l3 & 3);
    const unsigned offA0 = srow0 * (D * 2) + ch * 16, offA1 = (srow0 + 16) * (D * 2) + (ch ^ 4) * 16;
    unsigned offB[NB];
#pragma unroll
    for (int i = 0; i < NB; ++i) {
        const unsigned c = (w + 4 * i) * 64 + lane, rho = c / 18, cc = c - rho * 18;
        const unsigned grow = (rho & ~12u) | ((rho & 4) << 1) | ((rho & 8) >> 1);
        offB[i] = grow * (F * 2) + cc * 16;
    }
    const char* xb = (const char*)x;
    const char* wb = (const char*)w_in + (FH + pj * 144) * 2;
    auto stage = [&](int t) {
        int tt = t + rot; if (tt >= 9) tt -= 9;
        LAS char* sa = lds + (t & 3) * SLOT + w * 4096;
        LAS char* sb = lds + (t & 3) * SLOT + SL_A + w * 1024;
        const char* xs = xb + tt * 128;
        const char* wsrc = wb + (size_t)tt * (64 * F * 2);
        glds16(xs + offA0, sa); glds16(xs + offA1, sa + 1024); glds16(xs + 4 * D * 2 + offA0, sa + 2048); glds16(xs + 4 * D * 2 + offA1, sa + 3072);
#pragma unroll
        for (int i = 0; i < NB; ++i) glds16(wsrc + offB[i], sb + i * 4096);
    };
    stage(0); stage(1); stage(2);
    SCHED0();
    const int it = lt < 144 ? lt : 143;
    const int sg = it / 18, dc = it - sg * 18;
    const int d0 = (b & 7) * 72 + dc * 4, s0 = (b >> 3) * 64 + sg * 8;
    u32x2 kw[11], vw[8]; u32x4 sk0, sk1, xv[3];
    const int item = b * 4 + w;
    const int kap = item / 9, db = item - kap * 9;
    LAS char* scr = lds + K1_SCR + w * 4096;
    const __amdgpu_buffer_rsrc_t rs = ws_rsrc(ws);
    auto sideloads = [&]() {
#pragma unroll
    for (int i = 0; i < 11; ++i) { const int s = s0 - 3 + i; GLD2(kw[i], kin, (unsigned)(((s < 0 ? 0 : s) * D + d0) * 2)); }
#pragma unroll
    for (int i = 0; i < 8; ++i) GLD2(vw[i], vin, (unsigned)(((s0 + i) * D + d0) * 2));
    GLD4(sk0, shk, (unsigned)(d0 * 8)); GLD4(sk1, shk, (unsigned)(d0 * 8 + 16));
#pragma unroll
    for (int i = 0; i < 3; ++i) { int c = lt + 256 * i; if (c > 575) c = 575; GLD4(xv[i], x, (unsigned)((pi * 128 + pj * 8) * (D * 2) + c * 16)); }
    if (item < 648) {
        const int q = kap / 9, e = kap - q * 9;
        const int pr = e & 3;
        const int ga = e == 8 ? 18 * q + 4 : 18 * q + (e >> 2) * 9 + 2 * pr + (pr >> 1);
        const int gb = e == 8 ? ga + 9 : ga + 1;
        const char* src = (const char*)w_out + db * 128 + l3 * (D * 2) + (((lane & 7) ^ ((l3 >> 1) << 1)) << 4);
        glds16(src + (size_t)(ga * 16) * (D * 2), scr); glds16(src + (size_t)(ga * 16 + 8) * (D * 2), scr + 1024);
        glds16(src + (size_t)(gb * 16) * (D * 2), scr + 2048); glds16(src + (size_t)(gb * 16 + 8) * (D * 2), scr + 3072);
    }
    };
    auto kvjob = [&](int j) {
        const unsigned sa = (j & 2) ? ((j & 1) ? sk1[2] : sk1[0]) : ((j & 1) ? sk0[2] : sk0[0]);
        const unsigned sb = (j & 2) ? ((j & 1) ? sk1[3] : sk1[1]) : ((j & 1) ? sk0[3] : sk0[1]);
        const float h0 = bflo(sa), h1 = bfhi(sa), h2 = bflo(sb), h3 = bfhi(sb);
        float kk[11], o[8];
#pragma unroll
        for (int i = 0; i < 11; ++i) { const unsigned wv = kw[i][j >> 1]; kk[i] = (j & 1) ? bfhi(wv) : bflo(wv); if (i < 3) kk[i] = s0 == 0 ? 0.f : kk[i]; }
#pragma unroll
        for (int i = 0; i < 8; i += 2) {
            f32x2 a = rbf2((f32x2){kk[i], kk[i + 1]} * h3);
            a = rbf2(a + rbf2((f32x2){kk[i + 1], kk[i + 2]} * h2));
            a = rbf2(a + rbf2((f32x2){kk[i + 2], kk[i + 3]} * h1));
            a = rbf2(a + rbf2((f32x2){kk[i + 3], kk[i + 4]} * h0));
            const unsigned v0 = vw[i][j >> 1], v1 = vw[i + 1][j >> 1];
            o[i] = a.x * ((j & 1) ? bfhi(v0) : bflo(v0)); o[i + 1] = a.y * ((j & 1) ? bfhi(v1) : bflo(v1));
        }
        const u32x4 r = {pk2(o[7], o[6]), pk2(o[5], o[4]), pk2(o[3], o[2]), pk2(o[1], o[0])};
        if (lt < 144) st16<K1_AUXS>(rs, (unsigned)WS_KVT + ((d0 + j) * S + s0) * 2, r);
    };
#pragma unroll
    for (int t = 0; t < 9; ++t) {
        if (t == K1_SIDET + 1 || t == K1_SIDET + 2) WAIT_V(2 * PPW + 24); else if (t < 7) WAIT_V(2 * PPW); else if (t == 7) WAIT_V(PPW); else WAIT_V(0);
        if (t == K1_SIDET + 3) {
            asm volatile("" : "+v"(kw[0]), "+v"(kw[1]), "+v"(kw[2]), "+v"(kw[3]), "+v"(kw[4]), "+v"(kw[5]), "+v"(kw[6]), "+v"(kw[7]), "+v"(kw[8]), "+v"(kw[9]), "+v"(kw[10]),
                         "+v"(vw[0]), "+v"(vw[1]), "+v"(vw[2]), "+v"(vw[3]), "+v"(vw[4]), "+v"(vw[5]), "+v"(vw[6]), "+v"(vw[7]),
                         "+v"(sk0), "+v"(sk1), "+v"(xv[0]), "+v"(xv[1]), "+v"(xv[2]) :: "memory");
        }
        BAR();
        if (t + 3 < 9) stage(t + 3);
        SCHED0();
        if (t == K1_SIDET) sideloads();
        if (t >= K1_SIDET + 3 && t < K1_SIDET + 7) kvjob(t - K1_SIDET - 3);
        SCHED0();
    }
    if (item < 648) {
        const int g = lane >> 4, q4 = (lane >> 2) & 3, p4 = lane & 3, sig = (2 * g + (q4 >> 1)) & 3;
#pragma unroll
        for (int n = 0; n < 4; ++n) {
            LAS char* p = scr + (4 * g + q4) * 128 + ((n ^ sig) << 5) + p4 * 8;
            st16<K1_AUXS>(rs, (unsigned)WS_WT + ((kap * 36 + db * 4 + n) * 64 + lane) * 16, __builtin_bit_cast(u32x4, cat8(trread(p), trread(p + 2048))));
        }
    }
    if (lt == 0) *(unsigned*)(ws + WS_FZ + pj * 256 + pi * 4) = 0u;
    if (lt < 3 && b * 3 + lt < 576) *(unsigned*)(ws + WS_FY + (b * 3 + lt) * 4) = 0u;
    BAR();
#pragma unroll
    for (int i = 0; i < 3; ++i) {
        const int c = lt + 256 * i;
        if (c < 576) {
            const int rr = c / 72, cc = c - rr * 72, row = pj * 8 + rr;
            const f32x2 st = *(LAS f32x2*)(lds + K1_ROWST + row * 8);
            u32x4 o;
#pragma unroll
            for (int j = 0; j < 4; ++j) o[j] = pk2v(rbf2(up2(xv[i][j]) - st.x) * st.y);
            st16<K1_AUXS>(rs, (unsigned)WS_XN + ((pi * 9 + (cc >> 3)) * 128 + row) * 128 + (((cc & 7) ^ ((row >> 1) & 7)) << 4), o);
        }
    }
}

__global__ __launch_bounds__(512, 2) void k1_z2(const u16* __restrict__ x, const u16* __restrict__ w_in, const u16* __restrict__ kin, const u16* __restrict__ vin,
                                                const u16* __restrict__ shk, const u16* __restrict__ w_out, char* __restrict__ ws) {
    extern __shared__ __attribute__((aligned(16))) char smem_[];
    LAS char* lds = (LAS char*)smem_;
    const int b = blockIdx.x, xcd = b & 7, r = b >> 3;
    const int pi = (xcd & 1) * 8 + (r & 7), pj = (xcd >> 1) * 4 + (r >> 3);
    const int tid = threadIdx.x, wid = __builtin_amdgcn_readfirstlane(tid >> 6), lane = tid & 63;
    if (wid >= 4) {
        const int rot = K1_ROT ? (pi + pj) % 9 : 0;
        if (wid < 6) k1_loader<5>(x, w_in, kin, vin, shk, w_out, ws, lds, b, pi, pj, rot, wid - 4, lane);
        else k1_loader<4>(x, w_in, kin, vin, shk, w_out, ws, lds, b, pi, pj, rot, wid - 4, lane);
        return;
    }
    if (wid == 0) STAMP(0, 0);
    const int fr = lane & 15, g = lane >> 4, q4 = (lane >> 2) & 3, p4 = lane & 3;
    const int wm = wid & 1, wn = wid >> 1, nb = wn * 5;
    if (lane == 0) *(LAS unsigned*)(lds + K1_FLG + wid * 4) = 0u;
    const int swz = (g ^ ((fr >> 1) & 7)) << 4;
    const int a01 = (64 * wm + 32 * wn + fr) * 128, a23 = (64 * wm + 32 * (1 - wn) + fr) * 128;
    const int aX01 = a01 + (swz ^ (wn * 64)), aX23 = a23 + (swz ^ (wn * 64)), aY01 = a01 + (swz ^ (64 - wn * 64)), aY23 = a23 + (swz ^ (64 - wn * 64));
    const int bb = SL_A + (q4 + 4 * (g & 1) + 16 * (g >> 1)) * 288 + p4 * 8;
    const int bX01 = bb + (nb + 2 * wm) * 32 + wn * 9216, bX23 = bb + (nb + 2 - 2 * wm) * 32 + wn * 9216, bX4 = bb + 128 + wn * 9216;
    const int bY01 = bb + (nb + 2 * wm) * 32 + (1 - wn) * 9216, bY23 = bb + (nb + 2 - 2 * wm) * 32 + (1 - wn) * 9216;
    f32x4 acc[4][5], rs[2], rq[2], cs[3];
    const f32x4 zero4 = {0.f, 0.f, 0.f, 0.f};
#pragma unroll
    for (int m = 0; m < 4; ++m)
#pragma unroll
        for (int n = 0; n < 5; ++n) acc[m][n] = zero4;
    rs[0] = rs[1] = rq[0] = rq[1] = cs[0] = cs[1] = cs[2] = zero4;
    const bf16x8 ones = {0x3f80, 0x3f80, 0x3f80, 0x3f80, 0x3f80, 0x3f80, 0x3f80, 0x3f80};
    bf16x8 ax[4], bx[5], ay[4], by[4];
#define TRF(p) cat8(trread(p), trread((p) + 2304))
#define K1_READX(sb) do { ax[0] = *(LAS bf16x8*)((sb) + aX01); bx[0] = TRF((sb) + bX01); ax[1] = *(LAS bf16x8*)((sb) + aX01 + 2048); ax[2] = *(LAS bf16x8*)((sb) + aX23); ax[3] = *(LAS bf16x8*)((sb) + aX23 + 2048); \
        bx[1] = TRF((sb) + bX01 + 32); bx[2] = TRF((sb) + bX23); bx[3] = TRF((sb) + bX23 + 32); bx[4] = TRF((sb) + bX4); } while (0)
#define K1_READY(sb) do { ay[0] = *(LAS bf16x8*)((sb) + aY01); by[0] = TRF((sb) + bY01); ay[1] = *(LAS bf16x8*)((sb) + aY01 + 2048); ay[2] = *(LAS bf16x8*)((sb) + aY23); ay[3] = *(LAS bf16x8*)((sb) + aY23 + 2048); \
        by[1] = TRF((sb) + bY01 + 32); by[2] = TRF((sb) + bY23); by[3] = TRF((sb) + bY23 + 32); } while (0)
#define K1_STATS(A, B) do { rs[0] = mfma16(ones, A[0], rs[0]); rq[0] = mfma16(A[0], A[0], rq[0]); rs[1] = mfma16(ones, A[1], rs[1]); rq[1] = mfma16(A[1], A[1], rq[1]); \
        cs[0] = mfma16(ones, B[0], cs[0]); cs[1] = mfma16(ones, B[1], cs[1]); } while (0)
#define K1_MMAX() do { _Pragma("unroll") for (int l = 0; l < 5; ++l) _Pragma("unroll") for (int m = 0; m < 4; ++m) acc[m][l] = mfma16(ax[m], bx[l], acc[m][l]); \
        K1_STATS(ax, bx); cs[2] = mfma16(ones, bx[4], cs[2]); } while (0)
#define K1_MMAY() do { _Pragma("unroll") for (int l = 0; l < 4; ++l) _Pragma("unroll") for (int m = 0; m < 4; ++m) acc[m][l] = mfma16(ay[m], by[l], acc[m][l]); \
        K1_STATS(ay, by); } while (0)
    WAIT_L0();
    BAR();
    if (wid == 0) { STAMP(0, 1); STAMPC(0, 14); }
    K1_READX(lds);
#pragma unroll 1
    for (int t = 0; t < 8; ++t) {
        LAS char* sb = lds + (t & 3) * SLOT;
        LAS char* sn = lds + ((t + 1) & 3) * SLOT;
        K1_READY(sb);
        K1_MMAX();
        SCHED_IL(12, 27);
        WAIT_L0();
        BAR();
        K1_READX(sn);
        K1_MMAY();
        SCHED_IL(14, 22);
    }
    K1_READY(lds);
    K1_MMAX();
    SCHED_IL(12, 27);
    K1_MMAY();
    SCHED0();
    if (wid == 0) { STAMP(0, 2); STAMPC(0, 15); }
    if ((fr >> 2) == g) {
#pragma unroll
        for (int i = 0; i < 2; ++i) {
            const float sq = (fr & 2) ? ((fr & 1) ? rq[i][3] : rq[i][2]) : ((fr & 1) ? rq[i][1] : rq[i][0]);
            const float mean = rs[i][0] * (1.0f / D);
            const float var = fmaxf(sq * (1.0f / D) - mean * mean, 0.f);
            const float den = rbf(__builtin_sqrtf(rbf(rbf(var) + 1e-5f)));
            *(LAS f32x2*)(lds + K1_ROWST + (64 * wm + 32 * wn + 8 * g + 4 * i + (fr & 3)) * 8) = (f32x2){rbf(mean), 1.0f / den};
        }
    }
    if (g == 0) {
        LAS float* c = (LAS float*)(lds + K1_CS);
        c[(nb + 2 * wm) * 16 + fr] = cs[0][0];
        c[(nb + 2 * wm + 1) * 16 + fr] = cs[1][0];
        if (wm == 0) c[(wn ? 9 : 4) * 16 + fr] = cs[2][0];
    }
    LAS char* xb = lds + SLOT + wm * 4096 + lane * 16;
    *(LAS f32x4*)(xb + wn * 2048) = acc[2][4];
    *(LAS f32x4*)(xb + wn * 2048 + 1024) = acc[3][4];
    WAIT_L0();
    BAR();
    acc[0][4] += *(LAS f32x4*)(xb + (1 - wn) * 2048);
    acc[1][4] += *(LAS f32x4*)(xb + (1 - wn) * 2048 + 1024);
    float cc[5];
    {
        LAS float* c = (LAS float*)(lds + K1_CS) + fr;
        cc[0] = c[(nb + 2 * wm) * 16]; cc[1] = c[(nb + 2 * wm + 1) * 16]; cc[2] = c[(nb + 2 - 2 * wm) * 16]; cc[3] = c[(nb + 3 - 2 * wm) * 16]; cc[4] = c[4 * 16] + c[9 * 16];
    }
    if (wid == 0) STAMP(0, 4);
    const __amdgpu_buffer_rsrc_t rsw = ws_rsrc(ws);
    const unsigned dst = (unsigned)WS_Z2R + ((pj * 32 + pi * 2 + wm) * 18) * 1024 + lane * 16;
    f32x2 keep = {0.f, 0.f};
#pragma unroll
    for (int ip = 0; ip < 2; ++ip) {
        const int mp = ip ^ wn;
        f32x2 mb[4], rd[4];
#pragma unroll
        for (int j = 0; j < 4; ++j) {
            const f32x4 s4 = *(LAS f32x4*)(lds + K1_ROWST + (64 * wm + 32 * mp + 8 * g + 2 * j) * 8);
            mb[j] = (f32x2){s4[0], s4[2]}; rd[j] = (f32x2){s4[1], s4[3]};
        }
        f32x2 sum[4], sq[4];
#pragma unroll
        for (int j = 0; j < 4; ++j) { sum[j] = (f32x2){0.f, 0.f}; sq[j] = sum[j]; }
#pragma unroll
        for (int l = 0; l < (ip == 0 ? 5 : 4); ++l) {
            u32x4 o;
#pragma unroll
            for (int j = 0; j < 4; ++j) {
                const f32x4 a = acc[2 * ip + (j >> 1)][l];
                const f32x2 a2 = (j & 1) ? (f32x2){a[2], a[3]} : (f32x2){a[0], a[1]};
                const f32x2 z = gelu2(__builtin_elementwise_fma(mb[j], (f32x2){-cc[l], -cc[l]}, a2) * rd[j]);
                o[j] = pk2v(z);
                sum[j] += z; sq[j] = __builtin_elementwise_fma(z, z, sq[j]);
            }
            const int n = l == 4 ? 4 : nb + ((l + 2 * wm) & 3);
            st16<K1_AUX>(rsw, dst + n * 2048 + mp * 1024, o);
        }
#pragma unroll
        for (int j = 0; j < 4; ++j) {
            const float s0 = rowsum16(sum[j].x), s1 = rowsum16(sum[j].y), q0 = rowsum16(sq[j].x), q1 = rowsum16(sq[j].y);
            if (fr == 8 * ip + 2 * j) keep = (f32x2){s0, q0};
            if (fr == 8 * ip + 2 * j + 1) keep = (f32x2){s1, q1};
        }
    }
    const int prow = 64 * wm + 32 * ((fr >> 3) ^ wn) + 8 * g + (fr & 7);
    if (wn) {
        *(LAS f32x2*)(lds + K1_PST + prow * 8) = keep;
        WAIT_L0();
        if (lane == 0) *(volatile LAS unsigned*)(lds + K1_FLG + wm * 4) = 1u;
    } else {
        while (*(volatile LAS unsigned*)(lds + K1_FLG + wm * 4) == 0u) __builtin_amdgcn_s_sleep(1);
        asm volatile("" ::: "memory");
        keep += *(LAS f32x2*)(lds + K1_PST + prow * 8);
        *(f32x2*)(ws + WS_ST2 + ((size_t)(pi * 128 + prow) * 16 + pj) * 8) = keep;
    }
    if (wid == 0) { STAMP(0, 5); WAIT_V(0); STAMP(0, 3); }
}

constexpr int KB_NBUF = 3, KB_RING = KB_NBUF * SLOT;
constexpr int KB_STASH = KB_RING;
constexpr int KB_LDS = KB_STASH + 4 * 12288;

template <int NB>
__device__ __forceinline__ void kb_loader(const u16* __restrict__ w_in, const u16* __restrict__ w_sp, char* __restrict__ ws, LAS char* lds,
                                          int pi, int pj, int rot, int rot2, int w, int lane) {
    constexpr int PPW = 4 + NB;
    const int l3 = lane >> 3;
    unsigned offB[NB];
#pragma unroll
    for (int i = 0; i < NB; ++i) {
        const unsigned c = (w + 4 * i) * 64 + lane, rho = c / 18, cc = c - rho * 18;
        const unsigned grow = (rho & ~12u) | ((rho & 4) << 1) | ((rho & 8) >> 1);
        offB[i] = grow * (F * 2) + cc * 16;
    }
    const char* xn = ws + WS_XN + (size_t)pi * 9 * 16384 + w * 4096;
    const char* wb = (const char*)w_in + pj * 144 * 2;
    const unsigned lo = lane * 16;
    auto stageA = [&](int t, int so) {
        int tt = t + rot; if (tt >= 9) tt -= 9;
        LAS char* sa = lds + so + w * 4096;
        LAS char* sb = lds + so + SL_A + w * 1024;
        const char* xs = xn + tt * 16384;
        const char* wsrc = wb + (size_t)tt * (64 * F * 2);
#pragma unroll
        for (int i = 0; i < 4; ++i) glds16(xs + i * 1024 + lo, sa + i * 1024);
#pragma unroll
        for (int i = 0; i < NB; ++i) glds16(wsrc + offB[i], sb + i * 4096);
    };
    const unsigned ch = (lane & 7) ^ (l3 >> 1);
    const unsigned offA0 = (pi * 128 + 32 * w + l3) * (S * 2) + ch * 16, offA1 = (pi * 128 + 32 * w + 8 + l3) * (S * 2) + (ch ^ 4) * 16;
    const char* zb = ws + WS_Z2N + (size_t)pj * 32 * SL_B + w * 1024;
    auto stageB = [&](int u, int so) {
        const int kt = (u + rot2) & 31;
        LAS char* sa = lds + so + w * 4096;
        LAS char* sb = lds + so + SL_A + w * 1024;
        const char* as = (const char*)w_sp + kt * 128;
        const char* zs = zb + kt * SL_B;
        glds16(as + offA0, sa); glds16(as + offA1, sa + 1024); glds16(as + 16 * S * 2 + offA0, sa + 2048); glds16(as + 16 * S * 2 + offA1, sa + 3072);
#pragma unroll
        for (int i = 0; i < NB; ++i) glds16c(zs + i * 4096 + lo, sb + i * 4096);
    };
    stageA(0, 0); stageA(1, SLOT);
    SCHED0();
    const unsigned toff = ((pj * 32 + pi * 2 + (w >> 1)) * 18 + (w & 1)) * 1024 + lane * 16;
    u32x4 v[9], sp[4];
#pragma unroll
    for (int i = 0; i < 4; ++i) GLD4(sp[i], ws, (unsigned)WS_ST2 + (pi * 128 + w * 32 + (lane >> 1)) * 128 + (lane & 1) * 64 + i * 16);
#pragma unroll
    for (int n = 0; n < 9; ++n) GLD4(v[n], ws, (unsigned)WS_Z2R + toff + n * 2048);
    const __amdgpu_buffer_rsrc_t rs = ws_rsrc(ws);
    f32x2 mb[4], rd[4];
    int so = 2 * SLOT;
#pragma unroll
    for (int t = 0; t < 7; ++t) {
        if (t < 2) WAIT_V(PPW + 13); else if (KB_PUBT == 5 && t == 3) WAIT_V(PPW + KB_NSPLIT); else if (KB_PUBT == 5 && t == 4) WAIT_V(PPW + 9 - KB_NSPLIT); else WAIT_V(PPW);
        if (t == 2) asm volatile("" : "+v"(v[0]), "+v"(v[1]), "+v"(v[2]), "+v"(v[3]), "+v"(v[4]), "+v"(v[5]), "+v"(v[6]), "+v"(v[7]), "+v"(v[8]), "+v"(sp[0]), "+v"(sp[1]), "+v"(sp[2]), "+v"(sp[3]) :: "memory");
        BAR();
        if (t == KB_PUBT && w == 0 && lane == 0) __hip_atomic_store((GAS unsigned*)(ws + WS_FZ + pj * 256) + pi, 1u, RLX_AGENT);
        if (t == 2) {
            const f32x4 t4 = (__builtin_bit_cast(f32x4, sp[0]) + __builtin_bit_cast(f32x4, sp[1])) + (__builtin_bit_cast(f32x4, sp[2]) + __builtin_bit_cast(f32x4, sp[3]));
            float sm = t4[0] + t4[2], sq = t4[1] + t4[3];
            sm += dppf<0xb1>(sm); sq += dppf<0xb1>(sq);
            const float mean = sm * (1.0f / FH);
            const float var = fmaxf(sq * (1.0f / FH) - mean * mean, 0.f);
            const float mbv = rbf(mean), rdv = 1.0f / rbf(__builtin_sqrtf(rbf(rbf(var) + 1e-5f)));
#pragma unroll
            for (int j = 0; j < 4; ++j) {
                const int s0 = (8 * (lane >> 4) + 2 * j) * 2, s1 = s0 + 2;
                mb[j] = (f32x2){__shfl(mbv, s0), __shfl(mbv, s1)}; rd[j] = (f32x2){__shfl(rdv, s0), __shfl(rdv, s1)};
            }
        }
        if (t == 2 || t == 3) {
#pragma unroll
            for (int n = (t == 2 ? 0 : KB_NSPLIT); n < (t == 2 ? KB_NSPLIT : 9); ++n) {
                u32x4 o;
#pragma unroll
                for (int j = 0; j < 4; ++j) o[j] = pk2v(rbf2(up2(v[n][j]) - mb[j]) * rd[j]);
                st16<16>(rs, (unsigned)WS_Z2N + toff + n * 2048, o);
            }
        }
        SCHED0();
        stageA(t + 2, so);
        SCHED0();
        so += SLOT; if (so == KB_RING) so = 0;
    }
    WAIT_V(PPW);
    BAR();
    {
        GAS unsigned* fl = (GAS unsigned*)(ws + WS_FZ + pj * 256) + (lane & 15);
        unsigned spins = 0;
        if (w == 0) STAMP(3, 8);
        while (__builtin_amdgcn_ballot_w64(__hip_atomic_load(fl, RLX_AGENT) != 1u) != 0ull) { if (++spins > SPIN_CAP) break; __builtin_amdgcn_s_sleep(2); }
        if (w == 0) STAMP(3, 9);
        asm volatile("" ::: "memory");
    }
    stageB(0, so);
    if (w == 0) STAMP(3, 10);
    so += SLOT; if (so == KB_RING) so = 0;
    LAS char* st = lds + KB_STASH + w * 12288 + lane * 16;
#pragma unroll 1
    for (int u = 1; u < 32; ++u) {
        WAIT_V(PPW);
        BAR();
        stageB(u, so);
        so += SLOT; if (so == KB_RING) so = 0;
        if (u >= 4 && u < 12) {
            LAS u32x4* p = (LAS u32x4*)(st + (u - 4) * 1024);
            u32x4 v = *p;
#pragma unroll
            for (int j = 0; j < 4; ++j) v[j] = pk2v(gelu2(up2(v[j])));
            *p = v;
        } else if (u >= 12 && u < 14) {
            LAS char* p = st + (8 + u - 12) * 1024;
            const f32x4 a = *(LAS f32x4*)p + *(LAS f32x4*)(lds + KB_STASH + (w ^ 2) * 12288 + lane * 16 + (10 + u - 12) * 1024);
            *(LAS u32x2*)p = (u32x2){pk2v(gelu2(rbf2((f32x2){a[0], a[1]}))), pk2v(gelu2(rbf2((f32x2){a[2], a[3]})))};
        }
        WAIT_L0();
    }
    WAIT_V(PPW); BAR();
    WAIT_V(0); BAR();
}

__global__ __launch_bounds__(512, 2) void kb_main(const u16* __restrict__ w_in, const u16* __restrict__ w_sp, const u16* __restrict__ b_sp, char* __restrict__ ws) {
    extern __shared__ __attribute__((aligned(16))) char smem_[];
    LAS char* lds = (LAS char*)smem_;
    const int b = blockIdx.x, xcd = b & 7, r = b >> 3;
    const int pi = (xcd & 1) * 8 + (r & 7), pj = (xcd >> 1) * 4 + (r >> 3);
    const int tid = threadIdx.x, wid = __builtin_amdgcn_readfirstlane(tid >> 6), lane = tid & 63;
    if (wid >= 4) {
        const int rot = KB_ROT ? pi % 9 : 0;
        const int rot2 = KB_ROT2;
        if (wid < 6) kb_loader<5>(w_in, w_sp, ws, lds, pi, pj, rot, rot2, wid - 4, lane);
        else kb_loader<4>(w_in, w_sp, ws, lds, pi, pj, rot, rot2, wid - 4, lane);
        return;
    }
    if (wid == 0) STAMP(1, 0);
    const int fr = lane & 15, g = lane >> 4, q4 = (lane >> 2) & 3, p4 = lane & 3;
    const int wm = wid & 1, wn = wid >> 1, nb = wn * 5;
    float bias[4];
#pragma unroll
    for (int m = 0; m < 4; ++m) bias[m] = bflo((unsigned)b_sp[pi * 128 + 64 * wm + 16 * ((m + 2 * wn) & 3) + fr]);
    if (wid == 0) STAMP(1, 1);
    const int swz = (g ^ ((fr >> 1) & 7)) << 4;
    const int a01 = (64 * wm + 32 * wn + fr) * 128, a23 = (64 * wm + 32 * (1 - wn) + fr) * 128;
    const int aX01 = a01 + (swz ^ (wn * 64)), aX23 = a23 + (swz ^ (wn * 64)), aY01 = a01 + (swz ^ (64 - wn * 64)), aY23 = a23 + (swz ^ (64 - wn * 64));
    const int bb = SL_A + (q4 + 4 * (g & 1) + 16 * (g >> 1)) * 288 + p4 * 8;
    const int tX = bb + nb * 32 + wn * 9216, tX4 = bb + 128 + wn * 9216, tY = bb + nb * 32 + (1 - wn) * 9216;
    const int fX = SL_A + lane * 16 + nb * 2048 + wn * 1024, fX4 = SL_A + lane * 16 + 4 * 2048 + wn * 1024, fY = SL_A + lane * 16 + nb * 2048 + (1 - wn) * 1024;
    f32x4 acc[4][5];
    const f32x4 zero4 = {0.f, 0.f, 0.f, 0.f};
#pragma unroll
    for (int m = 0; m < 4; ++m)
#pragma unroll
        for (int n = 0; n < 5; ++n) acc[m][n] = zero4;
    bf16x8 ax[4], bx[5], ay[4], by[4];
#define RD(p) (*(LAS bf16x8*)(p))
#define KB_READXT(sb) do { bx[0] = TRF((sb) + tX); ax[0] = RD((sb) + aX01); ax[1] = RD((sb) + aX01 + 2048); ax[2] = RD((sb) + aX23); ax[3] = RD((sb) + aX23 + 2048); \
        bx[1] = TRF((sb) + tX + 32); bx[2] = TRF((sb) + tX + 64); bx[3] = TRF((sb) + tX + 96); bx[4] = TRF((sb) + tX4); } while (0)
#define KB_READYT(sb) do { by[0] = TRF((sb) + tY); ay[0] = RD((sb) + aY01); ay[1] = RD((sb) + aY01 + 2048); ay[2] = RD((sb) + aY23); ay[3] = RD((sb) + aY23 + 2048); \
        by[1] = TRF((sb) + tY + 32); by[2] = TRF((sb) + tY + 64); by[3] = TRF((sb) + tY + 96); } while (0)
#define KB_READXF(sb) do { bx[0] = RD((sb) + fX); ax[0] = RD((sb) + aX01); ax[1] = RD((sb) + aX01 + 2048); ax[2] = RD((sb) + aX23); ax[3] = RD((sb) + aX23 + 2048); \
        bx[1] = RD((sb) + fX + 2048); bx[2] = RD((sb) + fX + 4096); bx[3] = RD((sb) + fX + 6144); bx[4] = RD((sb) + fX4); } while (0)
#define KB_READYF(sb) do { by[0] = RD((sb) + fY); ay[0] = RD((sb) + aY01); ay[1] = RD((sb) + aY01 + 2048); ay[2] = RD((sb) + aY23); ay[3] = RD((sb) + aY23 + 2048); \
        by[1] = RD((sb) + fY + 2048); by[2] = RD((sb) + fY + 4096); by[3] = RD((sb) + fY + 6144); } while (0)
#define KB_MMAX() do { _Pragma("unroll") for (int l = 0; l < 5; ++l) _Pragma("unroll") for (int m = 0; m < 4; ++m) acc[m][l] = mfma16(bx[l], ax[m], acc[m][l]); } while (0)
#define KB_MMAY() do { _Pragma("unroll") for (int l = 0; l < 4; ++l) _Pragma("unroll") for (int m = 0; m < 4; ++m) acc[m][l] = mfma16(by[l], ay[m], acc[m][l]); } while (0)
    BAR();
    if (wid == 0) { STAMP(1, 2); STAMPC(1, 14); }
    KB_READXT(lds);
    int so = 0;
#pragma unroll 1
    for (int t = 0; t < 8; ++t) {
        LAS char* sb = lds + so;
        so += SLOT; if (so == KB_RING) so = 0;
        LAS char* sn = lds + so;
        KB_READYT(sb);
        KB_MMAX();
        SCHED_IL(12, 20);
        WAIT_L0();
        BAR();
        KB_READXT(sn);
        KB_MMAY();
        SCHED_IL(14, 16);
    }
    {
        LAS char* sb = lds + so;
        so += SLOT; if (so == KB_RING) so = 0;
        LAS char* sn = lds + so;
        KB_READYT(sb);
        KB_MMAX();
        SCHED_IL(12, 20);
        if (wid == 0) STAMP(1, 3);
        WAIT_L0();
        BAR();
        if (wid == 0) STAMP(1, 4);
        KB_READXF(sn);
        KB_MMAY();
        SCHED_IL(9, 16);
        LAS char* st = lds + KB_STASH + wid * 12288 + lane * 16;
#pragma unroll
        for (int m = 0; m < 4; ++m) {
#pragma unroll
            for (int lp = 0; lp < 2; ++lp) {
                const f32x4 a = acc[m][2 * lp], c = acc[m][2 * lp + 1];
                *(LAS u32x4*)(st + (m * 2 + lp) * 1024) = (u32x4){pk2(a[0], a[1]), pk2(a[2], a[3]), pk2(c[0], c[1]), pk2(c[2], c[3])};
            }
            *(LAS f32x4*)(st + (8 + m) * 1024) = acc[m][4];
#pragma unroll
            for (int n = 0; n < 5; ++n) acc[m][n] = zero4;
        }
        SCHED0();
    }
#pragma unroll 1
    for (int u = 0; u < 31; ++u) {
        LAS char* sb = lds + so;
        so += SLOT; if (so == KB_RING) so = 0;
        LAS char* sn = lds + so;
        KB_READYF(sb);
        KB_MMAX();
        SCHED_IL(8, 20);
        WAIT_L0();
        BAR();
        KB_READXF(sn);
        KB_MMAY();
        SCHED_IL(9, 16);
    }
    {
        LAS char* sb = lds + so;
        KB_READYF(sb);
        KB_MMAX();
        SCHED_IL(8, 20);
        LAS char* xb = lds + (so == 0 ? SLOT : 0) + wm * 4096 + lane * 16;
        *(LAS f32x4*)(xb + wn * 2048) = acc[2][4];
        *(LAS f32x4*)(xb + wn * 2048 + 1024) = acc[3][4];
        KB_MMAY();
        SCHED0();
        if (wid == 0) { STAMP(1, 9); STAMPC(1, 15); }
        WAIT_L0();
        BAR();
        acc[0][4] += *(LAS f32x4*)(xb + (1 - wn) * 2048);
        acc[1][4] += *(LAS f32x4*)(xb + (1 - wn) * 2048 + 1024);
    }
    LAS char* st = lds + KB_STASH + wid * 12288 + lane * 16;
    const __amdgpu_buffer_rsrc_t rsw = ws_rsrc(ws);
    const int kap0 = 9 * (pj >> 1) + 4 * (pj & 1) + 2 * wn, kap4 = 9 * (pj >> 1) + 8;
    auto gate = [](unsigned z, float a0, float a1, float bs) { return pk2v(up2(z) * rbf2(rbf2((f32x2){a0, a1}) + bs)); };
#pragma unroll
    for (int m = 0; m < 4; ++m) {
        const int tf = pi * 8 + 4 * wm + ((m + 2 * wn) & 3);
        const unsigned dst = (unsigned)WS_P + tf * 72 * 1024 + lane * 16;
#pragma unroll
        for (int lp = 0; lp < 2; ++lp) {
            const u32x4 z = *(LAS u32x4*)(st + (m * 2 + lp) * 1024);
            const f32x4 a = acc[m][2 * lp], c = acc[m][2 * lp + 1];
            st16<KB_AUX>(rsw, dst + (kap0 + lp) * 1024, (u32x4){gate(z[0], a[0], a[1], bias[m]), gate(z[1], a[2], a[3], bias[m]), gate(z[2], c[0], c[1], bias[m]), gate(z[3], c[2], c[3], bias[m])});
        }
        if (m < 2) {
            const u32x2 z = *(LAS u32x2*)(st + (8 + m) * 1024);
            const f32x4 a = acc[m][4];
            st8<KB_AUX>(rsw, dst + kap4 * 1024 + (pj & 1) * 8, (u32x2){gate(z[0], a[0], a[1], bias[m]), gate(z[1], a[2], a[3], bias[m])});
        }
    }
    if (wid == 0) { WAIT_V(0); STAMP(1, 10); }
}

constexpr int CV_U = 512 + 4096;
constexpr int CV_HSTRIDE = 4160;
constexpr int CV_WAVE = CV_U + 4 * CV_HSTRIDE;
constexpr int K3_NBLK = 26 * 9;
constexpr int K3_RS = 272, K3_Y = 90112, K3_LDS = K3_Y + 64 * 176;

__device__ __forceinline__ void conv_wave(const u16* __restrict__ hk, const u16* __restrict__ dv, char* __restrict__ ws, LAS char* U, int d, int lane) {
    const int fr = lane & 15, g = lane >> 4;
    LAS char* H = U + CV_U;
    __builtin_amdgcn_s_setprio(2);
    {
        const char* usrc = ws + WS_KVT + (size_t)d * S * 2 + lane * 16;
        const char* hsrc = (const char*)hk + (size_t)d * S * 2 + lane * 16;
        u32x4 uu[4], hh[4]; u32x2 hn[4];
#pragma unroll
        for (int i = 0; i < 4; ++i) { uu[i] = *(const u32x4*)(usrc + i * 1024); hh[i] = *(const u32x4*)(hsrc + i * 1024); }
#pragma unroll
        for (int i = 0; i < 4; ++i) { bool last = (i == 3 && lane == 63); hn[i] = last ? (u32x2){0u, 0u} : *(const u32x2*)(hsrc + i * 1024 + 16); }
        if (lane < 32) *(LAS u32x4*)(U + lane * 16) = (u32x4){0u, 0u, 0u, 0u};
        const unsigned h0 = __builtin_amdgcn_readfirstlane(hh[0][0]), h1 = __builtin_amdgcn_readfirstlane(hh[0][1]);
        if (lane < 8) {
            const int c = lane >> 1, half = lane & 1;
            u32x4 z = {0u, 0u, 0u, 0u};
            if (half) {
                if (c == 1) z[3] = h0 << 16;
                if (c == 2) z[3] = h0;
                if (c == 3) { z[2] = h0 << 16; z[3] = __builtin_amdgcn_alignbit(h1, h0, 16); }
            }
            *(LAS u32x4*)(H + c * CV_HSTRIDE + half * 16) = z;
        }
#pragma unroll
        for (int i = 0; i < 4; ++i) {
            *(LAS u32x4*)(U + 512 + i * 1024 + lane * 16) = uu[i];
            const unsigned w0 = hh[i][0], w1 = hh[i][1], w2 = hh[i][2], w3 = hh[i][3], w4 = hn[i][0], w5 = hn[i][1];
            const unsigned a01 = __builtin_amdgcn_alignbit(w1, w0, 16), a12 = __builtin_amdgcn_alignbit(w2, w1, 16), a23 = __builtin_amdgcn_alignbit(w3, w2, 16),
                           a34 = __builtin_amdgcn_alignbit(w4, w3, 16), a45 = __builtin_amdgcn_alignbit(w5, w4, 16);
            LAS char* p = H + 32 + i * 1024 + lane * 16;
            *(LAS u32x4*)(p) = hh[i];
            *(LAS u32x4*)(p + CV_HSTRIDE) = (u32x4){a01, a12, a23, a34};
            *(LAS u32x4*)(p + 2 * CV_HSTRIDE) = (u32x4){w1, w2, w3, w4};
            *(LAS u32x4*)(p + 3 * CV_HSTRIDE) = (u32x4){a12, a23, a34, a45};
        }
    }
    WAIT_L0();
    SCHED0();
    if ((d & 3) == 0) STAMP(4, 4);
    const int e0 = 16 * (g >> 1) - 8 * (g & 1) - 7 + fr + 16;
    const int ac = e0 & 3;
    LAS char* ap = H + ac * CV_HSTRIDE + (e0 - ac) * 2;
    LAS char* bp = U + 512 + 32 * (fr - (g >> 1)) + 16 * (g & 1);
    f32x4 acc[8];
#pragma unroll
    for (int n = 0; n < 8; ++n) acc[n] = (f32x4){0.f, 0.f, 0.f, 0.f};
#pragma unroll 1
    for (int bb = 0; bb < 8; ++bb) {
        bf16x8 A[8], B[8];
#pragma unroll
        for (int a = 0; a < 8; ++a) {
            LAS char* p = ap + (8 * a + bb) * 64;
            A[a] = cat8(*(LAS bf16x4*)p, *(LAS bf16x4*)(p + 8));
            B[a] = *(LAS bf16x8*)(bp + a * 512 - bb * 64);
        }
#pragma unroll
        for (int a = 0; a < 8; ++a)
#pragma unroll
            for (int c = 0; c + a < 8; ++c) acc[a + c] = mfma16(A[a], B[c], acc[a + c]);
    }
    if ((d & 3) == 0) STAMP(4, 5);
    const float dd = bflo((unsigned)dv[d]);
    GAS unsigned long long* dst = (GAS unsigned long long*)(ws + WS_YT + (size_t)d * S * 2 + (16 * fr + 4 * g) * 2);
#pragma unroll
    for (int nu = 0; nu < 8; ++nu) {
        const u32x2 uw = *(LAS u32x2*)(U + 512 + (256 * nu + 16 * fr + 8 * (g >> 1)) * 2 + ((g & 1) ? 0 : 8));
        const f32x2 y01 = rbf2((f32x2){acc[nu][0], acc[nu][1]}) + rbf2((f32x2){bfhi(uw[1]), bflo(uw[1])} * dd);
        const f32x2 y23 = rbf2((f32x2){acc[nu][2], acc[nu][3]}) + rbf2((f32x2){bfhi(uw[0]), bflo(uw[0])} * dd);
        __hip_atomic_store(dst + nu * 64, ((unsigned long long)pk2v(y23) << 32) | pk2v(y01), RLX_AGENT);
    }
    if ((d & 3) == 0) STAMP(4, 6);
    WAIT_V(0);
    if ((d & 3) == 0) STAMP(4, 7);
    if (lane == 0) __hip_atomic_store((GAS unsigned*)(ws + WS_FY) + d, 1u, RLX_AGENT);
}

__global__ __launch_bounds__(768, 3) void k3_out(const u16* __restrict__ x, const u16* __restrict__ hk, const u16* __restrict__ dv, char* __restrict__ ws, u16* __restrict__ out) {
    extern __shared__ __attribute__((aligned(16))) char smem_[];
    LAS char* lds = (LAS char*)smem_;
    int L; { const int q = K3_NBLK / 8, rr = K3_NBLK % 8, xc = blockIdx.x & 7, off = blockIdx.x >> 3; L = (xc < rr ? xc * (q + 1) : rr * (q + 1) + (xc - rr) * q) + off; }
    const int rt = L / 9, ctile = L - rt * 9;
    const int tid = (int)threadIdx.x - 256, wid = __builtin_amdgcn_readfirstlane(tid >> 6), lane = tid & 63;
    if (wid < 0) {
        if (blockIdx.x < 144) conv_wave(hk, dv, ws, lds + (wid + 4) * CV_WAVE, blockIdx.x * 4 + wid + 4, lane);
        if (wid == -4) STAMP(2, 5);
        return;
    }
    if (wid == 0) STAMP(2, 0);
    const int fr = lane & 15, g = lane >> 4;
    const char* srcA = ws + WS_P + (size_t)(wid * 9) * 1024 + lane * 16;
    int tfo[5];
#pragma unroll
    for (int m = 0; m < 5; ++m) { int tf = rt * 5 + m; if (tf > 127) tf = 127; tfo[m] = tf * 72 * 1024; }
    const char* srcB = ws + WS_WT + ((size_t)(wid * 9) * 36 + ctile * 4) * 1024 + lane * 16;
    f32x4 acc[5][4];
#pragma unroll
    for (int m = 0; m < 5; ++m)
#pragma unroll
        for (int n = 0; n < 4; ++n) acc[m][n] = (f32x4){0.f, 0.f, 0.f, 0.f};
    bf16x8 a[2][5], bq[2][4];
#define K3_LOAD(s, i) do { _Pragma("unroll") for (int n = 0; n < 4; ++n) bq[s][n] = *(const bf16x8*)(srcB + (i) * (36 * 1024) + n * 1024); \
        _Pragma("unroll") for (int m = 0; m < 5; ++m) a[s][m] = *(const bf16x8*)(srcA + tfo[m] + (i) * 1024); } while (0)
    K3_LOAD(0, 0);
    K3_LOAD(1, 1);
    SCHED0();
#pragma unroll
    for (int i = 0; i < 9; ++i) {
#pragma unroll
        for (int m = 0; m < 5; ++m)
#pragma unroll
            for (int n = 0; n < 4; ++n) acc[m][n] = mfma16(bq[i & 1][n], a[i & 1][m], acc[m][n]);
        SCHED0();
        if (i + 2 < 9) K3_LOAD(i & 1, i + 2);
        SCHED0();
    }
    if (wid == 0) STAMP(2, 1);
    BAR();
    if (wid == 0) STAMP(2, 2);
    if (wid >= 4) {
#pragma unroll
        for (int m = 0; m < 5; ++m)
#pragma unroll
            for (int n = 0; n < 4; ++n) *(LAS f32x4*)(lds + (wid - 4) * 20480 + (m * 4 + n) * 1024 + lane * 16) = acc[m][n];
    }
    WAIT_L0();
    BAR();
    if (wid < 4) {
#pragma unroll
        for (int m = 0; m < 5; ++m)
#pragma unroll
            for (int n = 0; n < 4; ++n) acc[m][n] += *(LAS f32x4*)(lds + wid * 20480 + (m * 4 + n) * 1024 + lane * 16);
    } else {
        GAS unsigned* fl = (GAS unsigned*)(ws + WS_FY) + ctile * 64 + lane;
        unsigned spins = 0;
        if (wid == 4) STAMP(4, 0);
        while (__builtin_amdgcn_ballot_w64(__hip_atomic_load(fl, RLX_AGENT) != 1u) != 0ull) { if (++spins > SPIN_CAP) break; __builtin_amdgcn_s_sleep(2); }
        if (wid == 4) STAMP(4, 1);
        asm volatile("" ::: "memory");
        const __amdgpu_buffer_rsrc_t rs = ws_rsrc(ws);
        u32x4 yv[3];
#pragma unroll
        for (int i = 0; i < 3; ++i) {
            int c = tid - 256 + 256 * i; if (c > 639) c = 639;
            const int dd = c / 10, tc = c - dd * 10;
            int t0 = rt * 80 + tc * 8; if (t0 > S - 8) t0 = S - 8;
            yv[i] = ld16<16>(rs, (unsigned)WS_YT + ((ctile * 64 + dd) * S + t0) * 2);
        }
#pragma unroll
        for (int i = 0; i < 3; ++i) {
            int c = tid - 256 + 256 * i; if (c > 639) c = 639;
            const int dd = c / 10, tc = c - dd * 10;
            *(LAS u32x4*)(lds + K3_Y + dd * 176 + tc * 16) = yv[i];
        }
        if (wid == 4) STAMP(4, 2);
    }
    WAIT_L0();
    BAR();
    if (wid < 4) {
#pragma unroll
        for (int m = 0; m < 5; ++m)
#pragma unroll
            for (int n = 0; n < 4; ++n) *(LAS f32x4*)(lds + wid * (80 * K3_RS) + (m * 16 + fr) * K3_RS + (n * 16 + 4 * g) * 4) = acc[m][n];
    }
    WAIT_L0();
    BAR();
    if (wid == 0) STAMP(2, 3);
    for (int c = tid; c < 640; c += 512) {
        const int tl = c >> 3, dc = c & 7, t = rt * 80 + tl;
        if (t < S) {
            const u32x4 xv = *(const u32x4*)(x + (size_t)t * D + ctile * 64 + dc * 8);
            const u32x4 nv = *(const u32x4*)(ws + WS_XN + ((size_t)((t >> 7) * 9 + ctile) * 128 + (t & 127)) * 128 + ((dc ^ ((t >> 1) & 7)) << 4));
            f32x4 s0 = {0.f, 0.f, 0.f, 0.f}, s1 = s0;
#pragma unroll
            for (int w = 0; w < 4; ++w) {
                s0 += *(LAS f32x4*)(lds + w * (80 * K3_RS) + tl * K3_RS + dc * 32);
                s1 += *(LAS f32x4*)(lds + w * (80 * K3_RS) + tl * K3_RS + dc * 32 + 16);
            }
            float res[8];
#pragma unroll
            for (int j = 0; j < 8; ++j) {
                const float o = rbf(j < 4 ? s0[j] : s1[j - 4]);
                const float sg = rbf(o * __builtin_amdgcn_rcpf(1.0f + __builtin_amdgcn_exp2f(-1.4426950408889634f * o)));
                const float xx = (j & 1) ? bfhi(xv[j >> 1]) : bflo(xv[j >> 1]);
                const float xn = (j & 1) ? bfhi(nv[j >> 1]) : bflo(nv[j >> 1]);
                const float y = bflo((unsigned)*(LAS u16*)(lds + K3_Y + (dc * 8 + j) * 176 + tl * 2));
                res[j] = xx + rbf(rbf(xn * y) * sg);
            }
            *(u32x4*)(out + (size_t)t * D + ctile * 64 + dc * 8) = (u32x4){pk2(res[0], res[1]), pk2(res[2], res[3]), pk2(res[4], res[5]), pk2(res[6], res[7])};
        }
    }
    if (wid == 0) { WAIT_V(0); STAMP(2, 4); }
}

extern "C" void kernel_launch(void* const* d_in, const int* in_sizes, int n_in,
                              void* d_out, int out_size, void* d_ws, size_t ws_size,
                              hipStream_t stream) {
    const u16* q = (const u16*)d_in[0];
    const u16* k = (const u16*)d_in[1];
    const u16* v = (const u16*)d_in[2];
    const u16* sk = (const u16*)d_in[3];
    const u16* h = (const u16*)d_in[4];
    const u16* Dv = (const u16*)d_in[5];
    const u16* w_in = (const u16*)d_in[7];
    const u16* w_sp = (const u16*)d_in[8];
    const u16* b_sp = (const u16*)d_in[9];
    const u16* w_out = (const u16*)d_in[10];
    char* ws = (char*)d_ws;
    (void)hipFuncSetAttribute((const void*)k1_z2, hipFuncAttributeMaxDynamicSharedMemorySize, K1_LDS);
    (void)hipFuncSetAttribute((const void*)kb_main, hipFuncAttributeMaxDynamicSharedMemorySize, KB_LDS);
    (void)hipFuncSetAttribute((const void*)k3_out, hipFuncAttributeMaxDynamicSharedMemorySize, K3_LDS);
    k1_z2<<<256, 512, K1_LDS, stream>>>(q, w_in, k, v, sk, w_out, ws);
    kb_main<<<256, 512, KB_LDS, stream>>>(w_in, w_sp, b_sp, ws);
    k3_out<<<K3_NBLK, 768, K3_LDS, stream>>>(q, h, Dv, ws, (u16*)d_out);
}
```

```cpp
#include <hip/hip_runtime.h>
#include <stdint.h>
#define CODE_PF

#define LAS __attribute__((address_space(3)))
#define GAS __attribute__((address_space(1)))
typedef unsigned short u16;
typedef unsigned long long u64;
typedef short bf16x8 __attribute__((ext_vector_type(8)));
typedef short bf16x4 __attribute__((ext_vector_type(4)));
typedef float f32x4 __attribute__((ext_vector_type(4)));
typedef float f32x2 __attribute__((ext_vector_type(2)));
typedef unsigned u32x4 __attribute__((ext_vector_type(4)));
typedef unsigned u32x2 __attribute__((ext_vector_type(2)));
typedef __bf16 bf16x2_t __attribute__((ext_vector_type(2)));

constexpr int S = 2048, D = 576, F = 4608, FH = 2304;

__device__ __forceinline__ float bflo(unsigned w) { return __uint_as_float(w << 16); }
__device__ __forceinline__ float bfhi(unsigned w) { return __uint_as_float(w & 0xffff0000u); }
__device__ __forceinline__ unsigned pk2v(f32x2 p) { return __builtin_bit_cast(unsigned, __builtin_convertvector(p, bf16x2_t)); }
__device__ __forceinline__ unsigned pk2(float lo, float hi) { return pk2v((f32x2){lo, hi}); }
__device__ __forceinline__ f32x2 up2(unsigned w) { return (f32x2){bflo(w), bfhi(w)}; }
__device__ __forceinline__ float rbf(float x) { return bflo(pk2(x, x)); }
__device__ __forceinline__ f32x2 rbf2(f32x2 x) { return up2(pk2v(x)); }
__device__ __forceinline__ f32x2 fma2(f32x2 a, f32x2 b, f32x2 c) { return __builtin_elementwise_fma(a, b, c); }
__device__ __forceinline__ f32x2 lo2(f32x4 a) { return (f32x2){a[0], a[1]}; }
__device__ __forceinline__ f32x2 hi2(f32x4 a) { return (f32x2){a[2], a[3]}; }
__device__ __forceinline__ f32x4 mfma16(bf16x8 a, bf16x8 b, f32x4 c) { return __builtin_amdgcn_mfma_f32_16x16x32_bf16(a, b, c, 0, 0, 0); }
__device__ __forceinline__ void glds16(const void* g, LAS char* l) { __builtin_amdgcn_global_load_lds((const unsigned*)g, (LAS unsigned*)l, 16, 0, 0); }
__device__ __forceinline__ void glds16c(const void* g, LAS char* l) { __builtin_amdgcn_global_load_lds((const unsigned*)g, (LAS unsigned*)l, 16, 0, 16); }
__device__ __forceinline__ void glds4(const void* g, LAS char* l) { __builtin_amdgcn_global_load_lds((const unsigned*)g, (LAS unsigned*)l, 4, 0, 0); }
__device__ __forceinline__ bf16x4 trread(LAS char* p) { return __builtin_bit_cast(bf16x4, __builtin_amdgcn_ds_read_tr16_b64_v4i16((LAS bf16x4*)p)); }
__device__ __forceinline__ bf16x8 cat8(bf16x4 a, bf16x4 b) { return __builtin_shufflevector(a, b, 0, 1, 2, 3, 4, 5, 6, 7); }
#define WAIT_V(n) asm volatile("s_waitcnt vmcnt(%0)" ::"n"(n) : "memory")
#define WAIT_VB(n) __builtin_amdgcn_s_waitcnt(((n) & 15) | (7 << 4) | (15 << 8) | (((n) >> 4) << 14))
#define WAIT_L0() asm volatile("s_waitcnt lgkmcnt(0)" ::: "memory")
#define BAR() do { asm volatile("" ::: "memory"); __builtin_amdgcn_s_barrier(); asm volatile("" ::: "memory"); } while (0)
#define SCHED0() __builtin_amdgcn_sched_barrier(0)
#define RLX_AGENT __ATOMIC_RELAXED, __HIP_MEMORY_SCOPE_AGENT
#define PIN9(z) asm volatile("" : "+v"(z[0]), "+v"(z[1]), "+v"(z[2]), "+v"(z[3]), "+v"(z[4]), "+v"(z[5]), "+v"(z[6]), "+v"(z[7]), "+v"(z[8]))
__device__ __forceinline__ int opqs(int x) { asm volatile("" : "+s"(x)); return x; }
__device__ __forceinline__ int opqv(int x) { asm volatile("" : "+v"(x)); return x; }
#define RFL(x) __builtin_amdgcn_readfirstlane(x)

#define STAMP(kid, i) do { } while (0)
#define STAMPC(kid, i) do { } while (0)

__device__ __forceinline__ f32x2 gelu2(f32x2 v) {
    const float c1 = -2.0f * 0.7978845608028654f * 1.4426950408889634f;
    const float c2 = c1 * 0.044715f;
    f32x2 a = v * fma2(v * v, (f32x2){c2, c2}, (f32x2){c1, c1});
    f32x2 d = (f32x2){__builtin_amdgcn_exp2f(a.x), __builtin_amdgcn_exp2f(a.y)} + (f32x2){1.0f, 1.0f};
    return v * (f32x2){__builtin_amdgcn_rcpf(d.x), __builtin_amdgcn_rcpf(d.y)};
}
__device__ __forceinline__ f32x2 ln_consts(float sm, float sq, float inv_n) {
    const float mean = sm * inv_n;
    const float var = fmaxf(sq * inv_n - mean * mean, 0.f);
    return (f32x2){rbf(mean), __builtin_amdgcn_rcpf(rbf(__builtin_amdgcn_sqrtf(rbf(rbf(var) + 1e-5f))))};
}

constexpr unsigned SZ_Z = S * FH * 2, SZ_X = S * D * 2;
constexpr unsigned WS_Z2N = 0;
constexpr unsigned WS_P = WS_Z2N + SZ_Z;
constexpr unsigned WS_KVT = WS_P + SZ_Z;
constexpr unsigned WS_YT = WS_KVT + SZ_X;
constexpr unsigned WS_WT = WS_YT + SZ_X;
constexpr unsigned WS_ST2 = WS_WT + FH * D * 2;
constexpr unsigned WS_ST1 = WS_ST2 + 256 * 1024;
constexpr unsigned WS_FS = WS_ST1 + 16384;
constexpr unsigned WS_FZ = WS_FS + 2048;
constexpr unsigned WS_FP = WS_FZ + 2048;
constexpr unsigned WS_FK = WS_FP + 2048;
constexpr unsigned WS_FY = WS_FK + 2048;
constexpr unsigned WS_E = WS_FY + 4608 + 128;
constexpr unsigned WS_END = WS_E + 128;
constexpr u64 TAG_INC = 0x9E3779B97F4A7C15ull;

__device__ __forceinline__ __amdgpu_buffer_rsrc_t ws_rsrc(char* ws) { return __builtin_amdgcn_make_buffer_rsrc(ws, 0, (int)WS_END, 0x00020000); }
__device__ __forceinline__ void st16c(__amdgpu_buffer_rsrc_t r, unsigned off, unsigned soff, u32x4 v) { __builtin_amdgcn_raw_buffer_store_b128(v, r, off, soff, 16); }
__device__ __forceinline__ void st8c(__amdgpu_buffer_rsrc_t r, unsigned off, unsigned soff, u32x2 v) { __builtin_amdgcn_raw_buffer_store_b64(v, r, off, soff, 16); }
__device__ __forceinline__ u32x4 ld16c(__amdgpu_buffer_rsrc_t r, unsigned off, unsigned soff) { return __builtin_amdgcn_raw_buffer_load_b128(r, off, soff, 16); }
__device__ __forceinline__ unsigned ld4c(__amdgpu_buffer_rsrc_t r, unsigned off) { return __builtin_amdgcn_raw_buffer_load_b32(r, off, 0, 16); }
__device__ __forceinline__ unsigned ld2c(__amdgpu_buffer_rsrc_t r, unsigned off, unsigned soff) { return __builtin_amdgcn_raw_buffer_load_b16(r, off, soff, 16); }
__device__ __forceinline__ u64 ldflag(const char* p) { return __hip_atomic_load((GAS u64*)p, RLX_AGENT); }
__device__ __forceinline__ void stflag(char* p, u64 v) { __hip_atomic_store((GAS u64*)p, v, RLX_AGENT); }
__device__ __forceinline__ bool allset(u64 v, u64 tag) { return __builtin_amdgcn_ballot_w64(v != tag) == 0ull; }

constexpr int SL_A = 16384, SL_B = 18432, SLOT = SL_A + SL_B;
constexpr unsigned SPIN_CAP = 1u << 20;
constexpr int L_X4 = 4 * SLOT;
constexpr int L_WSCR = L_X4 + 8192;
constexpr int L_ROWST = L_WSCR + 12288;
constexpr int L_ZST = L_ROWST + 1024;
constexpr int L_CS = L_ZST + 1024;
constexpr int L_MISC = L_CS + 768;
constexpr int L_DUMP = L_MISC + 256;
constexpr int LDS_SZ = 163840;
constexpr int M_TAG = L_MISC, M_READY = L_MISC + 8, M_HINT = L_MISC + 12, M_CNT = L_MISC + 16;
constexpr int CV_U = 512 + 4096, CV_HSTRIDE = 4160, CV_WAVE = CV_U + 4 * CV_HSTRIDE;
constexpr int RED_W = 18432, RED_C = 3 * CV_WAVE + RED_W;

#define SCHED_IL(nr, nm) do { _Pragma("unroll") for (int i_ = 0; i_ < (nr); ++i_) { __builtin_amdgcn_sched_group_barrier(0x008, 1, 0); __builtin_amdgcn_sched_group_barrier(0x100, 1, 0); } \
        __builtin_amdgcn_sched_group_barrier(0x008, (nm) - (nr), 0); SCHED0(); } while (0)

__device__ __forceinline__ void spin_flags(const char* p, u64 tag) {
    unsigned spins = 0;
    while (!allset(ldflag(p), tag)) { if (++spins > SPIN_CAP) break; __builtin_amdgcn_s_sleep(1); }
    asm volatile("" ::: "memory");
}
__device__ __forceinline__ void signal4(LAS char* lds, int cnt, char* flag, int lane, u64 tag) {
    if (lane == 0) {
        const unsigned old = __hip_atomic_fetch_add((LAS unsigned*)(lds + M_CNT + cnt * 4), 1u, __ATOMIC_RELAXED, __HIP_MEMORY_SCOPE_WORKGROUP);
        if (old == 3u) stflag(flag, tag);
    }
}

#define NDELAY 2

struct ConvIn { u32x4 uu[4], hh[4]; u32x2 hn[4]; unsigned dd; };
__device__ __forceinline__ void conv_load(ConvIn& c, const u16* __restrict__ hk, const u16* __restrict__ dv, __amdgpu_buffer_rsrc_t rs, int d, int lane) {
    const char* hsrc = (const char*)hk + (size_t)d * (S * 2);
#pragma unroll
    for (int i = 0; i < 4; ++i) { c.uu[i] = ld16c(rs, lane * 16 + i * 1024, WS_KVT + d * (S * 2)); c.hh[i] = *(const u32x4*)(hsrc + (unsigned)(lane * 16 + i * 1024)); }
#pragma unroll
    for (int i = 0; i < 4; ++i) { const unsigned o = lane * 16 + i * 1024 + 16; c.hn[i] = *(const u32x2*)(hsrc + (o > 4088u ? 4088u : o)); }
    c.dd = ((const unsigned*)dv)[d >> 1];
}
__device__ __forceinline__ void conv_run(ConvIn& c, char* __restrict__ ws, LAS char* U, int d, int lane, u64 tag) {
    const int fr = lane & 15, g = lane >> 4;
    LAS char* H = U + CV_U;
    if (lane == 63) c.hn[3] = (u32x2){0u, 0u};
    if (lane < 32) *(LAS u32x4*)(U + lane * 16) = (u32x4){0u, 0u, 0u, 0u};
    const unsigned h0 = RFL(c.hh[0][0]), h1 = RFL(c.hh[0][1]);
    if (lane < 8) {
        const int cc = lane >> 1, half = lane & 1;
        u32x4 z = {0u, 0u, 0u, 0u};
        if (half) {
            if (cc == 1) z[3] = h0 << 16;
            if (cc == 2) z[3] = h0;
            if (cc == 3) { z[2] = h0 << 16; z[3] = __builtin_amdgcn_alignbit(h1, h0, 16); }
        }
        *(LAS u32x4*)(H + cc * CV_HSTRIDE + half * 16) = z;
    }
#pragma unroll
    for (int i = 0; i < 4; ++i) {
        *(LAS u32x4*)(U + 512 + i * 1024 + lane * 16) = c.uu[i];
        const unsigned w0 = c.hh[i][0], w1 = c.hh[i][1], w2 = c.hh[i][2], w3 = c.hh[i][3], w4 = c.hn[i][0], w5 = c.hn[i][1];
        const unsigned a01 = __builtin_amdgcn_alignbit(w1, w0, 16), a12 = __builtin_amdgcn_alignbit(w2, w1, 16), a23 = __builtin_amdgcn_alignbit(w3, w2, 16),
                       a34 = __builtin_amdgcn_alignbit(w4, w3, 16), a45 = __builtin_amdgcn_alignbit(w5, w4, 16);
        LAS char* p = H + 32 + i * 1024 + lane * 16;
        *(LAS u32x4*)(p) = c.hh[i];
        *(LAS u32x4*)(p + CV_HSTRIDE) = (u32x4){a01, a12, a23, a34};
        *(LAS u32x4*)(p + 2 * CV_HSTRIDE) = (u32x4){w1, w2, w3, w4};
        *(LAS u32x4*)(p + 3 * CV_HSTRIDE) = (u32x4){a12, a23, a34, a45};
    }
    WAIT_L0();
    SCHED0();
    STAMP(3, 3);
    const int e0 = 16 * (g >> 1) - 8 * (g & 1) - 7 + fr + 16;
    const int ac = e0 & 3;
    LAS char* ap = H + ac * CV_HSTRIDE + (e0 - ac) * 2;
    LAS char* bp = U + 512 + 32 * (fr - (g >> 1)) + 16 * (g & 1);
    f32x4 acc[8];
#pragma unroll
    for (int n = 0; n < 8; ++n) acc[n] = (f32x4){0.f, 0.f, 0.f, 0.f};
#pragma unroll 1
    for (int bb = 0; bb < 8; ++bb) {
        bf16x8 A[8], B[8];
#pragma unroll
        for (int a = 0; a < 8; ++a) {
            LAS char* p = ap + (8 * a + bb) * 64;
            B[a] = *(LAS bf16x8*)(bp + a * 512 - bb * 64);
            A[a] = cat8(*(LAS bf16x4*)p, *(LAS bf16x4*)(p + 8));
        }
#pragma unroll
        for (int s = 0; s < 8; ++s) {
#pragma unroll
            for (int a = 0; a < s; ++a) if (a + s < 8) { acc[a + s] = mfma16(A[a], B[s], acc[a + s]); acc[a + s] = mfma16(A[s], B[a], acc[a + s]); }
            if (2 * s < 8) acc[2 * s] = mfma16(A[s], B[s], acc[2 * s]);
        }
    }
    STAMP(3, 4);
    const float dd = (d & 1) ? bfhi(c.dd) : bflo(c.dd);
    GAS u64* dst = (GAS u64*)(ws + WS_YT + (size_t)d * S * 2 + (16 * fr + 4 * g) * 2);
#pragma unroll
    for (int nu = 0; nu < 8; ++nu) {
        const u32x2 uw = *(LAS u32x2*)(U + 512 + (256 * nu + 16 * fr + 8 * (g >> 1)) * 2 + ((g & 1) ? 0 : 8));
        const f32x2 y01 = fma2((f32x2){bfhi(uw[1]), bflo(uw[1])}, (f32x2){dd, dd}, lo2(acc[nu]));
        const f32x2 y23 = fma2((f32x2){bfhi(uw[0]), bflo(uw[0])}, (f32x2){dd, dd}, hi2(acc[nu]));
        __hip_atomic_store(dst + nu * 64, ((u64)pk2v(y23) << 32) | pk2v(y01), RLX_AGENT);
    }
    WAIT_V(0);
    if (lane == 0) stflag(ws + WS_FY + d * 8, tag);
    STAMP(3, 5);
}

constexpr int RB = 5;
#define G3_LDB(s, i) do { _Pragma("unroll") for (int l = 0; l < 4; ++l) bq[s][l] = __builtin_bit_cast(bf16x8, ld16c(rs, lane * 16 + l * 1024, sB + (i) * (36 * 1024))); \
        bq[s][4] = __builtin_bit_cast(bf16x8, ld16c(rs, lane * 16, sB4 + (i) * (36 * 1024))); } while (0)
#define G3_LDA(s, i) do { _Pragma("unroll") for (int m = 0; m < 4; ++m) aq[s][m] = __builtin_bit_cast(bf16x8, ld16c(rs, lane * 16, sA + m * (72 * 1024) + (i) * 1024)); } while (0)
#define G3_PRE() do { _Pragma("unroll") for (int i = 0; i < RB; ++i) G3_LDB(i, i); SCHED0(); } while (0)
#define G3_MAIN(kid) do { \
        STAMP(kid, 6); \
        spin_flags(ws + WS_FP + (rg >> 1) * 128 + (2 * wid + (lane & 1)) * 8, tag); \
        STAMP(kid, 7); \
        bf16x8 aq[3][4]; \
        G3_LDA(0, 0); G3_LDA(1, 1); G3_LDA(2, 2); \
        SCHED0(); \
        _Pragma("unroll") for (int i = 0; i < 9; ++i) { \
            _Pragma("unroll") for (int m = 0; m < 4; ++m) _Pragma("unroll") for (int l = 0; l < 5; ++l) acc[m][l] = mfma16(bq[i % RB][l], aq[i % 3][m], i == 0 ? zero4 : acc[m][l]); \
            SCHED0(); \
            if (i + 3 < 9) G3_LDA(i % 3, i + 3); \
            if (i + RB < 9) G3_LDB(i % RB, i + RB); \
            SCHED0(); \
        } \
        STAMP(kid, 8); \
    } while (0)

__global__ __launch_bounds__(512, 2) void mega(const u16* __restrict__ x, const u16* __restrict__ kin, const u16* __restrict__ vin, const u16* __restrict__ shk,
                                               const u16* __restrict__ hk, const u16* __restrict__ dv, const u16* __restrict__ w_in, const u16* __restrict__ w_sp,
                                               const u16* __restrict__ b_sp, const u16* __restrict__ w_out, char* __restrict__ ws, u16* __restrict__ out) {
    extern __shared__ __attribute__((aligned(16))) char smem_[];
    LAS char* lds = (LAS char*)smem_;
    const int b = blockIdx.x, xcd = b & 7, r = b >> 3;
    const int pi = (xcd & 1) * 8 + (r & 7), pj = (xcd >> 1) * 4 + (r >> 3);
    const int tid = threadIdx.x, wid = RFL(tid >> 6), lane = tid & 63;
    const int fr = lane & 15, g = lane >> 4, q4 = (lane >> 2) & 3, p4 = lane & 3;
    const __amdgpu_buffer_rsrc_t rs = ws_rsrc(ws);
    const f32x4 zero4 = {0.f, 0.f, 0.f, 0.f};
    const int rg = (xcd & 1) * 16 + (xcd >> 1) * 4 + (r & 3), ct = r >> 2;
    const int nb3 = (9 * ct) >> 1, n30 = nb3 + (ct & 1), n34 = (ct & 1) ? nb3 : nb3 + 4;
    const unsigned sA = WS_P + (4 * rg * 72 + 9 * wid) * 1024, sB = WS_WT + (9 * wid * 36 + n30) * 1024, sB4 = WS_WT + (9 * wid * 36 + n34) * 1024;
    f32x4 acc[4][5];
    u64 tag;
#define TAG_READY() do { asm volatile("s_load_dwordx2 %0, %1, 0x0\n s_waitcnt lgkmcnt(0)" : "=s"(tag) : "s"(ws + WS_E) : "memory"); tag += TAG_INC; } while (0)

    if (wid >= 4) {
        const int w = wid - 4, l3 = lane >> 3;
        const unsigned ch = (lane & 7) ^ (l3 >> 1);
        const int rot = (pi + pj) % 9;
        {
            const unsigned srow0 = pi * 128 + 32 * w + 8 * (l3 >> 2) + (l3 & 3);
            const unsigned offA0 = srow0 * (D * 2) + ch * 16, offA1 = (srow0 + 16) * (D * 2) + (ch ^ 4) * 16;
            unsigned offB[5];
#pragma unroll
            for (int i = 0; i < 5; ++i) {
                const unsigned c = (w + 4 * i) * 64 + lane, rho = c / 18, cc = c - rho * 18;
                const unsigned grow = (rho & ~12u) | ((rho & 4) << 1) | ((rho & 8) >> 1);
                offB[i] = grow * (F * 2) + cc * 16;
            }
            const char* wb = (const char*)w_in + (FH + pj * 144) * 2;
            int tt = rot;
#pragma unroll 1
            for (int t = -3; t < 6; ++t) {
                if (t >= 0) {
                    if (w < 2) WAIT_V(18); else WAIT_V(16);
                    BAR();
                }
                LAS char* sa = lds + ((t + 3) & 3) * SLOT + w * 4096;
                LAS char* sb = lds + ((t + 3) & 3) * SLOT + SL_A + w * 1024;
                const char* xs = (const char*)x + tt * 128;
                const char* wsrc = wb + (size_t)tt * (64 * F * 2);
                glds16(xs + offA0, sa); glds16(xs + offA1, sa + 1024); glds16(xs + 4 * D * 2 + offA0, sa + 2048); glds16(xs + 4 * D * 2 + offA1, sa + 3072);
#pragma unroll
                for (int i = 0; i < 4; ++i) glds16(wsrc + offB[i], sb + i * 4096);
                if (w < 2) glds16(wsrc + offB[4], sb + 4 * 4096);
                if (++tt == 9) tt = 0;
            }
        }
        TAG_READY();
        const unsigned offE = (pi * 128 + 64 * w + l3) * (S * 2) + ch * 16, offO = (pi * 128 + 64 * w + 8 + l3) * (S * 2) + (ch ^ 4) * 16;
        auto stageA = [&](int t) {
            LAS char* sa = lds + (t & 3) * SLOT + w * 8192;
            const char* as = (const char*)w_sp + ((2 * pi + t - 18) & 31) * 128;
#pragma unroll
            for (int i = 0; i < 4; ++i) { glds16(as + i * (16 * S * 2) + offE, sa + i * 2048); glds16(as + i * (16 * S * 2) + offO, sa + i * 2048 + 1024); }
        };
        auto stageB = [&](int t) {
            LAS char* sb = lds + (t & 3) * SLOT + SL_A + (w - 2) * 9216;
            const char* zs = ws + WS_Z2N + (pj * 32 + ((2 * pi + t - 18) & 31)) * SL_B + (w - 2) * 9216;
#pragma unroll
            for (int i = 0; i < 9; ++i) glds16c(zs + i * 1024 + (unsigned)(lane * 16), sb + i * 1024);
        };
        if (w < 3) {
            const unsigned oE = (pi * 128 + 8 * w + l3) * (D * 2) + (ch ^ ((w & 1) * 4)) * 16, oO = oE ^ 64;
            const int q0 = w == 0 ? 2 : w - 1;
            unsigned oB[3];
#pragma unroll
            for (int j = 0; j < 3; ++j) {
                const unsigned c = (q0 + 3 * j) * 64 + lane, rho = c / 18, cc = c - rho * 18;
                const unsigned grow = (rho & ~12u) | ((rho & 4) << 1) | ((rho & 8) >> 1);
                oB[j] = grow * (F * 2) + cc * 16;
            }
            const char* wb = (const char*)w_in + pj * 144 * 2;
            int tt = rot;
#pragma unroll 1
            for (int t = 6; t < 15; ++t) {
                if (t == 9) BAR();
                if (t < 8) WAIT_V(16); else WAIT_V(22);
                BAR();
                LAS char* sa = lds + ((t + 3) & 3) * SLOT + w * 1024;
                LAS char* sb = lds + ((t + 3) & 3) * SLOT + SL_A + q0 * 1024;
                const char* xs = (const char*)x + tt * 128;
                const char* wsrc = wb + (size_t)tt * (64 * F * 2);
#pragma unroll
                for (int i = 0; i < 5; ++i) glds16(xs + i * (24 * D * 2) + ((i & 1) ? oO : oE), sa + i * 3072);
                if (w == 0) glds16(xs + 5 * (24 * D * 2) + oO, sa + 5 * 3072);
#pragma unroll
                for (int j = 0; j < 6; ++j) glds16(wsrc + (j / 3) * (32 * F * 2) + oB[j % 3], sb + j * 3072);
                if (++tt == 9) tt = 0;
            }
            if (w < 2) {
#pragma unroll 1
                for (int t = 15; t < 20; ++t) {
                    if (t == 18) BAR();
                    if (t < 17) WAIT_V(19); else WAIT_V(16);
                    BAR();
                    stageA(t + 3);
                }
            } else {
                WAIT_V(22); BAR();
                WAIT_V(11); BAR();
                WAIT_VB(0); BAR();
                BAR();
            }
        } else {
            WAIT_V(16); BAR();
            {
                const char* p = (const char*)w_sp + (size_t)pi * (128 * S * 2) + pj * 256;
#pragma unroll
                for (int i = 0; i < 4; ++i) glds4(p + i * (32 * S * 2) + (unsigned)((lane >> 1) * (S * 2) + (lane & 1) * 128), lds + L_DUMP);
            }
            const int kap = b / 3, th = b - kap * 3;
            if (b < 216) {
                const int q = kap / 9, e = kap - q * 9, pr = e & 3;
                const int ga = e == 8 ? 18 * q + 4 : 18 * q + (e >> 2) * 9 + 2 * pr + (pr >> 1);
                const int gb = e == 8 ? ga + 9 : ga + 1;
                unsigned o[3];
#pragma unroll
                for (int j = 0; j < 3; ++j) { const unsigned c = j * 64 + lane, rr = c / 24; o[j] = rr * (D * 2) + (c - rr * 24) * 16; }
                const char* sa = (const char*)w_out + th * 384 + ga * (16 * D * 2);
                const char* sb = (const char*)w_out + th * 384 + gb * (16 * D * 2);
#pragma unroll
                for (int i = 0; i < 6; ++i) glds16(sa + (i / 3) * (8 * D * 2) + o[i % 3], lds + L_WSCR + i * 1024);
#pragma unroll
                for (int i = 0; i < 6; ++i) glds16(sb + (i / 3) * (8 * D * 2) + o[i % 3], lds + L_WSCR + 6144 + i * 1024);
                WAIT_V(24); BAR();
                WAIT_V(16); BAR();
            } else {
                WAIT_V(12); BAR();
                WAIT_V(4); BAR();
            }
            BAR();
            if (b < 216) {
#pragma unroll
                for (int n = 0; n < 12; ++n) {
                    LAS char* p = lds + L_WSCR + (4 * g + q4) * 384 + n * 32 + p4 * 8;
                    st16c(rs, lane * 16, WS_WT + (kap * 36 + th * 12 + n) * 1024, __builtin_bit_cast(u32x4, cat8(trread(p), trread(p + 6144))));
                }
            }
            if (pj == 0) st16c(rs, lane * 16, WS_ST1 + pi * 1024, *(LAS u32x4*)(lds + L_ROWST + lane * 16));
            WAIT_L0();
            BAR();
            STAMP(2, 0);
            {
                LAS char* p = lds + L_X4 + (lane >> 5) * 4096 + (lane & 31) * 16;
                const f32x4 s4 = *(LAS f32x4*)p + *(LAS f32x4*)(p + 2048);
                st16c(rs, lane * 16, WS_ST2 + (pi * 16 + pj) * 1024, __builtin_bit_cast(u32x4, s4));
            }
            BAR();
            WAIT_V(0);
            if (lane == 0) stflag(ws + WS_FS + (pi * 16 + pj) * 8, tag);
            STAMP(2, 1);
            int bi = 11;
            unsigned spins = 0;
            for (;;) {
                const u64 v = ldflag(ws + WS_FS + pi * 128 + fr * 8);
                if (bi < 18) { BAR(); ++bi; }
                if (allset(v, tag) || ++spins > SPIN_CAP) break;
            }
            asm volatile("" ::: "memory");
            STAMP(2, 2);
            {
                u32x4 pv[16];
#pragma unroll
                for (int j = 0; j < 16; ++j) pv[j] = ld16c(rs, lane * 16, WS_ST2 + (pi * 16 + j) * 1024);
                if (bi < 18) { BAR(); ++bi; }
                f32x4 s4 = __builtin_bit_cast(f32x4, pv[0]);
#pragma unroll
                for (int j = 1; j < 16; ++j) s4 += __builtin_bit_cast(f32x4, pv[j]);
                const f32x2 c0 = ln_consts(s4[0], s4[1], 1.0f / FH), c1 = ln_consts(s4[2], s4[3], 1.0f / FH);
                *(LAS f32x4*)(lds + L_ZST + lane * 16) = (f32x4){c0.y, c1.y, -c0.x * c0.y, -c1.x * c1.y};
                WAIT_L0();
                if (lane == 0) *(volatile LAS unsigned*)(lds + M_READY) = 1u;
                WAIT_L0();
            }
            STAMP(2, 3);
            {
                const unsigned o = (unsigned)(((b >> 3) * 64 + lane) * (D * 2) + (b & 7) * 144);
                glds4((const char*)kin + o, lds + L_DUMP); glds4((const char*)kin + o + 128, lds + L_DUMP);
                glds4((const char*)vin + o, lds + L_DUMP); glds4((const char*)vin + o + 128, lds + L_DUMP);
                glds4((const char*)hk + (unsigned)(b * 9216 + lane * 128), lds + L_DUMP);
                glds4((const char*)hk + (unsigned)(b * 9216 + 8192 + (lane & 7) * 128), lds + L_DUMP);
            }
            for (; bi < 19; ++bi) BAR();
            WAIT_VB(0);
        }
        if (w >= 2) {
            const char* fz = ws + WS_FZ + pj * 128 + fr * 8;
            bool ok;
            unsigned spins = 0;
            for (;;) {
                ok = allset(ldflag(fz), tag);
                if (ok || *(volatile LAS unsigned*)(lds + M_HINT) != 0u || ++spins > SPIN_CAP) break;
            }
            asm volatile("" ::: "memory");
            int next = 20;
            if (ok) { if (w == 3) STAMP(2, 4); stageB(next++); }
            BAR();
            if (!ok) { ok = allset(ldflag(fz), tag); asm volatile("" ::: "memory"); if (ok && w == 3) STAMP(2, 4); }
            if (ok) for (; next < 22; ++next) stageB(next);
            BAR();
            if (!ok) { spin_flags(fz, tag); if (w == 3) STAMP(2, 4); }
            for (; next < 23; ++next) stageB(next);
            if (b == 0 && w == 3 && lane == 0) stflag(ws + WS_E, tag);
        }
        const int cd = w < 2 ? 2 * b + w : 512 + b;
        const bool hasconv = w < 2 || (w == 2 && b < 64);
#pragma unroll 1
        for (int t = 20; t < 45; ++t) {
            if (w < 2) WAIT_V(16); else WAIT_V(18);
            BAR();
            if (w < 2) stageA(t + 3); else stageB(t + 3);
        }
        if (w < 2) WAIT_V(16); else WAIT_V(18);
        BAR();
        const char* fk = ws + WS_FK + ((hasconv ? cd : 0) / 72) * 256 + (lane & 31) * 8;
        u64 fkv;
        asm volatile("global_load_dwordx2 %0, %1, off sc1" : "=v"(fkv) : "v"(fk) : "memory");
        if (w < 2) stageA(48); else stageB(48);
        if (w < 2) WAIT_V(16); else WAIT_V(18);
        BAR();
        if (w < 2) stageA(49); else stageB(49);
        if (w < 2) WAIT_V(16); else WAIT_V(18);
        BAR();
        asm volatile("" : "+v"(fkv));
        if (!allset(fkv, tag)) spin_flags(fk, tag);
        asm volatile("" ::: "memory");
        bf16x8 bq[RB][5];
        G3_PRE();
        ConvIn cin;
        if (hasconv) conv_load(cin, hk, dv, rs, cd, lane);
        SCHED0();
        WAIT_V(8 + 5 * RB);
        BAR();
        WAIT_VB(5 * RB);
        BAR();
        BAR();
        if (w == 0) STAMP(3, 0);
        if (hasconv) conv_run(cin, ws, lds + w * CV_WAVE, cd, lane, tag);
        SCHED0();
        G3_MAIN(3);
    } else {
        if (wid == 0) STAMP(0, 0);
        const int wm = wid & 1, wn = wid >> 1, nb = wn * 5;
        if (tid < 6) *(LAS unsigned*)(lds + M_READY + tid * 4) = 0u;
        {
            const char* pc; unsigned len;
            asm volatile("s_getpc_b64 %0\n.Lpf_here%=:\n s_mov_b32 %1, .Lmega_tail+1024-.Lpf_here%=" : "=s"(pc), "=s"(len));
            unsigned d0_, d1_;
            const unsigned o0 = tid * 128, o1 = o0 + 32768;
            asm volatile("global_load_dword %0, %1, %2" : "=v"(d0_) : "v"(o0 < len - 4 ? o0 : 0u), "s"(pc));
            asm volatile("global_load_dword %0, %1, %2" : "=v"(d1_) : "v"(o1 < len - 4 ? o1 : 0u), "s"(pc));
            asm volatile("s_waitcnt vmcnt(0)" : "+v"(d0_), "+v"(d1_));
        }
        const int swz = (g ^ ((fr >> 1) & 7)) << 4;
        const int a01 = (64 * wm + 32 * wn + fr) * 128, a23 = (64 * wm + 32 * (1 - wn) + fr) * 128;
        const int aX01 = a01 + (swz ^ (wn * 64)), aX23 = a23 + (swz ^ (wn * 64)), aY01 = a01 + (swz ^ (64 - wn * 64)), aY23 = a23 + (swz ^ (64 - wn * 64));
        const int bb = SL_A + (q4 + 4 * (g & 1) + 16 * (g >> 1)) * 288 + p4 * 8;
        const int bX01 = bb + (nb + 2 * wm) * 32 + wn * 9216, bX23 = bb + (nb + 2 - 2 * wm) * 32 + wn * 9216, bX4 = bb + 128 + wn * 9216;
        const int bY01 = bb + (nb + 2 * wm) * 32 + (1 - wn) * 9216, bY23 = bb + (nb + 2 - 2 * wm) * 32 + (1 - wn) * 9216;
        f32x4 rsm[2], rq[2], cs[3];
#define ZACC() do { _Pragma("unroll") for (int m = 0; m < 4; ++m) _Pragma("unroll") for (int n = 0; n < 5; ++n) acc[m][n] = zero4; } while (0)
        ZACC();
        rsm[0] = rsm[1] = rq[0] = rq[1] = cs[0] = cs[1] = cs[2] = zero4;
        const bf16x8 ones = {0x3f80, 0x3f80, 0x3f80, 0x3f80, 0x3f80, 0x3f80, 0x3f80, 0x3f80};
        bf16x8 ax[4], bx[5], ay[4], by[4];
#define RD(p) (*(LAS bf16x8*)(p))
#define TRF(p) cat8(trread(p), trread((p) + 2304))
#define READXT(sb) do { ax[0] = RD((sb) + aX01); bx[0] = TRF((sb) + bX01); ax[1] = RD((sb) + aX01 + 2048); ax[2] = RD((sb) + aX23); ax[3] = RD((sb) + aX23 + 2048); \
        bx[1] = TRF((sb) + bX01 + 32); bx[2] = TRF((sb) + bX23); bx[3] = TRF((sb) + bX23 + 32); bx[4] = TRF((sb) + bX4); } while (0)
#define READYT(sb) do { ay[0] = RD((sb) + aY01); by[0] = TRF((sb) + bY01); ay[1] = RD((sb) + aY01 + 2048); ay[2] = RD((sb) + aY23); ay[3] = RD((sb) + aY23 + 2048); \
        by[1] = TRF((sb) + bY01 + 32); by[2] = TRF((sb) + bY23); by[3] = TRF((sb) + bY23 + 32); } while (0)
#define Z2_STATS(A, B) do { rsm[0] = mfma16(ones, A[0], rsm[0]); rq[0] = mfma16(A[0], A[0], rq[0]); rsm[1] = mfma16(ones, A[1], rsm[1]); rq[1] = mfma16(A[1], A[1], rq[1]); \
        cs[0] = mfma16(ones, B[0], cs[0]); cs[1] = mfma16(ones, B[1], cs[1]); } while (0)
#define Z2_MMAX() do { _Pragma("unroll") for (int l = 0; l < 5; ++l) _Pragma("unroll") for (int m = 0; m < 4; ++m) acc[m][l] = mfma16(ax[m], bx[l], acc[m][l]); \
        Z2_STATS(ax, bx); cs[2] = mfma16(ones, bx[4], cs[2]); } while (0)
#define Z2_MMAY() do { _Pragma("unroll") for (int l = 0; l < 4; ++l) _Pragma("unroll") for (int m = 0; m < 4; ++m) acc[m][l] = mfma16(ay[m], by[l], acc[m][l]); \
        Z2_STATS(ay, by); } while (0)
#define MMAX() do { _Pragma("unroll") for (int l = 0; l < 5; ++l) _Pragma("unroll") for (int m = 0; m < 4; ++m) acc[m][l] = mfma16(bx[l], ax[m], acc[m][l]); } while (0)
#define MMAY() do { _Pragma("unroll") for (int l = 0; l < 4; ++l) _Pragma("unroll") for (int m = 0; m < 4; ++m) acc[m][l] = mfma16(by[l], ay[m], acc[m][l]); } while (0)
#define MMAX0() do { _Pragma("unroll") for (int l = 0; l < 5; ++l) _Pragma("unroll") for (int m = 0; m < 4; ++m) acc[m][l] = mfma16(bx[l], ax[m], zero4); } while (0)
#define Z1_MMAX() do { MMAX(); cs[0] = mfma16(bx[0], ones, cs[0]); cs[1] = mfma16(bx[1], ones, cs[1]); cs[2] = mfma16(bx[4], ones, cs[2]); } while (0)
#define Z1_MMAX0() do { MMAX0(); cs[0] = mfma16(bx[0], ones, zero4); cs[1] = mfma16(bx[1], ones, zero4); cs[2] = mfma16(bx[4], ones, zero4); } while (0)
#define PINCS() asm volatile("" : "+v"(cs[0]), "+v"(cs[1]), "+v"(cs[2]))
#define Z1_MMAY() do { MMAY(); cs[0] = mfma16(by[0], ones, cs[0]); cs[1] = mfma16(by[1], ones, cs[1]); } while (0)
        TAG_READY();
        BAR();
        if (wid == 0) { STAMP(0, 1); STAMPC(0, 14); }
        READXT(lds);
#pragma unroll 1
        for (int t = 0; t < 8; ++t) {
            LAS char* sb = lds + (t & 3) * SLOT;
            LAS char* sn = lds + ((t + 1) & 3) * SLOT;
            READYT(sb);
            Z2_MMAX();
            SCHED_IL(12, 27);
            WAIT_L0();
            BAR();
            READXT(sn);
            Z2_MMAY();
            SCHED_IL(14, 22);
        }
        READYT(lds + opqs(0));
        Z2_MMAX();
        SCHED_IL(12, 27);
        LAS char* xb = lds + L_X4 + wm * 4096 + lane * 16;
        *(LAS f32x4*)(xb + wn * 2048) = acc[2][4];
        *(LAS f32x4*)(xb + wn * 2048 + 1024) = acc[3][4];
        Z2_MMAY();
        SCHED0();
        if (wid == 0) STAMP(0, 2);
        if ((fr >> 2) == g) {
#pragma unroll
            for (int i = 0; i < 2; ++i) {
                const float sq = (fr & 2) ? ((fr & 1) ? rq[i][3] : rq[i][2]) : ((fr & 1) ? rq[i][1] : rq[i][0]);
                const f32x2 c = ln_consts(rsm[i][0], sq, 1.0f / D);
                LAS float* p = (LAS float*)(lds + L_ROWST + (32 * wm + 16 * wn + 4 * g + 2 * i + ((fr >> 1) & 1)) * 16 + (fr & 1) * 4);
                p[0] = c.x; p[2] = c.y;
            }
        }
        if (g == 0) {
            LAS float* c = (LAS float*)(lds + L_CS);
            c[(nb + 2 * wm) * 16 + fr] = cs[0][0];
            c[(nb + 2 * wm + 1) * 16 + fr] = cs[1][0];
            if (wm == 0) c[(wn ? 9 : 4) * 16 + fr] = cs[2][0];
        }
        WAIT_L0();
        BAR();
        acc[0][4] += *(LAS f32x4*)(xb + (1 - wn) * 2048);
        acc[1][4] += *(LAS f32x4*)(xb + (1 - wn) * 2048 + 1024);
        u32x4 zp[9];
        {
            float cc[5];
            LAS float* c = (LAS float*)(lds + L_CS) + fr;
            cc[0] = c[(nb + 2 * wm) * 16]; cc[1] = c[(nb + 2 * wm + 1) * 16]; cc[2] = c[(nb + 2 - 2 * wm) * 16]; cc[3] = c[(nb + 3 - 2 * wm) * 16]; cc[4] = c[4 * 16] + c[9 * 16];
#pragma unroll
            for (int ip = 0; ip < 2; ++ip) {
                const int mp = ip ^ wn;
                f32x4 st[4];
#pragma unroll
                for (int j = 0; j < 4; ++j) st[j] = *(LAS f32x4*)(lds + L_ROWST + (32 * wm + 16 * mp + 4 * g + j) * 16);
                f32x2 sum[4], sq[4];
#pragma unroll
                for (int l = 0; l < (ip == 0 ? 5 : 4); ++l) {
#pragma unroll
                    for (int j = 0; j < 4; ++j) {
                        const f32x4 a = acc[2 * ip + (j >> 1)][l];
                        const f32x2 z = gelu2(fma2(lo2(st[j]), (f32x2){-cc[l], -cc[l]}, (j & 1) ? hi2(a) : lo2(a)) * hi2(st[j]));
                        zp[ip * 5 + l][j] = pk2v(z);
                        if (l == 0) { sum[j] = z; sq[j] = z * z; } else { sum[j] += z; sq[j] = fma2(z, z, sq[j]); }
                    }
                }
                float s0 = sum[0].x, s1 = sq[0].x, s2 = sum[0].y, s3 = sq[0].y, s4 = sum[1].x, s5 = sq[1].x, s6 = sum[1].y, s7 = sq[1].y;
                asm volatile(
                    "s_nop 1\n"
                    "v_add_f32_dpp %0, %0, %0 row_mirror row_mask:0xf bank_mask:0x3\n v_add_f32_dpp %1, %1, %1 row_mirror row_mask:0xf bank_mask:0x3\n"
                    "v_add_f32_dpp %2, %2, %2 row_mirror row_mask:0xf bank_mask:0x3\n v_add_f32_dpp %3, %3, %3 row_mirror row_mask:0xf bank_mask:0x3\n"
                    "v_add_f32_dpp %4, %4, %4 row_mirror row_mask:0xf bank_mask:0x3\n v_add_f32_dpp %5, %5, %5 row_mirror row_mask:0xf bank_mask:0x3\n"
                    "v_add_f32_dpp %6, %6, %6 row_mirror row_mask:0xf bank_mask:0x3\n v_add_f32_dpp %7, %7, %7 row_mirror row_mask:0xf bank_mask:0x3\n"
                    "v_add_f32_dpp %0, %8, %8 row_mirror row_mask:0xf bank_mask:0xc\n v_add_f32_dpp %1, %9, %9 row_mirror row_mask:0xf bank_mask:0xc\n"
                    "v_add_f32_dpp %2, %10, %10 row_mirror row_mask:0xf bank_mask:0xc\n v_add_f32_dpp %3, %11, %11 row_mirror row_mask:0xf bank_mask:0xc\n"
                    "v_add_f32_dpp %4, %12, %12 row_mirror row_mask:0xf bank_mask:0xc\n v_add_f32_dpp %5, %13, %13 row_mirror row_mask:0xf bank_mask:0xc\n"
                    "v_add_f32_dpp %6, %14, %14 row_mirror row_mask:0xf bank_mask:0xc\n v_add_f32_dpp %7, %15, %15 row_mirror row_mask:0xf bank_mask:0xc\n"
                    "v_add_f32_dpp %0, %0, %0 row_half_mirror row_mask:0xf bank_mask:0x5\n v_add_f32_dpp %1, %1, %1 row_half_mirror row_mask:0xf bank_mask:0x5\n"
                    "v_add_f32_dpp %2, %2, %2 row_half_mirror row_mask:0xf bank_mask:0x5\n v_add_f32_dpp %3, %3, %3 row_half_mirror row_mask:0xf bank_mask:0x5\n"
                    "v_add_f32_dpp %0, %4, %4 row_half_mirror row_mask:0xf bank_mask:0xa\n v_add_f32_dpp %1, %5, %5 row_half_mirror row_mask:0xf bank_mask:0xa\n"
                    "v_add_f32_dpp %2, %6, %6 row_half_mirror row_mask:0xf bank_mask:0xa\n v_add_f32_dpp %3, %7, %7 row_half_mirror row_mask:0xf bank_mask:0xa\n"
                    "v_add_f32_dpp %0, %0, %0 quad_perm:[1,0,3,2] row_mask:0xf bank_mask:0xf\n v_add_f32_dpp %1, %1, %1 quad_perm:[1,0,3,2] row_mask:0xf bank_mask:0xf\n"
                    "v_add_f32_dpp %2, %2, %2 quad_perm:[1,0,3,2] row_mask:0xf bank_mask:0xf\n v_add_f32_dpp %3, %3, %3 quad_perm:[1,0,3,2] row_mask:0xf bank_mask:0xf\n"
                    "v_add_f32_dpp %0, %0, %0 quad_perm:[2,3,0,1] row_mask:0xf bank_mask:0xf\n v_add_f32_dpp %1, %1, %1 quad_perm:[2,3,0,1] row_mask:0xf bank_mask:0xf\n"
                    "v_add_f32_dpp %2, %2, %2 quad_perm:[2,3,0,1] row_mask:0xf bank_mask:0xf\n v_add_f32_dpp %3, %3, %3 quad_perm:[2,3,0,1] row_mask:0xf bank_mask:0xf\n"
                    : "+v"(s0), "+v"(s1), "+v"(s2), "+v"(s3), "+v"(s4), "+v"(s5), "+v"(s6), "+v"(s7)
                    : "v"(sum[2].x), "v"(sq[2].x), "v"(sum[2].y), "v"(sq[2].y), "v"(sum[3].x), "v"(sq[3].x), "v"(sum[3].y), "v"(sq[3].y));
                if (p4 == 0) *(LAS f32x4*)(lds + L_X4 + wm * 4096 + (1 - wn) * 2048 + (16 * mp + 4 * g + q4) * 16) = (f32x4){s0, s1, s2, s3};
            }
        }
        PIN9(zp);
        if (wid == 0) STAMP(0, 3);
        WAIT_L0();
        BAR();
        const unsigned zdst = WS_Z2N + ((pj * 32 + pi * 2 + wm) * 18) * 1024;
        auto normalise = [&]() {
#pragma unroll
            for (int ip = 0; ip < 2; ++ip) {
                const int mp = ip ^ wn;
                f32x4 st[4];
#pragma unroll
                for (int j = 0; j < 4; ++j) st[j] = *(LAS f32x4*)(lds + L_ZST + (32 * wm + 16 * mp + 4 * g + j) * 16);
#pragma unroll
                for (int l = 0; l < (ip == 0 ? 5 : 4); ++l) {
#pragma unroll
                    for (int j = 0; j < 4; ++j) zp[ip * 5 + l][j] = pk2v(fma2(up2(zp[ip * 5 + l][j]), lo2(st[j]), hi2(st[j])));
                    const int n = l == 4 ? 4 : nb + ((l + 2 * wm) & 3);
                    st16c(rs, lane * 16, zdst + n * 2048 + mp * 1024, zp[ip * 5 + l]);
                }
            }
        };
        char* fzp = ws + WS_FZ + (pj * 16 + pi) * 8;
        int zs = -1;
        LAS char* sp = lds + opqs(SLOT);
        READXT(sp);
        READYT(sp);
        Z1_MMAX0();
        SCHED0();
        PINCS();
        WAIT_L0();
        BAR();
        sp = lds + opqs(2 * SLOT);
        READXT(sp);
        Z1_MMAY();
        SCHED_IL(14, 18);
#pragma unroll 1
        for (int t = 10; t < 17; ++t) {
            LAS char* sb = lds + (t & 3) * SLOT;
            LAS char* sn = lds + ((t + 1) & 3) * SLOT;
            READYT(sb);
            Z1_MMAX();
            SCHED_IL(12, 23);
            PINCS();
            WAIT_L0();
            BAR();
            const unsigned rdy = *(volatile LAS unsigned*)(lds + M_READY);
            READXT(sn);
            Z1_MMAY();
            SCHED_IL(15, 18);
            if (zs < 0) {
                if (RFL(rdy) != 0u) { if (wid == 0) STAMP(1, 0); normalise(); zs = NDELAY; if (wid == 0) STAMP(1, 1); }
            } else if (zs > 0) {
                if (--zs == 0) { if (wid == 0) STAMP(1, 2); WAIT_V(0); signal4(lds, 0, fzp, lane, tag); if (wid == 0) STAMP(1, 3); }
            }
            SCHED0();
        }
        sp = lds + opqs(SLOT);
        READYT(sp);
        Z1_MMAX();
        SCHED_IL(12, 23);
        *(LAS f32x4*)(xb + wn * 2048) = acc[2][4];
        *(LAS f32x4*)(xb + wn * 2048 + 1024) = acc[3][4];
        Z1_MMAY();
        SCHED0();
        if (wid == 0) STAMP(0, 4);
        if (fr == 0) {
            LAS char* c = lds + L_CS + g * 16;
            *(LAS f32x4*)(c + (nb + 2 * wm) * 64) = cs[0];
            *(LAS f32x4*)(c + (nb + 2 * wm + 1) * 64) = cs[1];
            if (wm == 0) *(LAS f32x4*)(c + (wn ? 9 : 4) * 64) = cs[2];
        }
        if (zs == 1) { if (wid == 0) STAMP(1, 2); WAIT_V(0); signal4(lds, 0, fzp, lane, tag); zs = 0; if (wid == 0) STAMP(1, 3); }
        WAIT_L0();
        BAR();
        if (zs < 0) { if (wid == 0) STAMP(1, 0); normalise(); if (wid == 0) STAMP(1, 1); }
        SCHED0();
#pragma unroll
        for (int ip = 0; ip < 2; ++ip)
#pragma unroll
            for (int l = 0; l < (ip == 0 ? 5 : 4); ++l) {
                const int n = l == 4 ? 4 : nb + ((l + 2 * wm) & 3);
                *(LAS u32x4*)(lds + (2 + wm) * SLOT + SL_A + n * 2048 + (ip ^ wn) * 1024 + lane * 16) = zp[ip * 5 + l];
            }
        const int it = tid < 144 ? tid : 143;
        const int sg = it / 18, dc = it - sg * 18;
        const int d0 = (b & 7) * 72 + dc * 4, s0 = (b >> 3) * 64 + sg * 8;
        u32x2 kw[11], vw[8]; u32x4 sk0, sk1;
        if (wid < 3) {
#pragma unroll
            for (int i = 0; i < 11; ++i) { const int s = s0 - 3 + i; kw[i] = *(const u32x2*)((const char*)kin + (unsigned)(((s < 0 ? 0 : s) * D + d0) * 2)); }
#pragma unroll
            for (int i = 0; i < 8; ++i) vw[i] = *(const u32x2*)((const char*)vin + (unsigned)(((s0 + i) * D + d0) * 2));
            sk0 = *(const u32x4*)((const char*)shk + (unsigned)(d0 * 8)); sk1 = *(const u32x4*)((const char*)shk + (unsigned)(d0 * 8 + 16));
        }
        float bias[4];
#pragma unroll
        for (int m = 0; m < 4; ++m) bias[m] = bflo((unsigned)b_sp[pi * 128 + 64 * wm + 16 * ((m + 2 * wn) & 3) + fr]);
        SCHED0();
        acc[0][4] += *(LAS f32x4*)(xb + (1 - wn) * 2048);
        acc[1][4] += *(LAS f32x4*)(xb + (1 - wn) * 2048 + 1024);
        {
            f32x4 cc[5];
            LAS char* c = lds + L_CS + g * 16;
            cc[0] = cs[0]; cc[1] = cs[1]; cc[2] = *(LAS f32x4*)(c + (nb + 2 - 2 * wm) * 64); cc[3] = *(LAS f32x4*)(c + (nb + 3 - 2 * wm) * 64);
            cc[4] = *(LAS f32x4*)(c + 4 * 64) + *(LAS f32x4*)(c + 9 * 64);
#pragma unroll
            for (int m = 0; m < 4; ++m) {
                LAS float* p = (LAS float*)(lds + L_ROWST + (32 * wm + 8 * ((m + 2 * wn) & 3) + (fr >> 1)) * 16 + (fr & 1) * 4);
                const float mb = -p[0], rd = p[2];
#pragma unroll
                for (int l = 0; l < (m < 2 ? 5 : 4); ++l) {
                    const unsigned z0 = pk2v(gelu2(fma2(lo2(cc[l]), (f32x2){mb, mb}, lo2(acc[m][l])) * rd));
                    const unsigned z1 = pk2v(gelu2(fma2(hi2(cc[l]), (f32x2){mb, mb}, hi2(acc[m][l])) * rd));
                    if (l < 4) { zp[m * 2 + (l >> 1)][(l & 1) * 2] = z0; zp[m * 2 + (l >> 1)][(l & 1) * 2 + 1] = z1; }
                    else { zp[8][m * 2] = z0; zp[8][m * 2 + 1] = z1; }
                }
            }
        }
        PIN9(zp);
        SCHED0();
        if (wid == 0) STAMP(0, 5);
        if (zs != 0) { if (wid == 0) STAMP(1, 2); WAIT_V(0); signal4(lds, 0, fzp, lane, tag); if (wid == 0) STAMP(1, 3); }
        asm volatile("" : "+v"(bias[0]), "+v"(bias[1]), "+v"(bias[2]), "+v"(bias[3]));
        if (wid < 3) {
#pragma unroll
            for (int jp = 0; jp < 2; ++jp) {
                const u32x4 sk = jp ? sk1 : sk0;
                const f32x2 h0 = {bflo(sk[0]), bflo(sk[2])}, h1 = {bfhi(sk[0]), bfhi(sk[2])}, h2 = {bflo(sk[1]), bflo(sk[3])}, h3 = {bfhi(sk[1]), bfhi(sk[3])};
                f32x2 kk[11], o[8];
#pragma unroll
                for (int i = 0; i < 11; ++i) kk[i] = up2((i < 3 && s0 == 0) ? 0u : kw[i][jp]);
#pragma unroll
                for (int i = 0; i < 8; ++i) o[i] = fma2(kk[i + 3], h0, fma2(kk[i + 2], h1, fma2(kk[i + 1], h2, kk[i] * h3))) * up2(vw[i][jp]);
                if (tid < 144) {
                    st16c(rs, (d0 * S + s0) * 2, WS_KVT + 2 * jp * (S * 2), (u32x4){pk2(o[7].x, o[6].x), pk2(o[5].x, o[4].x), pk2(o[3].x, o[2].x), pk2(o[1].x, o[0].x)});
                    st16c(rs, (d0 * S + s0) * 2, WS_KVT + (2 * jp + 1) * (S * 2), (u32x4){pk2(o[7].y, o[6].y), pk2(o[5].y, o[4].y), pk2(o[3].y, o[2].y), pk2(o[1].y, o[0].y)});
                }
            }
        }
        if (tid == 0) *(volatile LAS unsigned*)(lds + M_HINT) = 1u;
        const int fb = SL_A + lane * 16;
        const int fX01 = fb + (nb + 2 * wm) * 2048 + wn * 1024, fX23 = fb + (nb + 2 - 2 * wm) * 2048 + wn * 1024, fX4 = fb + 4 * 2048 + wn * 1024;
        const int fY01 = fb + (nb + 2 * wm) * 2048 + (1 - wn) * 1024, fY23 = fb + (nb + 2 - 2 * wm) * 2048 + (1 - wn) * 1024;
#define READXF(sb) do { bx[0] = RD((sb) + fX01); ax[0] = RD((sb) + aX01); ax[1] = RD((sb) + aX01 + 2048); ax[2] = RD((sb) + aX23); ax[3] = RD((sb) + aX23 + 2048); \
        bx[1] = RD((sb) + fX01 + 2048); bx[2] = RD((sb) + fX23); bx[3] = RD((sb) + fX23 + 2048); bx[4] = RD((sb) + fX4); } while (0)
#define READYF(sb) do { by[0] = RD((sb) + fY01); ay[0] = RD((sb) + aY01); ay[1] = RD((sb) + aY01 + 2048); ay[2] = RD((sb) + aY23); ay[3] = RD((sb) + aY23 + 2048); \
        by[1] = RD((sb) + fY01 + 2048); by[2] = RD((sb) + fY23); by[3] = RD((sb) + fY23 + 2048); } while (0)
        if (wid == 0) STAMP(0, 6);
        WAIT_L0();
        BAR();
        if (wid == 0) STAMP(0, 7);
        sp = lds + opqs(2 * SLOT);
        READXF(sp);
        READYF(sp);
        MMAX0();
        SCHED0();
        WAIT_L0();
        BAR();
        sp = lds + opqs(3 * SLOT);
        READXF(sp);
        MMAY();
        SCHED_IL(9, 16);
#pragma unroll 1
        for (int t = 19; t < 49; ++t) {
            LAS char* sb = lds + (t & 3) * SLOT;
            LAS char* sn = lds + ((t + 1) & 3) * SLOT;
            READYF(sb);
            MMAX();
            SCHED_IL(8, 20);
            WAIT_L0();
            BAR();
            READXF(sn);
            MMAY();
            SCHED_IL(9, 16);
            if (t == 22) { WAIT_V(0); signal4(lds, 1, ws + WS_FK + ((b & 7) * 32 + (b >> 3)) * 8, lane, tag); if (wid == 0) { STAMP(0, 8); STAMPC(1, 14); } }
            SCHED0();
        }
        if (wid == 0) { STAMP(0, 9); STAMPC(1, 15); }
        sp = lds + opqs(SLOT);
        READYF(sp);
        MMAX();
        SCHED_IL(8, 20);
        {
            LAS char* p = lds + RED_C + (wid ^ 2) * RED_W + lane * 16;
            *(LAS f32x4*)p = acc[2][4];
            *(LAS f32x4*)(p + 1024) = acc[3][4];
        }
        MMAY();
        SCHED0();
        if (wid == 0) { STAMP(0, 11); STAMPC(0, 15); }
        WAIT_L0();
        BAR();
        acc[0][4] += *(LAS f32x4*)(lds + RED_C + wid * RED_W + lane * 16);
        acc[1][4] += *(LAS f32x4*)(lds + RED_C + wid * RED_W + 1024 + lane * 16);
        {
            const int kap0 = 9 * (pj >> 1) + 4 * (pj & 1) + 2 * wn, kap4 = 9 * (pj >> 1) + 8;
#pragma unroll
            for (int m = 0; m < 4; ++m) {
                const unsigned dst = WS_P + (pi * 8 + 4 * wm + ((m + 2 * wn) & 3)) * (72 * 1024);
                const f32x2 bs = {bias[m], bias[m]};
#pragma unroll
                for (int lp = 0; lp < 2; ++lp) {
                    const u32x4 z = zp[m * 2 + lp];
                    const f32x4 a = acc[m][2 * lp], c = acc[m][2 * lp + 1];
                    st16c(rs, lane * 16, dst + (kap0 + (lp ^ wm)) * 1024, (u32x4){pk2v(up2(z[0]) * (lo2(a) + bs)), pk2v(up2(z[1]) * (hi2(a) + bs)), pk2v(up2(z[2]) * (lo2(c) + bs)), pk2v(up2(z[3]) * (hi2(c) + bs))});
                }
                if (m < 2) st8c(rs, lane * 16, dst + kap4 * 1024 + (pj & 1) * 8, (u32x2){pk2v(up2(zp[8][2 * m]) * (lo2(acc[m][4]) + bs)), pk2v(up2(zp[8][2 * m + 1]) * (hi2(acc[m][4]) + bs))});
            }
        }
        if (wid == 0) STAMP(4, 0);
        WAIT_V(0);
        signal4(lds, 2, ws + WS_FP + (pi * 16 + pj) * 8, lane, tag);
        if (wid == 0) STAMP(4, 1);
        SCHED0();
        bf16x8 bq[RB][5];
        G3_PRE();
        G3_MAIN(4);
    }
    {
        const int lane = opqv(tid) & 63, fr = lane & 15, g = lane >> 4;
        const int m = wid >> 1, l0 = 2 * (wid & 1);
        const int tA = 64 * rg + 16 * m + fr, dA = 16 * (n30 + l0) + 4 * g;
        const int tH = 64 * rg + 32 * (wid & 1) + 16 * (g >> 1) + fr, dH = 16 * n34 + 8 * (ct & 1) + 4 * (g & 1);
        const char* fy = ws + WS_FY + (lane < 32 ? 16 * (n30 + l0) + lane : 16 * n34 + 8 * (ct & 1) + (lane & 7)) * 8;
        const u64 fyv = ldflag(fy);
        u32x2 xv[3]; unsigned yv[3][4]; float mb[2], rd[2];
        xv[0] = *(const u32x2*)((const char*)x + (unsigned)((tA * D + dA) * 2));
        xv[1] = *(const u32x2*)((const char*)x + (unsigned)((tA * D + dA + 16) * 2));
        xv[2] = *(const u32x2*)((const char*)x + (unsigned)((tH * D + dH) * 2));
        mb[0] = __uint_as_float(ld4c(rs, WS_ST1 + (tA >> 1) * 16 + (tA & 1) * 4)); rd[0] = __uint_as_float(ld4c(rs, WS_ST1 + (tA >> 1) * 16 + (tA & 1) * 4 + 8));
        mb[1] = __uint_as_float(ld4c(rs, WS_ST1 + (tH >> 1) * 16 + (tH & 1) * 4)); rd[1] = __uint_as_float(ld4c(rs, WS_ST1 + (tH >> 1) * 16 + (tH & 1) * 4 + 8));
        SCHED0();
        LAS char* red = lds + (wid < 4 ? RED_C + wid * RED_W : (wid - 4) * CV_WAVE);
#pragma unroll
        for (int mm = 0; mm < 2; ++mm)
#pragma unroll
            for (int l = 0; l < 4; ++l) *(LAS f32x4*)(red + (mm * 4 + l) * 1024 + lane * 16) = acc[mm][l];
        SCHED0();
        if (!allset(fyv, tag)) spin_flags(fy, tag);
        asm volatile("" ::: "memory");
#pragma unroll
        for (int rr = 0; rr < 4; ++rr) {
            yv[0][rr] = ld2c(rs, (dA * S + tA) * 2, WS_YT + rr * (S * 2));
            yv[1][rr] = ld2c(rs, (dA * S + tA) * 2, WS_YT + (16 + rr) * (S * 2));
            yv[2][rr] = ld2c(rs, (dH * S + tH) * 2, WS_YT + rr * (S * 2));
        }
        SCHED0();
#pragma unroll
        for (int mm = 2; mm < 4; ++mm)
#pragma unroll
            for (int l = 0; l < 4; ++l) *(LAS f32x4*)(red + (mm * 4 + l) * 1024 + lane * 16) = acc[mm][l];
        if ((g >> 1) == (ct & 1)) {
#pragma unroll
            for (int mm = 0; mm < 4; ++mm) *(LAS f32x4*)(red + 16384 + mm * 512 + (lane & 31) * 16) = acc[mm][4];
        }
        if (wid == 0) STAMP(4, 9);
        asm volatile("s_waitcnt lgkmcnt(0)\n s_barrier\n.Lmega_tail:" ::: "memory");
        if (wid == 0) STAMP(4, 10);
#pragma unroll
        for (int u = 0; u < 3; ++u) {
            if (u == 2 && wid >= 2) break;
            const int off = (u < 2 ? (2 * wid + u) * 1024 : 16384 + wid * 1024) + lane * 16;
            f32x4 o = zero4;
#pragma unroll
            for (int v = 0; v < 4; ++v) o += *(LAS f32x4*)(lds + v * CV_WAVE + off) + *(LAS f32x4*)(lds + RED_C + v * RED_W + off);
            float res[4];
#pragma unroll
            for (int rr = 0; rr < 4; ++rr) {
                const float sg = o[rr] * __builtin_amdgcn_rcpf(1.0f + __builtin_amdgcn_exp2f(-1.4426950408889634f * o[rr]));
                const float xx = (rr & 1) ? bfhi(xv[u][rr >> 1]) : bflo(xv[u][rr >> 1]);
                res[rr] = fmaf((xx - mb[u >> 1]) * rd[u >> 1] * bflo(yv[u][rr]), sg, xx);
            }
            const unsigned oo = u < 2 ? (tA * D + dA + 16 * u) * 2 : (tH * D + dH) * 2;
            *(u32x2*)((char*)out + oo) = (u32x2){pk2(res[0], res[1]), pk2(res[2], res[3])};
        }
        if (wid == 0) { WAIT_V(0); STAMP(4, 11); }
    }
}

extern "C" void kernel_launch(void* const* d_in, const int* in_sizes, int n_in,
                              void* d_out, int out_size, void* d_ws, size_t ws_size,
                              hipStream_t stream) {
    (void)hipFuncSetAttribute((const void*)mega, hipFuncAttributeMaxDynamicSharedMemorySize, LDS_SZ);
    mega<<<256, 512, LDS_SZ, stream>>>((const u16*)d_in[0], (const u16*)d_in[1], (const u16*)d_in[2], (const u16*)d_in[3], (const u16*)d_in[4], (const u16*)d_in[5],
                                       (const u16*)d_in[7], (const u16*)d_in[8], (const u16*)d_in[9], (const u16*)d_in[10], (char*)d_ws, (u16*)d_out);
}
```

```cpp
#include <hip/hip_runtime.h>
#include <stdint.h>
#define CODE_PF
#define E4

#define LAS __attribute__((address_space(3)))
#define GAS __attribute__((address_space(1)))
typedef unsigned short u16;
typedef unsigned long long u64;
typedef short bf16x8 __attribute__((ext_vector_type(8)));
typedef short bf16x4 __attribute__((ext_vector_type(4)));
typedef float f32x4 __attribute__((ext_vector_type(4)));
typedef float f32x2 __attribute__((ext_vector_type(2)));
typedef unsigned u32x4 __attribute__((ext_vector_type(4)));
typedef unsigned u32x2 __attribute__((ext_vector_type(2)));
typedef __bf16 bf16x2_t __attribute__((ext_vector_type(2)));

constexpr int S = 2048, D = 576, F = 4608, FH = 2304;

__device__ __forceinline__ float bflo(unsigned w) { return __uint_as_float(w << 16); }
__device__ __forceinline__ float bfhi(unsigned w) { return __uint_as_float(w & 0xffff0000u); }
__device__ __forceinline__ unsigned pk2v(f32x2 p) { return __builtin_bit_cast(unsigned, __builtin_convertvector(p, bf16x2_t)); }
__device__ __forceinline__ unsigned pk2(float lo, float hi) { return pk2v((f32x2){lo, hi}); }
__device__ __forceinline__ f32x2 up2(unsigned w) { return (f32x2){bflo(w), bfhi(w)}; }
__device__ __forceinline__ float rbf(float x) { return bflo(pk2(x, x)); }
__device__ __forceinline__ f32x2 rbf2(f32x2 x) { return up2(pk2v(x)); }
__device__ __forceinline__ f32x2 fma2(f32x2 a, f32x2 b, f32x2 c) { return __builtin_elementwise_fma(a, b, c); }
__device__ __forceinline__ f32x2 lo2(f32x4 a) { return (f32x2){a[0], a[1]}; }
__device__ __forceinline__ f32x2 hi2(f32x4 a) { return (f32x2){a[2], a[3]}; }
__device__ __forceinline__ f32x4 mfma16(bf16x8 a, bf16x8 b, f32x4 c) { return __builtin_amdgcn_mfma_f32_16x16x32_bf16(a, b, c, 0, 0, 0); }
__device__ __forceinline__ void glds16(const void* g, LAS char* l) { __builtin_amdgcn_global_load_lds((const unsigned*)g, (LAS unsigned*)l, 16, 0, 0); }
__device__ __forceinline__ void glds16c(const void* g, LAS char* l) { __builtin_amdgcn_global_load_lds((const unsigned*)g, (LAS unsigned*)l, 16, 0, 16); }
__device__ __forceinline__ void glds4(const void* g, LAS char* l) { __builtin_amdgcn_global_load_lds((const unsigned*)g, (LAS unsigned*)l, 4, 0, 0); }
__device__ __forceinline__ bf16x4 trread(LAS char* p) { return __builtin_bit_cast(bf16x4, __builtin_amdgcn_ds_read_tr16_b64_v4i16((LAS bf16x4*)p)); }
__device__ __forceinline__ bf16x8 cat8(bf16x4 a, bf16x4 b) { return __builtin_shufflevector(a, b, 0, 1, 2, 3, 4, 5, 6, 7); }
#define WAIT_V(n) asm volatile("s_waitcnt vmcnt(%0)" ::"n"(n) : "memory")
#define WAIT_VB(n) __builtin_amdgcn_s_waitcnt(((n) & 15) | (7 << 4) | (15 << 8) | (((n) >> 4) << 14))
#define WAIT_L0() asm volatile("s_waitcnt lgkmcnt(0)" ::: "memory")
#define BAR() do { asm volatile("" ::: "memory"); __builtin_amdgcn_s_barrier(); asm volatile("" ::: "memory"); } while (0)
#define SCHED0() __builtin_amdgcn_sched_barrier(0)
#define RLX_AGENT __ATOMIC_RELAXED, __HIP_MEMORY_SCOPE_AGENT
#define PIN9(z) asm volatile("" : "+v"(z[0]), "+v"(z[1]), "+v"(z[2]), "+v"(z[3]), "+v"(z[4]), "+v"(z[5]), "+v"(z[6]), "+v"(z[7]), "+v"(z[8]))
__device__ __forceinline__ int opqs(int x) { asm volatile("" : "+s"(x)); return x; }
__device__ __forceinline__ int opqv(int x) { asm volatile("" : "+v"(x)); return x; }
#define RFL(x) __builtin_amdgcn_readfirstlane(x)

#define STAMP(kid, i) do { } while (0)
#define STAMPC(kid, i) do { } while (0)

__device__ __forceinline__ f32x2 gelu2(f32x2 v) {
    const float c1 = -2.0f * 0.7978845608028654f * 1.4426950408889634f;
    const float c2 = c1 * 0.044715f;
    f32x2 a = v * fma2(v * v, (f32x2){c2, c2}, (f32x2){c1, c1});
    f32x2 d = (f32x2){__builtin_amdgcn_exp2f(a.x), __builtin_amdgcn_exp2f(a.y)} + (f32x2){1.0f, 1.0f};
    return v * (f32x2){__builtin_amdgcn_rcpf(d.x), __builtin_amdgcn_rcpf(d.y)};
}
__device__ __forceinline__ f32x2 ln_consts(float sm, float sq, float inv_n) {
    const float mean = sm * inv_n;
    const float var = fmaxf(sq * inv_n - mean * mean, 0.f);
    return (f32x2){rbf(mean), __builtin_amdgcn_rcpf(rbf(__builtin_amdgcn_sqrtf(rbf(rbf(var) + 1e-5f))))};
}

constexpr unsigned SZ_Z = S * FH * 2, SZ_X = S * D * 2;
constexpr unsigned WS_Z2N = 0;
constexpr unsigned WS_P = WS_Z2N + SZ_Z;
constexpr unsigned WS_KVT = WS_P + SZ_Z;
constexpr unsigned WS_YT = WS_KVT + SZ_X;
constexpr unsigned WS_WT = WS_YT + SZ_X;
constexpr unsigned WS_ST2 = WS_WT + FH * D * 2;
constexpr unsigned WS_ST1 = WS_ST2 + 256 * 1024;
constexpr unsigned WS_FS = WS_ST1 + 16384;
constexpr unsigned WS_FZ = WS_FS + 2048;
constexpr unsigned WS_FP = WS_FZ + 2048;
constexpr unsigned WS_FK = WS_FP + 2048;
constexpr unsigned WS_FY = WS_FK + 2048;
constexpr unsigned WS_E = WS_FY + 4608 + 128;
constexpr unsigned WS_END = WS_E + 128;
constexpr u64 TAG_INC = 0x9E3779B97F4A7C15ull;

__device__ __forceinline__ __amdgpu_buffer_rsrc_t ws_rsrc(char* ws) { return __builtin_amdgcn_make_buffer_rsrc(ws, 0, (int)WS_END, 0x00020000); }
__device__ __forceinline__ void st16c(__amdgpu_buffer_rsrc_t r, unsigned off, unsigned soff, u32x4 v) { __builtin_amdgcn_raw_buffer_store_b128(v, r, off, soff, 16); asm volatile("s_nop 1" :: "v"(v)); }
__device__ __forceinline__ void st8c(__amdgpu_buffer_rsrc_t r, unsigned off, unsigned soff, u32x2 v) { __builtin_amdgcn_raw_buffer_store_b64(v, r, off, soff, 16); }
__device__ __forceinline__ u32x4 ld16c(__amdgpu_buffer_rsrc_t r, unsigned off, unsigned soff) { return __builtin_amdgcn_raw_buffer_load_b128(r, off, soff, 16); }
__device__ __forceinline__ unsigned ld4c(__amdgpu_buffer_rsrc_t r, unsigned off) { return __builtin_amdgcn_raw_buffer_load_b32(r, off, 0, 16); }
__device__ __forceinline__ unsigned ld2c(__amdgpu_buffer_rsrc_t r, unsigned off, unsigned soff) { return __builtin_amdgcn_raw_buffer_load_b16(r, off, soff, 16); }
__device__ __forceinline__ __amdgpu_buffer_rsrc_t in_rsrc(const void* p, unsigned bytes) { return __builtin_amdgcn_make_buffer_rsrc((void*)p, 0, (int)bytes, 0x00020000); }
template <int IMM, int AUX> __device__ __forceinline__ void bl16(__amdgpu_buffer_rsrc_t r, LAS char* l, unsigned voff, unsigned soff) { __builtin_amdgcn_raw_ptr_buffer_load_lds(r, (LAS void*)l, 16, voff, soff, IMM, AUX); }
__device__ __forceinline__ void bl4(__amdgpu_buffer_rsrc_t r, LAS char* l, unsigned voff, unsigned soff) { __builtin_amdgcn_raw_ptr_buffer_load_lds(r, (LAS void*)l, 4, voff, soff, 0, 0); }
__device__ __forceinline__ u64 ldflag(const char* p) { return __hip_atomic_load((GAS u64*)p, RLX_AGENT); }
__device__ __forceinline__ void stflag(char* p, u64 v) { __hip_atomic_store((GAS u64*)p, v, RLX_AGENT); }
__device__ __forceinline__ bool allset(u64 v, u64 tag) { return __builtin_amdgcn_ballot_w64(v != tag) == 0ull; }

constexpr int SL_A = 16384, SL_B = 18432, SLOT = SL_A + SL_B;
constexpr unsigned SPIN_CAP = 1u << 20;
constexpr int L_X4 = 4 * SLOT;
constexpr int L_WSCR = L_X4 + 8192;
constexpr int L_ROWST = L_WSCR + 12288;
constexpr int L_ZST = L_ROWST + 1024;
constexpr int L_CS = L_ZST + 1024;
constexpr int L_MISC = L_CS + 768;
constexpr int L_DUMP = L_MISC + 256;
constexpr int LDS_SZ = 163840;
constexpr int M_TAG = L_MISC, M_READY = L_MISC + 8, M_HINT = L_MISC + 12, M_CNT = L_MISC + 16;
constexpr int CV_U = 512 + 4096, CV_HSTRIDE = 4160, CV_WAVE = CV_U + 4 * CV_HSTRIDE;
constexpr int RED_W = 18432, CV_BASE = L_ROWST - 3 * CV_WAVE;
typedef unsigned u32x3 __attribute__((ext_vector_type(3)));

#define SCHED_IL(nr, nm) do { _Pragma("unroll") for (int i_ = 0; i_ < (nr); ++i_) { __builtin_amdgcn_sched_group_barrier(0x008, 1, 0); __builtin_amdgcn_sched_group_barrier(0x100, 1, 0); } \
        __builtin_amdgcn_sched_group_barrier(0x008, (nm) - (nr), 0); SCHED0(); } while (0)

__device__ __forceinline__ void spin_flags(const char* p, u64 tag) {
    unsigned spins = 0;
    while (!allset(ldflag(p), tag)) { if (++spins > SPIN_CAP) break; __builtin_amdgcn_s_sleep(1); }
    asm volatile("" ::: "memory");
}
__device__ __forceinline__ void signal4(LAS char* lds, int cnt, char* flag, int lane, u64 tag) {
    if (lane == 0) {
        const unsigned old = __hip_atomic_fetch_add((LAS unsigned*)(lds + M_CNT + cnt * 4), 1u, __ATOMIC_RELAXED, __HIP_MEMORY_SCOPE_WORKGROUP);
        if (old == 3u) stflag(flag, tag);
    }
}

#define NDELAY 2

struct ConvIn { u32x4 uu[4], hh[4]; u32x2 hn[4]; unsigned dd; };
__device__ __forceinline__ void conv_load(ConvIn& c, const u16* __restrict__ hk, const u16* __restrict__ dv, __amdgpu_buffer_rsrc_t rs, int d, int lane) {
    const char* hsrc = (const char*)hk + (size_t)d * (S * 2);
#pragma unroll
    for (int i = 0; i < 4; ++i) { c.uu[i] = ld16c(rs, lane * 16 + i * 1024, WS_KVT + d * (S * 2)); c.hh[i] = *(const u32x4*)(hsrc + (unsigned)(lane * 16 + i * 1024)); }
#pragma unroll
    for (int i = 0; i < 4; ++i) { const unsigned o = lane * 16 + i * 1024 + 16; c.hn[i] = *(const u32x2*)(hsrc + (o > 4088u ? 4088u : o)); }
    c.dd = ((const unsigned*)dv)[d >> 1];
}
__device__ __forceinline__ void conv_stage(ConvIn& c, LAS char* U, int lane) {
    LAS char* H = U + CV_U;
    if (lane == 63) c.hn[3] = (u32x2){0u, 0u};
    if (lane < 32) *(LAS u32x4*)(U + lane * 16) = (u32x4){0u, 0u, 0u, 0u};
    const unsigned h0 = RFL(c.hh[0][0]), h1 = RFL(c.hh[0][1]);
    if (lane < 8) {
        const int cc = lane >> 1, half = lane & 1;
        u32x4 z = {0u, 0u, 0u, 0u};
        if (half) {
            if (cc == 1) z[3] = h0 << 16;
            if (cc == 2) z[3] = h0;
            if (cc == 3) { z[2] = h0 << 16; z[3] = __builtin_amdgcn_alignbit(h1, h0, 16); }
        }
        *(LAS u32x4*)(H + cc * CV_HSTRIDE + half * 16) = z;
    }
#pragma unroll
    for (int i = 0; i < 4; ++i) {
        *(LAS u32x4*)(U + 512 + i * 1024 + lane * 16) = c.uu[i];
        const unsigned w0 = c.hh[i][0], w1 = c.hh[i][1], w2 = c.hh[i][2], w3 = c.hh[i][3], w4 = c.hn[i][0], w5 = c.hn[i][1];
        const unsigned a01 = __builtin_amdgcn_alignbit(w1, w0, 16), a12 = __builtin_amdgcn_alignbit(w2, w1, 16), a23 = __builtin_amdgcn_alignbit(w3, w2, 16),
                       a34 = __builtin_amdgcn_alignbit(w4, w3, 16), a45 = __builtin_amdgcn_alignbit(w5, w4, 16);
        LAS char* p = H + 32 + i * 1024 + lane * 16;
        *(LAS u32x4*)(p) = c.hh[i];
        *(LAS u32x4*)(p + CV_HSTRIDE) = (u32x4){a01, a12, a23, a34};
        *(LAS u32x4*)(p + 2 * CV_HSTRIDE) = (u32x4){w1, w2, w3, w4};
        *(LAS u32x4*)(p + 3 * CV_HSTRIDE) = (u32x4){a12, a23, a34, a45};
    }
}
__device__ __forceinline__ void conv_run(unsigned ddw, __amdgpu_buffer_rsrc_t rs, char* __restrict__ ws, LAS char* U, int d, int lane) {
    const int fr = lane & 15, g = lane >> 4;
    LAS char* H = U + CV_U;
    const int e0 = 16 * (g >> 1) - 8 * (g & 1) - 7 + fr + 16;
    const int ac = e0 & 3;
    LAS char* ap = H + ac * CV_HSTRIDE + (e0 - ac) * 2;
    LAS char* bp = U + 512 + 32 * (fr - (g >> 1)) + 16 * (g & 1) - 448;
    f32x4 acc[8];
#pragma unroll
    for (int n = 0; n < 8; ++n) acc[n] = (f32x4){0.f, 0.f, 0.f, 0.f};
    bf16x8 LA[2][4], LB[2][4], HA[4], HB[4];
#define CV_RD(A, B, a0, pa, pb) do { _Pragma("unroll") for (int a = 0; a < 4; ++a) { B[a] = *(LAS bf16x8*)((pb) + (a0 + a) * 512); \
        A[a] = cat8(*(LAS bf16x4*)((pa) + (a0 + a) * 512), *(LAS bf16x4*)((pa) + (a0 + a) * 512 + 8)); } } while (0)
#define CV_STEP(cur, nxt, pa, pb) do { \
        CV_RD(HA, HB, 4, pa, pb); \
        _Pragma("unroll") for (int s = 0; s < 4; ++s) { \
            _Pragma("unroll") for (int a = 0; a < s; ++a) { acc[a + s] = mfma16(LA[cur][a], LB[cur][s], acc[a + s]); acc[a + s] = mfma16(LA[cur][s], LB[cur][a], acc[a + s]); } \
            acc[2 * s] = mfma16(LA[cur][s], LB[cur][s], acc[2 * s]); } \
        SCHED_IL(12, 16); \
        CV_RD(LA[nxt], LB[nxt], 0, (pa) + 64, (pb) - 64); \
        _Pragma("unroll") for (int s = 0; s < 4; ++s) \
            _Pragma("unroll") for (int a = 0; a + s < 4; ++a) { acc[a + s + 4] = mfma16(LA[cur][a], HB[s], acc[a + s + 4]); acc[a + s + 4] = mfma16(HA[s], LB[cur][a], acc[a + s + 4]); } \
        SCHED_IL(12, 20); \
    } while (0)
    CV_RD(LA[0], LB[0], 0, ap, bp + 448);
#pragma unroll 1
    for (int bb = 0; bb < 8; bb += 2) {
        LAS char* pa = ap + bb * 64;
        LAS char* pb = bp + (7 - bb) * 64;
        CV_STEP(0, 1, pa, pb);
        CV_STEP(1, 0, pa + 64, pb - 64);
    }
    STAMP(3, 4);
    const float dd = (d & 1) ? bfhi(ddw) : bflo(ddw);
    unsigned yw[8][2];
#pragma unroll
    for (int nu = 0; nu < 8; ++nu) {
        const u32x2 uw = *(LAS u32x2*)(U + 512 + (256 * nu + 16 * fr + 8 * (g >> 1)) * 2 + ((g & 1) ? 0 : 8));
        yw[nu][0] = pk2v(rbf2(lo2(acc[nu])) + rbf2((f32x2){bfhi(uw[1]), bflo(uw[1])} * dd));
        yw[nu][1] = pk2v(rbf2(hi2(acc[nu])) + rbf2((f32x2){bfhi(uw[0]), bflo(uw[0])} * dd));
    }
#pragma unroll
    for (int p = 0; p < 4; ++p) {
        const u32x2 s0 = __builtin_amdgcn_permlane16_swap(yw[2 * p][0], yw[2 * p + 1][0], false, false);
        const u32x2 s1 = __builtin_amdgcn_permlane16_swap(yw[2 * p][1], yw[2 * p + 1][1], false, false);
        st16c(rs, (256 * (g & 1) + 16 * fr + 8 * (g >> 1)) * 2 + p * 1024, WS_YT + d * (S * 2), (u32x4){s0[0], s1[0], s0[1], s1[1]});
    }
    STAMP(3, 5);
}

constexpr int RB = 5;
#define G3_LDB(s, i) do { _Pragma("unroll") for (int l = 0; l < 4; ++l) bq[s][l] = __builtin_bit_cast(bf16x8, ld16c(rs, lane * 16 + l * 1024, sB + (i) * (36 * 1024))); \
        bq[s][4] = __builtin_bit_cast(bf16x8, ld16c(rs, lane * 16, sB4 + (i) * (36 * 1024))); } while (0)
#define G3_LDA(s, i) do { _Pragma("unroll") for (int m = 0; m < 4; ++m) aq[s][m] = __builtin_bit_cast(bf16x8, ld16c(rs, lane * 16, sA + m * (72 * 1024) + (i) * 1024)); } while (0)
#define G3_PRE() do { _Pragma("unroll") for (int i = 0; i < RB; ++i) G3_LDB(i, i); SCHED0(); } while (0)
#define G3_MAIN(kid, MID) do { \
        STAMP(kid, 6); \
        { const char* fp = ws + WS_FP + (rg >> 1) * 128 + (2 * wid + (lane & 1)) * 8; \
          const bool ok = allset(ldflag(fp), tag); \
          MID; \
          if (!ok) spin_flags(fp, tag); \
          asm volatile("" ::: "memory"); } \
        STAMP(kid, 7); \
        bf16x8 aq[3][4]; \
        G3_LDA(0, 0); G3_LDA(1, 1); G3_LDA(2, 2); \
        SCHED0(); \
        _Pragma("unroll") for (int i = 0; i < 9; ++i) { \
            _Pragma("unroll") for (int m = 0; m < 4; ++m) _Pragma("unroll") for (int l = 0; l < 5; ++l) acc[m][l] = mfma16(bq[i % RB][l], aq[i % 3][m], i == 0 ? zero4 : acc[m][l]); \
            SCHED0(); \
            if (i + 3 < 9) G3_LDA(i % 3, i + 3); \
            if (i + RB < 9) G3_LDB(i % RB, i + RB); \
            SCHED0(); \
        } \
        STAMP(kid, 8); \
    } while (0)

__global__ __launch_bounds__(512, 2) void mega(const u16* __restrict__ x, const u16* __restrict__ kin, const u16* __restrict__ vin, const u16* __restrict__ shk,
                                               const u16* __restrict__ hk, const u16* __restrict__ dv, const u16* __restrict__ w_in, const u16* __restrict__ w_sp,
                                               const u16* __restrict__ b_sp, const u16* __restrict__ w_out, char* __restrict__ ws, u16* __restrict__ out) {
    extern __shared__ __attribute__((aligned(16))) char smem_[];
    LAS char* lds = (LAS char*)smem_;
    const int b = blockIdx.x, xcd = b & 7, r = b >> 3;
    const int pi = (xcd & 1) * 8 + (r & 7), pj = (xcd >> 1) * 4 + (r >> 3);
    const int tid = threadIdx.x, wid = RFL(tid >> 6), lane = tid & 63;
    const int fr = lane & 15, g = lane >> 4, q4 = (lane >> 2) & 3, p4 = lane & 3;
    const __amdgpu_buffer_rsrc_t rs = ws_rsrc(ws);
    const f32x4 zero4 = {0.f, 0.f, 0.f, 0.f};
    const int rg = (xcd & 1) * 16 + (xcd >> 1) * 4 + (r & 3), ct = r >> 2;
    const int nb3 = (9 * ct) >> 1, n30 = nb3 + (ct & 1), n34 = (ct & 1) ? nb3 : nb3 + 4;
    const unsigned sA = WS_P + (4 * rg * 72 + 9 * wid) * 1024, sB = WS_WT + (9 * wid * 36 + n30) * 1024, sB4 = WS_WT + (9 * wid * 36 + n34) * 1024;
    f32x4 acc[4][5];
    u64 tag;
#define TAG_READY() do { asm volatile("s_load_dwordx2 %0, %1, 0x0\n s_waitcnt lgkmcnt(0)" : "=s"(tag) : "s"(ws + WS_E) : "memory"); tag += TAG_INC; } while (0)

    if (wid >= 4) {
        const int w = wid - 4, l3 = lane >> 3;
        const unsigned ch = (lane & 7) ^ (l3 >> 1);
        const int rot = (pi + pj) % 9;
        const __amdgpu_buffer_rsrc_t rx = in_rsrc(x, SZ_X), rwi = in_rsrc(w_in, D * F * 2);
        const unsigned srow0 = 8 * (l3 >> 2) + (l3 & 3);
        const unsigned offA0 = srow0 * (D * 2) + ch * 16, offA1 = (srow0 + 16) * (D * 2) + (ch ^ 4) * 16;
        unsigned offB[5];
#pragma unroll
        for (int i = 0; i < 5; ++i) {
            const unsigned c = (w + 4 * i) * 64 + lane, rho = c / 18, cc = c - rho * 18;
            const unsigned grow = (rho & ~12u) | ((rho & 4) << 1) | ((rho & 8) >> 1);
            offB[i] = grow * (F * 2) + cc * 16;
        }
        int tt = rot;
        auto stage2 = [&](int t) {
            LAS char* sa = lds + (t & 3) * SLOT + w * 4096;
            LAS char* sb = lds + (t & 3) * SLOT + SL_A + w * 1024;
            const unsigned xs = (pi * 128 + 32 * w) * (D * 2) + tt * 128, wo = (FH + pj * 144) * 2 + tt * (64 * F * 2);
            bl16<0, 0>(rx, sa, offA0, xs); bl16<0, 0>(rx, sa + 1024, offA1, xs); bl16<0, 0>(rx, sa + 2048, offA0, xs + 4 * D * 2); bl16<0, 0>(rx, sa + 3072, offA1, xs + 4 * D * 2);
#pragma unroll
            for (int i = 0; i < 4; ++i) bl16<0, 0>(rwi, sb + i * 4096, offB[i], wo);
            if (w < 2) bl16<0, 0>(rwi, sb + 4 * 4096, offB[4], wo);
            if (++tt == 9) tt = 0;
        };
#pragma unroll 1
        for (int t = 0; t < 3; ++t) stage2(t);
        SCHED0();
        TAG_READY();
        const __amdgpu_buffer_rsrc_t rsp = in_rsrc(w_sp, S * S * 2);
        unsigned offE = l3 * (S * 2) + ch * 16;
        unsigned oE = l3 * (D * 2) + (ch ^ ((w & 1) * 4)) * 16;
        const int q0 = w == 0 ? 2 : w - 1;
        unsigned oB[3];
#pragma unroll
        for (int j = 0; j < 3; ++j) {
            const unsigned c = (q0 + 3 * j) * 64 + lane, rho = c / 18, cc = c - rho * 18;
            const unsigned grow = (rho & ~12u) | ((rho & 4) << 1) | ((rho & 8) >> 1);
            const unsigned c2 = j * 64 + lane, rr = c2 / 24;
            oB[j] = w < 3 ? grow * (F * 2) + cc * 16 : rr * (D * 2) + (c2 - rr * 24) * 16;
        }
        int kap = b / 3, th = b - kap * 3, woa, wob;
        {
            const int q = kap / 9, e = kap - q * 9, pr = e & 3;
            const int ga = e == 8 ? 18 * q + 4 : 18 * q + (e >> 2) * 9 + 2 * pr + (pr >> 1);
            woa = th * 384 + ga * (16 * D * 2); wob = woa + (e == 8 ? 9 : 1) * (16 * D * 2);
        }
        asm volatile("" : "+v"(offE), "+v"(oE), "+v"(oB[0]), "+v"(oB[1]), "+v"(oB[2]));
        asm volatile("" : "+s"(woa), "+s"(wob), "+s"(kap), "+s"(th));
#pragma unroll 1
        for (int t = 0; t < 6; ++t) {
            if (w < 2) WAIT_V(18); else WAIT_V(16);
            BAR();
            if (w == 1 && t < 2) STAMP(5, t);
            stage2(t + 3);
        }
        auto stageA = [&](int t) {
            LAS char* sa = lds + (t & 3) * SLOT + w * 8192;
            const unsigned as = (pi * 128 + 64 * w) * (S * 2) + ((2 * pi + t - 18) & 31) * 128;
#pragma unroll
            for (int i = 0; i < 8; ++i) bl16<0, 0>(rsp, sa + i * 1024, (i & 1) ? offE ^ 64 : offE, as + i * (8 * S * 2));
        };
        auto stageB = [&](int t) {
            LAS char* sb = lds + (t & 3) * SLOT + SL_A + (w - 2) * 9216;
            const unsigned zs = WS_Z2N + (pj * 32 + ((2 * pi + t - 18) & 31)) * SL_B + (w - 2) * 9216;
#pragma unroll
            for (int i = 0; i < 9; i += 3) { bl16<0, 16>(rs, sb + i * 1024, lane * 16, zs + i * 1024); bl16<1024, 16>(rs, sb + i * 1024, lane * 16, zs + i * 1024); bl16<2048, 16>(rs, sb + i * 1024, lane * 16, zs + i * 1024); }
        };
        if (w < 3) {
            u64 st7_ = 0, st8_ = 0;
#pragma unroll 1
            for (int t = 6; t < 15; ++t) {
                if (t == 9) BAR();
                if (t < 8) WAIT_V(16); else if (w == 0) WAIT_V(24); else WAIT_V(22);
                BAR();
                LAS char* sa = lds + ((t + 3) & 3) * SLOT + w * 1024;
                LAS char* sb = lds + ((t + 3) & 3) * SLOT + SL_A + q0 * 1024;
                const unsigned xs = (pi * 128 + 8 * w) * (D * 2) + tt * 128, wo = pj * 288 + tt * (64 * F * 2);
#pragma unroll
                for (int i = 0; i < 5; ++i) bl16<0, 0>(rx, sa + i * 3072, (i & 1) ? oE ^ 64 : oE, xs + i * (24 * D * 2));
                if (w == 0) bl16<0, 0>(rx, sa + 5 * 3072, oE ^ 64, xs + 5 * (24 * D * 2));
#pragma unroll
                for (int j = 0; j < 6; ++j) bl16<0, 0>(rwi, sb + j * 3072, oB[j % 3], wo + (j / 3) * (32 * F * 2));
                if (++tt == 9) tt = 0;
            }
            if (w < 2) {
                if (w == 0) WAIT_V(24); else WAIT_V(22);
                BAR();
                stageA(18);
                if (w == 0) WAIT_V(20); else WAIT_V(19);
                BAR();
                stageA(19);
                const int it = lane < 48 ? w * 48 + lane : w * 48 + 47;
                const int sg = it / 12, dc = it - sg * 12;
                const int d0 = (b & 7) * 72 + dc * 6, s0 = (b >> 3) * 64 + sg * 8;
                u32x3 kw[11], vw[8]; u32x4 sk[3];
                {
                    const __amdgpu_buffer_rsrc_t rk = in_rsrc(kin, SZ_X), rv = in_rsrc(vin, SZ_X);
                    const unsigned o = (s0 * D + d0) * 2;
#pragma unroll
                    for (int i = 0; i < 3; ++i) kw[i] = __builtin_amdgcn_raw_buffer_load_b96(rk, s0 == 0 ? 0x7fffff00u : o - (3 - i) * (D * 2), 0, 0);
#pragma unroll
                    for (int i = 0; i < 8; ++i) { kw[3 + i] = __builtin_amdgcn_raw_buffer_load_b96(rk, o, i * (D * 2), 0); vw[i] = __builtin_amdgcn_raw_buffer_load_b96(rv, o, i * (D * 2), 0); }
#pragma unroll
                    for (int i = 0; i < 3; ++i) sk[i] = *(const u32x4*)((const char*)shk + (unsigned)(d0 * 8 + i * 16));
                }
                SCHED0();
                WAIT_V(16 + 22); BAR();
                stageA(20);
                BAR();
                SCHED0();
#pragma unroll
                for (int jp = 0; jp < 3; ++jp) {
                    const f32x2 h0 = {bflo(sk[jp][0]), bflo(sk[jp][2])}, h1 = {bfhi(sk[jp][0]), bfhi(sk[jp][2])}, h2 = {bflo(sk[jp][1]), bflo(sk[jp][3])}, h3 = {bfhi(sk[jp][1]), bfhi(sk[jp][3])};
                    f32x2 kk[11], o[8];
#pragma unroll
                    for (int i = 0; i < 11; ++i) kk[i] = up2(kw[i][jp]);
#pragma unroll
                    for (int i = 0; i < 8; ++i) o[i] = fma2(kk[i + 3], h0, fma2(kk[i + 2], h1, fma2(kk[i + 1], h2, kk[i] * h3))) * up2(vw[i][jp]);
                    const unsigned vo = lane < 48 ? (unsigned)(d0 * S + s0) * 2 : 0x7fffff00u;
                    st16c(rs, vo, WS_KVT + 2 * jp * (S * 2), (u32x4){pk2(o[7].x, o[6].x), pk2(o[5].x, o[4].x), pk2(o[3].x, o[2].x), pk2(o[1].x, o[0].x)});
                    st16c(rs, vo, WS_KVT + (2 * jp + 1) * (S * 2), (u32x4){pk2(o[7].y, o[6].y), pk2(o[5].y, o[4].y), pk2(o[3].y, o[2].y), pk2(o[1].y, o[0].y)});
                }
                SCHED0();
                if (w == 0) STAMP(2, 5);
                BAR();
                stageA(21);
                BAR();
                stageA(22);
            } else {
                WAIT_V(22); BAR();
                WAIT_V(11); BAR();
                WAIT_VB(0); BAR();
                BAR();
            }
        } else {
            WAIT_V(16); BAR();
            {
                const __amdgpu_buffer_rsrc_t rwo = in_rsrc(w_out, b < 216 ? FH * D * 2 : 0);
#pragma unroll
                for (int i = 0; i < 6; ++i) bl16<0, 0>(rwo, lds + L_WSCR + i * 1024, oB[i % 3], woa + (i / 3) * (8 * D * 2));
#pragma unroll
                for (int i = 0; i < 6; ++i) bl16<0, 0>(rwo, lds + L_WSCR + 6144 + i * 1024, oB[i % 3], wob + (i / 3) * (8 * D * 2));
            }
            WAIT_V(20); BAR();
#pragma unroll
            for (int i = 0; i < 4; ++i) bl4(rsp, lds + L_DUMP, (lane >> 1) * (S * 2) + (lane & 1) * 128, (pi * 128 + i * 32) * (S * 2) + pj * 256);
            WAIT_V(16); BAR();
            BAR();
            if (b < 216) {
#pragma unroll
                for (int n = 0; n < 12; ++n) {
                    LAS char* p = lds + L_WSCR + (4 * g + q4) * 384 + n * 32 + p4 * 8;
                    st16c(rs, lane * 16, WS_WT + (kap * 36 + th * 12 + n) * 1024, __builtin_bit_cast(u32x4, cat8(trread(p), trread(p + 6144))));
                }
            }
            if (pj == 0) st16c(rs, lane * 16, WS_ST1 + pi * 1024, *(LAS u32x4*)(lds + L_ROWST + lane * 16));
            BAR();
            STAMP(2, 0);
            {
                LAS char* p = lds + L_X4 + (lane >> 5) * 4096 + (lane & 31) * 16;
                const f32x4 s4 = *(LAS f32x4*)p + *(LAS f32x4*)(p + 2048);
                st16c(rs, lane * 16, WS_ST2 + (pi * 16 + pj) * 1024, __builtin_bit_cast(u32x4, s4));
            }
            BAR();
            WAIT_V(0);
            if (lane == 0) stflag(ws + WS_FS + (pi * 16 + pj) * 8, tag);
            STAMP(2, 1);
            int bi = 11;
            unsigned spins = 0;
            for (;;) {
                const u64 v = ldflag(ws + WS_FS + pi * 128 + fr * 8);
                if (bi < 18) { BAR(); ++bi; }
                if (allset(v, tag) || ++spins > SPIN_CAP) break;
            }
            asm volatile("" ::: "memory");
            STAMP(2, 2);
            {
                u32x4 pv[16];
#pragma unroll
                for (int j = 0; j < 16; ++j) pv[j] = ld16c(rs, lane * 16, WS_ST2 + (pi * 16 + j) * 1024);
                if (bi < 18) { BAR(); ++bi; }
                f32x4 s4 = __builtin_bit_cast(f32x4, pv[0]);
#pragma unroll
                for (int j = 1; j < 16; ++j) s4 += __builtin_bit_cast(f32x4, pv[j]);
                const f32x2 c0 = ln_consts(s4[0], s4[1], 1.0f / FH), c1 = ln_consts(s4[2], s4[3], 1.0f / FH);
                *(LAS f32x4*)(lds + L_ZST + lane * 16) = (f32x4){c0.y, c1.y, -c0.x * c0.y, -c1.x * c1.y};
                WAIT_L0();
                if (lane == 0) *(volatile LAS unsigned*)(lds + M_READY) = 1u;
                WAIT_L0();
            }
            STAMP(2, 3);
            {
                const __amdgpu_buffer_rsrc_t rh = in_rsrc(hk, SZ_X);
                bl4(rh, lds + L_DUMP, lane * 128, b * 9216);
                bl4(rh, lds + L_DUMP, (lane & 7) * 128, b * 9216 + 8192);
            }
            for (; bi < 19; ++bi) BAR();
            WAIT_VB(0);
        }
        if (w >= 2) {
            const char* fz = ws + WS_FZ + pj * 128 + fr * 8;
            bool ok;
            unsigned spins = 0;
            for (;;) {
                ok = allset(ldflag(fz), tag);
                if (ok || *(volatile LAS unsigned*)(lds + M_HINT) != 0u || ++spins > SPIN_CAP) break;
            }
            asm volatile("" ::: "memory");
            int next = 20;
            if (ok) { if (w == 3) STAMP(2, 4); stageB(next++); }
            BAR();
            if (!ok) { ok = allset(ldflag(fz), tag); asm volatile("" ::: "memory"); if (ok && w == 3) STAMP(2, 4); }
            if (ok) for (; next < 22; ++next) stageB(next);
            BAR();
            if (!ok) { spin_flags(fz, tag); if (w == 3) STAMP(2, 4); }
            for (; next < 23; ++next) stageB(next);
            if (b == 0 && w == 3 && lane == 0) stflag(ws + WS_E, tag);
        }
        const int cd = w < 2 ? 2 * b + w : 512 + b;
        const bool hasconv = w < 2 || (w == 2 && b < 64);
#pragma unroll 1
        for (int t = 20; t < 43; ++t) {
            if (w < 2) WAIT_V(16); else WAIT_V(18);
            BAR();
            if (t == 20 && w == 3 && lane == 0) stflag(ws + WS_FK + ((b & 7) * 32 + (b >> 3)) * 8, tag);
            if (w < 2) stageA(t + 3); else stageB(t + 3);
        }
        if (w < 2) WAIT_V(16); else WAIT_V(18);
        BAR();
        const char* fk = ws + WS_FK + ((hasconv ? cd : 0) / 72) * 256 + (lane & 31) * 8;
        u64 fkv;
        asm volatile("global_load_dwordx2 %0, %1, off sc1" : "=v"(fkv) : "v"(fk) : "memory");
#pragma unroll 1
        for (int t = 44; t < 46; ++t) {
            if (w < 2) stageA(t + 2); else stageB(t + 2);
            if (w < 2) WAIT_V(16); else WAIT_V(18);
            BAR();
        }
        asm volatile("" : "+v"(fkv));
        if (!allset(fkv, tag)) spin_flags(fk, tag);
        asm volatile("" ::: "memory");
        ConvIn cin;
        if (hasconv) conv_load(cin, hk, dv, rs, cd, lane);
        SCHED0();
        if (w < 2) stageA(48); else stageB(48);
        if (w < 2) WAIT_V(16 + 12); else WAIT_V(18);
        BAR();
        if (w < 2) stageA(49); else stageB(49);
        if (w < 2) WAIT_V(16); else WAIT_V(18);
        BAR();
        bf16x8 bq[RB][5];
        G3_PRE();
        WAIT_V(8 + 5 * RB);
        BAR();
        WAIT_VB(5 * RB);
        if (hasconv) conv_stage(cin, lds + CV_BASE + w * CV_WAVE, lane);
        if (w == 0) STAMP(3, 3);
        BAR();
        BAR();
        BAR();
        if (w == 0) STAMP(3, 0);
        if (hasconv) conv_run(cin.dd, rs, ws, lds + CV_BASE + w * CV_WAVE, cd, lane);
        SCHED0();
        G3_MAIN(3, { WAIT_V(0); if (hasconv && lane == 0) stflag(ws + WS_FY + cd * 8, tag); });
    } else {
        if (wid == 0) STAMP(0, 0);
        const int wm = wid & 1, wn = wid >> 1, nb = wn * 5;
        if (tid < 6) *(LAS unsigned*)(lds + M_READY + tid * 4) = 0u;
        {
            const char* pc; unsigned len;
            asm volatile("s_getpc_b64 %0\n.Lpf_here%=:\n s_mov_b32 %1, .Lmega_tail+1024-.Lpf_here%=" : "=s"(pc), "=s"(len));
            unsigned d0_, d1_;
            const unsigned o0 = tid * 128, o1 = o0 + 32768;
            asm volatile("global_load_dword %0, %1, %2" : "=v"(d0_) : "v"(o0 < len - 4 ? o0 : 0u), "s"(pc));
            asm volatile("global_load_dword %0, %1, %2" : "=v"(d1_) : "v"(o1 < len - 4 ? o1 : 0u), "s"(pc));
            asm volatile("s_waitcnt vmcnt(0)" : "+v"(d0_), "+v"(d1_));
        }
        const int swz = (g ^ ((fr >> 1) & 7)) << 4;
        const int a01 = (64 * wm + 32 * wn + fr) * 128, a23 = (64 * wm + 32 * (1 - wn) + fr) * 128;
        const int aX01 = a01 + (swz ^ (wn * 64)), aX23 = a23 + (swz ^ (wn * 64)), aY01 = a01 + (swz ^ (64 - wn * 64)), aY23 = a23 + (swz ^ (64 - wn * 64));
        const int bb = SL_A + (q4 + 4 * (g & 1) + 16 * (g >> 1)) * 288 + p4 * 8;
        const int bX01 = bb + (nb + 2 * wm) * 32 + wn * 9216, bX23 = bb + (nb + 2 - 2 * wm) * 32 + wn * 9216, bX4 = bb + 128 + wn * 9216;
        const int bY01 = bb + (nb + 2 * wm) * 32 + (1 - wn) * 9216, bY23 = bb + (nb + 2 - 2 * wm) * 32 + (1 - wn) * 9216;
        f32x4 rsm[2], rq[2], cs[3];
#define ZACC() do { _Pragma("unroll") for (int m = 0; m < 4; ++m) _Pragma("unroll") for (int n = 0; n < 5; ++n) acc[m][n] = zero4; } while (0)
        ZACC();
        rsm[0] = rsm[1] = rq[0] = rq[1] = cs[0] = cs[1] = cs[2] = zero4;
        const bf16x8 ones = {0x3f80, 0x3f80, 0x3f80, 0x3f80, 0x3f80, 0x3f80, 0x3f80, 0x3f80};
        bf16x8 ax[4], bx[5], ay[4], by[4];
#define RD(p) (*(LAS bf16x8*)(p))
#define XOFF(c) ((c) < 2 ? (c) * RED_W : (c) == 2 ? 3 * RED_W + 16384 : 4 * RED_W)
#define TRF(p) cat8(trread(p), trread((p) + 2304))
#define READXT(sb) do { ax[0] = RD((sb) + aX01); bx[0] = TRF((sb) + bX01); ax[1] = RD((sb) + aX01 + 2048); ax[2] = RD((sb) + aX23); ax[3] = RD((sb) + aX23 + 2048); \
        bx[1] = TRF((sb) + bX01 + 32); bx[2] = TRF((sb) + bX23); bx[3] = TRF((sb) + bX23 + 32); bx[4] = TRF((sb) + bX4); } while (0)
#define READYT(sb) do { ay[0] = RD((sb) + aY01); by[0] = TRF((sb) + bY01); ay[1] = RD((sb) + aY01 + 2048); ay[2] = RD((sb) + aY23); ay[3] = RD((sb) + aY23 + 2048); \
        by[1] = TRF((sb) + bY01 + 32); by[2] = TRF((sb) + bY23); by[3] = TRF((sb) + bY23 + 32); } while (0)
#define Z2_STATS(A, B) do { rsm[0] = mfma16(ones, A[0], rsm[0]); rq[0] = mfma16(A[0], A[0], rq[0]); rsm[1] = mfma16(ones, A[1], rsm[1]); rq[1] = mfma16(A[1], A[1], rq[1]); \
        cs[0] = mfma16(ones, B[0], cs[0]); cs[1] = mfma16(ones, B[1], cs[1]); } while (0)
#define Z2_MMAX() do { _Pragma("unroll") for (int l = 0; l < 5; ++l) _Pragma("unroll") for (int m = 0; m < 4; ++m) acc[m][l] = mfma16(ax[m], bx[l], acc[m][l]); \
        Z2_STATS(ax, bx); cs[2] = mfma16(ones, bx[4], cs[2]); } while (0)
#define Z2_MMAY() do { _Pragma("unroll") for (int l = 0; l < 4; ++l) _Pragma("unroll") for (int m = 0; m < 4; ++m) acc[m][l] = mfma16(ay[m], by[l], acc[m][l]); \
        Z2_STATS(ay, by); } while (0)
#define MMAX() do { _Pragma("unroll") for (int l = 0; l < 5; ++l) _Pragma("unroll") for (int m = 0; m < 4; ++m) acc[m][l] = mfma16(bx[l], ax[m], acc[m][l]); } while (0)
#define MMAY() do { _Pragma("unroll") for (int l = 0; l < 4; ++l) _Pragma("unroll") for (int m = 0; m < 4; ++m) acc[m][l] = mfma16(by[l], ay[m], acc[m][l]); } while (0)
#define MMAX0() do { _Pragma("unroll") for (int l = 0; l < 5; ++l) _Pragma("unroll") for (int m = 0; m < 4; ++m) acc[m][l] = mfma16(bx[l], ax[m], zero4); } while (0)
#define Z1_MMAX() do { MMAX(); cs[0] = mfma16(bx[0], ones, cs[0]); cs[1] = mfma16(bx[1], ones, cs[1]); cs[2] = mfma16(bx[4], ones, cs[2]); } while (0)
#define Z1_MMAX0() do { MMAX0(); cs[0] = mfma16(bx[0], ones, zero4); cs[1] = mfma16(bx[1], ones, zero4); cs[2] = mfma16(bx[4], ones, zero4); } while (0)
#define PINCS() asm volatile("" : "+v"(cs[0]), "+v"(cs[1]), "+v"(cs[2]))
#define Z1_MMAY() do { MMAY(); cs[0] = mfma16(by[0], ones, cs[0]); cs[1] = mfma16(by[1], ones, cs[1]); } while (0)
        TAG_READY();
        BAR();
        if (wid == 0) { STAMP(0, 1); STAMPC(0, 14); }
        READXT(lds);
#pragma unroll 1
        for (int t = 0; t < 8; ++t) {
            LAS char* sb = lds + (t & 3) * SLOT;
            LAS char* sn = lds + ((t + 1) & 3) * SLOT;
            READYT(sb);
            Z2_MMAX();
            SCHED_IL(12, 27);
            WAIT_L0();
            BAR();
            READXT(sn);
            Z2_MMAY();
            SCHED_IL(14, 22);
        }
        READYT(lds + opqs(0));
        Z2_MMAX();
        SCHED_IL(12, 27);
        LAS char* xb = lds + L_X4 + wm * 4096 + lane * 16;
        *(LAS f32x4*)(xb + wn * 2048) = acc[2][4];
        *(LAS f32x4*)(xb + wn * 2048 + 1024) = acc[3][4];
        Z2_MMAY();
        SCHED0();
        if (wid == 0) STAMP(0, 2);
        if ((fr >> 2) == g) {
#pragma unroll
            for (int i = 0; i < 2; ++i) {
                const float sq = (fr & 2) ? ((fr & 1) ? rq[i][3] : rq[i][2]) : ((fr & 1) ? rq[i][1] : rq[i][0]);
                const f32x2 c = ln_consts(rsm[i][0], sq, 1.0f / D);
                LAS float* p = (LAS float*)(lds + L_ROWST + (32 * wm + 16 * wn + 4 * g + 2 * i + ((fr >> 1) & 1)) * 16 + (fr & 1) * 4);
                p[0] = c.x; p[2] = c.y;
            }
        }
        if (g == 0) {
            LAS float* c = (LAS float*)(lds + L_CS);
            c[(nb + 2 * wm) * 16 + fr] = cs[0][0];
            c[(nb + 2 * wm + 1) * 16 + fr] = cs[1][0];
            if (wm == 0) c[(wn ? 9 : 4) * 16 + fr] = cs[2][0];
        }
        WAIT_L0();
        BAR();
        acc[0][4] += *(LAS f32x4*)(xb + (1 - wn) * 2048);
        acc[1][4] += *(LAS f32x4*)(xb + (1 - wn) * 2048 + 1024);
        u32x4 zp[9];
        {
            float cc[5];
            LAS float* c = (LAS float*)(lds + L_CS) + fr;
            cc[0] = c[(nb + 2 * wm) * 16]; cc[1] = c[(nb + 2 * wm + 1) * 16]; cc[2] = c[(nb + 2 - 2 * wm) * 16]; cc[3] = c[(nb + 3 - 2 * wm) * 16]; cc[4] = c[4 * 16] + c[9 * 16];
#pragma unroll
            for (int ip = 0; ip < 2; ++ip) {
                const int mp = ip ^ wn;
                f32x4 st[4];
#pragma unroll
                for (int j = 0; j < 4; ++j) st[j] = *(LAS f32x4*)(lds + L_ROWST + (32 * wm + 16 * mp + 4 * g + j) * 16);
                f32x2 sum[4], sq[4];
#pragma unroll
                for (int l = 0; l < (ip == 0 ? 5 : 4); ++l) {
#pragma unroll
                    for (int j = 0; j < 4; ++j) {
                        const f32x4 a = acc[2 * ip + (j >> 1)][l];
                        const f32x2 z = gelu2(fma2(lo2(st[j]), (f32x2){-cc[l], -cc[l]}, (j & 1) ? hi2(a) : lo2(a)) * hi2(st[j]));
                        zp[ip * 5 + l][j] = pk2v(z);
                        if (l == 0) { sum[j] = z; sq[j] = z * z; } else { sum[j] += z; sq[j] = fma2(z, z, sq[j]); }
                    }
                }
                float s0 = sum[0].x, s1 = sq[0].x, s2 = sum[0].y, s3 = sq[0].y, s4 = sum[1].x, s5 = sq[1].x, s6 = sum[1].y, s7 = sq[1].y;
                asm volatile(
                    "s_nop 1\n"
                    "v_add_f32_dpp %0, %0, %0 row_mirror row_mask:0xf bank_mask:0x3\n v_add_f32_dpp %1, %1, %1 row_mirror row_mask:0xf bank_mask:0x3\n"
                    "v_add_f32_dpp %2, %2, %2 row_mirror row_mask:0xf bank_mask:0x3\n v_add_f32_dpp %3, %3, %3 row_mirror row_mask:0xf bank_mask:0x3\n"
                    "v_add_f32_dpp %4, %4, %4 row_mirror row_mask:0xf bank_mask:0x3\n v_add_f32_dpp %5, %5, %5 row_mirror row_mask:0xf bank_mask:0x3\n"
                    "v_add_f32_dpp %6, %6, %6 row_mirror row_mask:0xf bank_mask:0x3\n v_add_f32_dpp %7, %7, %7 row_mirror row_mask:0xf bank_mask:0x3\n"
                    "v_add_f32_dpp %0, %8, %8 row_mirror row_mask:0xf bank_mask:0xc\n v_add_f32_dpp %1, %9, %9 row_mirror row_mask:0xf bank_mask:0xc\n"
                    "v_add_f32_dpp %2, %10, %10 row_mirror row_mask:0xf bank_mask:0xc\n v_add_f32_dpp %3, %11, %11 row_mirror row_mask:0xf bank_mask:0xc\n"
                    "v_add_f32_dpp %4, %12, %12 row_mirror row_mask:0xf bank_mask:0xc\n v_add_f32_dpp %5, %13, %13 row_mirror row_mask:0xf bank_mask:0xc\n"
                    "v_add_f32_dpp %6, %14, %14 row_mirror row_mask:0xf bank_mask:0xc\n v_add_f32_dpp %7, %15, %15 row_mirror row_mask:0xf bank_mask:0xc\n"
                    "v_add_f32_dpp %0, %0, %0 row_half_mirror row_mask:0xf bank_mask:0x5\n v_add_f32_dpp %1, %1, %1 row_half_mirror row_mask:0xf bank_mask:0x5\n"
                    "v_add_f32_dpp %2, %2, %2 row_half_mirror row_mask:0xf bank_mask:0x5\n v_add_f32_dpp %3, %3, %3 row_half_mirror row_mask:0xf bank_mask:0x5\n"
                    "v_add_f32_dpp %0, %4, %4 row_half_mirror row_mask:0xf bank_mask:0xa\n v_add_f32_dpp %1, %5, %5 row_half_mirror row_mask:0xf bank_mask:0xa\n"
                    "v_add_f32_dpp %2, %6, %6 row_half_mirror row_mask:0xf bank_mask:0xa\n v_add_f32_dpp %3, %7, %7 row_half_mirror row_mask:0xf bank_mask:0xa\n"
                    "v_add_f32_dpp %0, %0, %0 quad_perm:[1,0,3,2] row_mask:0xf bank_mask:0xf\n v_add_f32_dpp %1, %1, %1 quad_perm:[1,0,3,2] row_mask:0xf bank_mask:0xf\n"
                    "v_add_f32_dpp %2, %2, %2 quad_perm:[1,0,3,2] row_mask:0xf bank_mask:0xf\n v_add_f32_dpp %3, %3, %3 quad_perm:[1,0,3,2] row_mask:0xf bank_mask:0xf\n"
                    "v_add_f32_dpp %0, %0, %0 quad_perm:[2,3,0,1] row_mask:0xf bank_mask:0xf\n v_add_f32_dpp %1, %1, %1 quad_perm:[2,3,0,1] row_mask:0xf bank_mask:0xf\n"
                    "v_add_f32_dpp %2, %2, %2 quad_perm:[2,3,0,1] row_mask:0xf bank_mask:0xf\n v_add_f32_dpp %3, %3, %3 quad_perm:[2,3,0,1] row_mask:0xf bank_mask:0xf\n"
                    : "+v"(s0), "+v"(s1), "+v"(s2), "+v"(s3), "+v"(s4), "+v"(s5), "+v"(s6), "+v"(s7)
                    : "v"(sum[2].x), "v"(sq[2].x), "v"(sum[2].y), "v"(sq[2].y), "v"(sum[3].x), "v"(sq[3].x), "v"(sum[3].y), "v"(sq[3].y));
                if (p4 == 0) *(LAS f32x4*)(lds + L_X4 + wm * 4096 + (1 - wn) * 2048 + (16 * mp + 4 * g + q4) * 16) = (f32x4){s0, s1, s2, s3};
            }
        }
        PIN9(zp);
        if (wid == 0) STAMP(0, 3);
        WAIT_L0();
        BAR();
        const unsigned zdst = WS_Z2N + ((pj * 32 + pi * 2 + wm) * 18) * 1024;
        auto normalise = [&]() {
#pragma unroll
            for (int ip = 0; ip < 2; ++ip) {
                const int mp = ip ^ wn;
                f32x4 st[4];
#pragma unroll
                for (int j = 0; j < 4; ++j) st[j] = *(LAS f32x4*)(lds + L_ZST + (32 * wm + 16 * mp + 4 * g + j) * 16);
#pragma unroll
                for (int l = 0; l < (ip == 0 ? 5 : 4); ++l) {
#pragma unroll
                    for (int j = 0; j < 4; ++j) zp[ip * 5 + l][j] = pk2v(fma2(up2(zp[ip * 5 + l][j]), lo2(st[j]), hi2(st[j])));
                    const int n = l == 4 ? 4 : nb + ((l + 2 * wm) & 3);
                    st16c(rs, lane * 16, zdst + n * 2048 + mp * 1024, zp[ip * 5 + l]);
                }
            }
        };
        char* fzp = ws + WS_FZ + (pj * 16 + pi) * 8;
        int zs = -1;
        LAS char* sp = lds + opqs(SLOT);
        READXT(sp);
        READYT(sp);
        Z1_MMAX0();
        SCHED0();
        PINCS();
        WAIT_L0();
        BAR();
        sp = lds + opqs(2 * SLOT);
        READXT(sp);
        Z1_MMAY();
        SCHED_IL(14, 18);
#pragma unroll 1
        for (int t = 10; t < 17; ++t) {
            LAS char* sb = lds + (t & 3) * SLOT;
            LAS char* sn = lds + ((t + 1) & 3) * SLOT;
            READYT(sb);
            Z1_MMAX();
            SCHED_IL(12, 23);
            PINCS();
            WAIT_L0();
            BAR();
            const unsigned rdy = *(volatile LAS unsigned*)(lds + M_READY);
            READXT(sn);
            Z1_MMAY();
            SCHED_IL(15, 18);
            if (zs < 0) {
                if (RFL(rdy) != 0u) { if (wid == 0) STAMP(1, 0); normalise(); zs = NDELAY; if (wid == 0) STAMP(1, 1); }
            } else if (zs > 0) {
                if (--zs == 0) { if (wid == 0) STAMP(1, 2); WAIT_V(0); signal4(lds, 0, fzp, lane, tag); if (wid == 0) STAMP(1, 3); }
            }
            SCHED0();
        }
        sp = lds + opqs(SLOT);
        READYT(sp);
        Z1_MMAX();
        SCHED_IL(12, 23);
        *(LAS f32x4*)(xb + wn * 2048) = acc[2][4];
        *(LAS f32x4*)(xb + wn * 2048 + 1024) = acc[3][4];
        Z1_MMAY();
        SCHED0();
        if (wid == 0) STAMP(0, 4);
        if (fr == 0) {
            LAS char* c = lds + L_CS + g * 16;
            *(LAS f32x4*)(c + (nb + 2 * wm) * 64) = cs[0];
            *(LAS f32x4*)(c + (nb + 2 * wm + 1) * 64) = cs[1];
            if (wm == 0) *(LAS f32x4*)(c + (wn ? 9 : 4) * 64) = cs[2];
        }
        if (zs == 1) { if (wid == 0) STAMP(1, 2); WAIT_V(0); signal4(lds, 0, fzp, lane, tag); zs = 0; if (wid == 0) STAMP(1, 3); }
        WAIT_L0();
        BAR();
        if (zs < 0) { if (wid == 0) STAMP(1, 0); normalise(); if (wid == 0) STAMP(1, 1); }
        SCHED0();
#pragma unroll
        for (int ip = 0; ip < 2; ++ip)
#pragma unroll
            for (int l = 0; l < (ip == 0 ? 5 : 4); ++l) {
                const int n = l == 4 ? 4 : nb + ((l + 2 * wm) & 3);
                *(LAS u32x4*)(lds + (2 + wm) * SLOT + SL_A + n * 2048 + (ip ^ wn) * 1024 + lane * 16) = zp[ip * 5 + l];
            }
        float bias[4];
#pragma unroll
        for (int m = 0; m < 4; ++m) bias[m] = bflo((unsigned)b_sp[pi * 128 + 64 * wm + 16 * ((m + 2 * wn) & 3) + fr]);
        SCHED0();
        acc[0][4] += *(LAS f32x4*)(xb + (1 - wn) * 2048);
        acc[1][4] += *(LAS f32x4*)(xb + (1 - wn) * 2048 + 1024);
        {
            f32x4 cc[5];
            LAS char* c = lds + L_CS + g * 16;
            cc[0] = cs[0]; cc[1] = cs[1]; cc[2] = *(LAS f32x4*)(c + (nb + 2 - 2 * wm) * 64); cc[3] = *(LAS f32x4*)(c + (nb + 3 - 2 * wm) * 64);
            cc[4] = *(LAS f32x4*)(c + 4 * 64) + *(LAS f32x4*)(c + 9 * 64);
#pragma unroll
            for (int m = 0; m < 4; ++m) {
                LAS float* p = (LAS float*)(lds + L_ROWST + (32 * wm + 8 * ((m + 2 * wn) & 3) + (fr >> 1)) * 16 + (fr & 1) * 4);
                const float mb = -p[0], rd = p[2];
#pragma unroll
                for (int l = 0; l < (m < 2 ? 5 : 4); ++l) {
                    const unsigned z0 = pk2v(gelu2(fma2(lo2(cc[l]), (f32x2){mb, mb}, lo2(acc[m][l])) * rd));
                    const unsigned z1 = pk2v(gelu2(fma2(hi2(cc[l]), (f32x2){mb, mb}, hi2(acc[m][l])) * rd));
                    if (l < 4) { zp[m * 2 + (l >> 1)][(l & 1) * 2] = z0; zp[m * 2 + (l >> 1)][(l & 1) * 2 + 1] = z1; }
                    else { zp[8][m * 2] = z0; zp[8][m * 2 + 1] = z1; }
                }
            }
        }
        PIN9(zp);
        SCHED0();
        if (wid == 0) STAMP(0, 5);
        if (zs != 0) { if (wid == 0) STAMP(1, 2); WAIT_V(0); signal4(lds, 0, fzp, lane, tag); if (wid == 0) STAMP(1, 3); }
        asm volatile("" : "+v"(bias[0]), "+v"(bias[1]), "+v"(bias[2]), "+v"(bias[3]));
        if (tid == 0) *(volatile LAS unsigned*)(lds + M_HINT) = 1u;
        const int fb = SL_A + lane * 16;
        const int fX01 = fb + (nb + 2 * wm) * 2048 + wn * 1024, fX23 = fb + (nb + 2 - 2 * wm) * 2048 + wn * 1024, fX4 = fb + 4 * 2048 + wn * 1024;
        const int fY01 = fb + (nb + 2 * wm) * 2048 + (1 - wn) * 1024, fY23 = fb + (nb + 2 - 2 * wm) * 2048 + (1 - wn) * 1024;
#define READXF(sb) do { bx[0] = RD((sb) + fX01); ax[0] = RD((sb) + aX01); ax[1] = RD((sb) + aX01 + 2048); ax[2] = RD((sb) + aX23); ax[3] = RD((sb) + aX23 + 2048); \
        bx[1] = RD((sb) + fX01 + 2048); bx[2] = RD((sb) + fX23); bx[3] = RD((sb) + fX23 + 2048); bx[4] = RD((sb) + fX4); } while (0)
#define READYF(sb) do { by[0] = RD((sb) + fY01); ay[0] = RD((sb) + aY01); ay[1] = RD((sb) + aY01 + 2048); ay[2] = RD((sb) + aY23); ay[3] = RD((sb) + aY23 + 2048); \
        by[1] = RD((sb) + fY01 + 2048); by[2] = RD((sb) + fY23); by[3] = RD((sb) + fY23 + 2048); } while (0)
        if (wid == 0) STAMP(0, 6);
        WAIT_L0();
        BAR();
        if (wid == 0) { STAMP(0, 7); STAMPC(1, 14); }
        sp = lds + opqs(2 * SLOT);
        READXF(sp);
        READYF(sp);
        MMAX0();
        SCHED0();
        WAIT_L0();
        BAR();
        sp = lds + opqs(3 * SLOT);
        READXF(sp);
        MMAY();
        SCHED_IL(9, 16);
#pragma unroll 1
        for (int t = 19; t < 49; ++t) {
            LAS char* sb = lds + (t & 3) * SLOT;
            LAS char* sn = lds + ((t + 1) & 3) * SLOT;
            READYF(sb);
            MMAX();
            SCHED_IL(8, 20);
            WAIT_L0();
            BAR();
            READXF(sn);
            MMAY();
            SCHED_IL(9, 16);
            SCHED0();
        }
        if (wid == 0) { STAMP(0, 9); STAMPC(1, 15); }
        sp = lds + opqs(SLOT);
        READYF(sp);
        MMAX();
        SCHED_IL(8, 20);
        {
            LAS char* p = lds + XOFF(wid ^ 2) + lane * 16;
            *(LAS f32x4*)p = acc[2][4];
            *(LAS f32x4*)(p + 1024) = acc[3][4];
        }
        MMAY();
        SCHED0();
        if (wid == 0) { STAMP(0, 11); STAMPC(0, 15); }
        WAIT_L0();
        BAR();
        acc[0][4] += *(LAS f32x4*)(lds + XOFF(wid) + lane * 16);
        acc[1][4] += *(LAS f32x4*)(lds + XOFF(wid) + 1024 + lane * 16);
        {
            const int kap0 = 9 * (pj >> 1) + 4 * (pj & 1) + 2 * wn, kap4 = 9 * (pj >> 1) + 8;
#pragma unroll
            for (int m = 0; m < 4; ++m) {
                const unsigned dst = WS_P + (pi * 8 + 4 * wm + ((m + 2 * wn) & 3)) * (72 * 1024);
                const f32x2 bs = {bias[m], bias[m]};
#pragma unroll
                for (int lp = 0; lp < 2; ++lp) {
                    const u32x4 z = zp[m * 2 + lp];
                    const f32x4 a = acc[m][2 * lp], c = acc[m][2 * lp + 1];
                    st16c(rs, lane * 16, dst + (kap0 + (lp ^ wm)) * 1024, (u32x4){pk2v(up2(z[0]) * (lo2(a) + bs)), pk2v(up2(z[1]) * (hi2(a) + bs)), pk2v(up2(z[2]) * (lo2(c) + bs)), pk2v(up2(z[3]) * (hi2(c) + bs))});
                }
                if (m < 2) st8c(rs, lane * 16, dst + kap4 * 1024 + (pj & 1) * 8, (u32x2){pk2v(up2(zp[8][2 * m]) * (lo2(acc[m][4]) + bs)), pk2v(up2(zp[8][2 * m + 1]) * (hi2(acc[m][4]) + bs))});
            }
        }
        if (wid == 0) STAMP(4, 0);
        BAR();
        WAIT_V(0);
        signal4(lds, 2, ws + WS_FP + (pi * 16 + pj) * 8, lane, tag);
        if (wid == 0) STAMP(4, 1);
        SCHED0();
        bf16x8 bq[RB][5];
        G3_PRE();
        G3_MAIN(4, {});
    }
    {
        const int lane = opqv(tid) & 63, fr = lane & 15, g = lane >> 4;
        const int m = wid >> 1, l0 = 2 * (wid & 1);
        const int tA = 64 * rg + 16 * m + fr, dA = 16 * (n30 + l0) + 4 * g;
        const int tH = 64 * rg + 32 * (wid & 1) + 16 * (g >> 1) + fr, dH = 16 * n34 + 8 * (ct & 1) + 4 * (g & 1);
        const char* fy = ws + WS_FY + (lane < 32 ? 16 * (n30 + l0) + lane : 16 * n34 + 8 * (ct & 1) + (lane & 7)) * 8;
        const u64 fyv = ldflag(fy);
        u32x2 xv[3]; unsigned yv[3][4]; float mb[2], rd[2];
        xv[0] = *(const u32x2*)((const char*)x + (unsigned)((tA * D + dA) * 2));
        xv[1] = *(const u32x2*)((const char*)x + (unsigned)((tA * D + dA + 16) * 2));
        xv[2] = *(const u32x2*)((const char*)x + (unsigned)((tH * D + dH) * 2));
        mb[0] = __uint_as_float(ld4c(rs, WS_ST1 + (tA >> 1) * 16 + (tA & 1) * 4)); rd[0] = __uint_as_float(ld4c(rs, WS_ST1 + (tA >> 1) * 16 + (tA & 1) * 4 + 8));
        mb[1] = __uint_as_float(ld4c(rs, WS_ST1 + (tH >> 1) * 16 + (tH & 1) * 4)); rd[1] = __uint_as_float(ld4c(rs, WS_ST1 + (tH >> 1) * 16 + (tH & 1) * 4 + 8));
        SCHED0();
        LAS char* red = lds + (wid < 2 ? wid * RED_W : wid < 4 ? (wid + 1) * RED_W : wid < 7 ? CV_BASE + (wid - 4) * CV_WAVE : 2 * RED_W);
#pragma unroll
        for (int mm = 0; mm < 2; ++mm)
#pragma unroll
            for (int l = 0; l < 4; ++l) *(LAS f32x4*)(red + (mm * 4 + l) * 1024 + lane * 16) = acc[mm][l];
        SCHED0();
        if (!allset(fyv, tag)) spin_flags(fy, tag);
        asm volatile("" ::: "memory");
#pragma unroll
        for (int rr = 0; rr < 4; ++rr) {
            yv[0][rr] = ld2c(rs, (dA * S + tA) * 2, WS_YT + rr * (S * 2));
            yv[1][rr] = ld2c(rs, (dA * S + tA) * 2, WS_YT + (16 + rr) * (S * 2));
            yv[2][rr] = ld2c(rs, (dH * S + tH) * 2, WS_YT + rr * (S * 2));
        }
        SCHED0();
#pragma unroll
        for (int mm = 2; mm < 4; ++mm)
#pragma unroll
            for (int l = 0; l < 4; ++l) *(LAS f32x4*)(red + (mm * 4 + l) * 1024 + lane * 16) = acc[mm][l];
        if ((g >> 1) == (ct & 1)) {
#pragma unroll
            for (int mm = 0; mm < 4; ++mm) *(LAS f32x4*)(red + 16384 + mm * 512 + (lane & 31) * 16) = acc[mm][4];
        }
        if (wid == 0) STAMP(4, 9);
        asm volatile("s_waitcnt lgkmcnt(0)\n s_barrier\n.Lmega_tail:" ::: "memory");
        if (wid == 0) STAMP(4, 10);
#pragma unroll
        for (int u = 0; u < 3; ++u) {
            if (u == 2 && wid >= 2) break;
            const int off = (u < 2 ? (2 * wid + u) * 1024 : 16384 + wid * 1024) + lane * 16;
            f32x4 o = zero4;
#pragma unroll
            for (int v = 0; v < 4; ++v) o += *(LAS f32x4*)(lds + v * RED_W + off) + *(LAS f32x4*)(lds + (v < 3 ? CV_BASE + v * CV_WAVE : 4 * RED_W) + off);
            float res[4];
#pragma unroll
            for (int rr = 0; rr < 4; ++rr) {
                const float ob = rbf(o[rr]);
                const float sg = rbf(ob * __builtin_amdgcn_rcpf(1.0f + __builtin_amdgcn_exp2f(-1.4426950408889634f * ob)));
                const float xx = (rr & 1) ? bfhi(xv[u][rr >> 1]) : bflo(xv[u][rr >> 1]);
                res[rr] = xx + rbf(rbf(rbf(rbf(xx - mb[u >> 1]) * rd[u >> 1]) * bflo(yv[u][rr])) * sg);
            }
            const unsigned oo = u < 2 ? (tA * D + dA + 16 * u) * 2 : (tH * D + dH) * 2;
            *(u32x2*)((char*)out + oo) = (u32x2){pk2(res[0], res[1]), pk2(res[2], res[3])};
        }
        if (wid == 0) { WAIT_V(0); STAMP(4, 11); }
    }
}

extern "C" void kernel_launch(void* const* d_in, const int* in_sizes, int n_in,
                              void* d_out, int out_size, void* d_ws, size_t ws_size,
                              hipStream_t stream) {
    (void)hipFuncSetAttribute((const void*)mega, hipFuncAttributeMaxDynamicSharedMemorySize, LDS_SZ);
    mega<<<256, 512, LDS_SZ, stream>>>((const u16*)d_in[0], (const u16*)d_in[1], (const u16*)d_in[2], (const u16*)d_in[3], (const u16*)d_in[4], (const u16*)d_in[5],
                                       (const u16*)d_in[7], (const u16*)d_in[8], (const u16*)d_in[9], (const u16*)d_in[10], (char*)d_ws, (u16*)d_out);
}
```

```cpp
#include <hip/hip_runtime.h>
#include <stdint.h>
#define CODE_PF

#define LAS __attribute__((address_space(3)))
#define GAS __attribute__((address_space(1)))
typedef unsigned short u16;
typedef unsigned long long u64;
typedef short bf16x8 __attribute__((ext_vector_type(8)));
typedef short bf16x4 __attribute__((ext_vector_type(4)));
typedef float f32x4 __attribute__((ext_vector_type(4)));
typedef float f32x2 __attribute__((ext_vector_type(2)));
typedef unsigned u32x4 __attribute__((ext_vector_type(4)));
typedef unsigned u32x2 __attribute__((ext_vector_type(2)));
typedef __bf16 bf16x2_t __attribute__((ext_vector_type(2)));

constexpr int S = 2048, D = 576, F = 4608, FH = 2304;

__device__ __forceinline__ float bflo(unsigned w) { return __uint_as_float(w << 16); }
__device__ __forceinline__ float bfhi(unsigned w) { return __uint_as_float(w & 0xffff0000u); }
__device__ __forceinline__ unsigned pk2v(f32x2 p) { return __builtin_bit_cast(unsigned, __builtin_convertvector(p, bf16x2_t)); }
__device__ __forceinline__ unsigned pk2(float lo, float hi) { return pk2v((f32x2){lo, hi}); }
__device__ __forceinline__ f32x2 up2(unsigned w) { return (f32x2){bflo(w), bfhi(w)}; }
__device__ __forceinline__ float rbf(float x) { return bflo(pk2(x, x)); }
__device__ __forceinline__ f32x2 rbf2(f32x2 x) { return up2(pk2v(x)); }
__device__ __forceinline__ f32x2 fma2(f32x2 a, f32x2 b, f32x2 c) { return __builtin_elementwise_fma(a, b, c); }
__device__ __forceinline__ f32x2 lo2(f32x4 a) { return (f32x2){a[0], a[1]}; }
__device__ __forceinline__ f32x2 hi2(f32x4 a) { return (f32x2){a[2], a[3]}; }
__device__ __forceinline__ f32x4 mfma16(bf16x8 a, bf16x8 b, f32x4 c) { return __builtin_amdgcn_mfma_f32_16x16x32_bf16(a, b, c, 0, 0, 0); }
__device__ __forceinline__ bf16x4 trread(LAS char* p) { return __builtin_bit_cast(bf16x4, __builtin_amdgcn_ds_read_tr16_b64_v4i16((LAS bf16x4*)p)); }
__device__ __forceinline__ bf16x8 cat8(bf16x4 a, bf16x4 b) { return __builtin_shufflevector(a, b, 0, 1, 2, 3, 4, 5, 6, 7); }
#define WAIT_V(n) asm volatile("s_waitcnt vmcnt(%0)" ::"n"(n) : "memory")
#define WAIT_VB(n) __builtin_amdgcn_s_waitcnt(((n) & 15) | (7 << 4) | (15 << 8) | (((n) >> 4) << 14))
#define WAIT_L0() asm volatile("s_waitcnt lgkmcnt(0)" ::: "memory")
#define BAR() do { asm volatile("" ::: "memory"); __builtin_amdgcn_s_barrier(); asm volatile("" ::: "memory"); } while (0)
#define SCHED0() __builtin_amdgcn_sched_barrier(0)
#define RLX_AGENT __ATOMIC_RELAXED, __HIP_MEMORY_SCOPE_AGENT
#define PIN9(z) asm volatile("" : "+v"(z[0]), "+v"(z[1]), "+v"(z[2]), "+v"(z[3]), "+v"(z[4]), "+v"(z[5]), "+v"(z[6]), "+v"(z[7]), "+v"(z[8]))
__device__ __forceinline__ int opqs(int x) { asm volatile("" : "+s"(x)); return x; }
__device__ __forceinline__ int opqv(int x) { asm volatile("" : "+v"(x)); return x; }
template <int I> struct IC { static constexpr int v = I; };
template <int A, int B, class Fn> __device__ __forceinline__ void sfor(Fn&& f) { if constexpr (A < B) { f(IC<A>{}); sfor<A + 1, B>(f); } }
#define SFOR(i, a, b) sfor<a, b>([&](auto i##_) __attribute__((always_inline)) { constexpr int i = decltype(i##_)::v;
#define SEND });
#define RFL(x) __builtin_amdgcn_readfirstlane(x)

#define TIMER "s_memrealtime"
#define STAMP(i) do { } while (0)

__device__ __forceinline__ f32x2 gelu2(f32x2 v) {
    const float c1 = -2.0f * 0.7978845608028654f * 1.4426950408889634f;
    const float c2 = c1 * 0.044715f;
    f32x2 a = v * fma2(v * v, (f32x2){c2, c2}, (f32x2){c1, c1});
    f32x2 d = (f32x2){__builtin_amdgcn_exp2f(a.x), __builtin_amdgcn_exp2f(a.y)} + (f32x2){1.0f, 1.0f};
    return v * (f32x2){__builtin_amdgcn_rcpf(d.x), __builtin_amdgcn_rcpf(d.y)};
}
__device__ __forceinline__ float adds(float a, float b) { float o; asm("v_add_f32 %0, %1, %2" : "=v"(o) : "v"(a), "v"(b)); return o; }
__device__ __forceinline__ float muls(float a, float b) { float o; asm("v_mul_f32 %0, %1, %2" : "=v"(o) : "v"(a), "v"(b)); return o; }
__device__ __forceinline__ f32x2 ln_consts(float sm, float sq, float inv_n) {
    float mean, m2;
    asm("v_mul_f32 %0, %2, %3\n v_mul_f32 %1, %2, %4" : "=&v"(mean), "=v"(m2) : "s"(inv_n), "v"(sm), "v"(sq));
    const float var = fmaxf(fmaf(-mean, mean, m2), 0.f);
    return (f32x2){rbf(mean), __builtin_amdgcn_rcpf(rbf(__builtin_amdgcn_sqrtf(rbf(rbf(var) + 1e-5f))))};
}

constexpr unsigned SZ_Z = S * FH * 2, SZ_X = S * D * 2;
constexpr unsigned WS_Z2N = 0;
constexpr unsigned WS_P = WS_Z2N + SZ_Z;
constexpr unsigned WS_KVT = WS_P + SZ_Z;
constexpr unsigned WS_YT = WS_KVT + SZ_X;
constexpr unsigned WS_WT = WS_YT + SZ_X;
constexpr unsigned WS_ST2 = WS_WT + FH * D * 2;
constexpr unsigned WS_ST1 = WS_ST2 + 256 * 1024;
constexpr unsigned WS_SR = WS_ST1 + 16384;
constexpr unsigned WS_SC = WS_SR + 16 * 12288;
constexpr unsigned WS_FZ = WS_SC + 32 * 12288;
constexpr unsigned WS_FP = WS_FZ + 8192;
constexpr unsigned WS_FK = WS_FP + 8192;
constexpr unsigned WS_FY = WS_FK + 2048;
constexpr unsigned WS_E = WS_FY + 4608 + 128;
constexpr unsigned WS_END = WS_E + 128;
constexpr u64 TAG_INC = 0x9E3779B97F4A7C15ull;

__device__ __forceinline__ __amdgpu_buffer_rsrc_t ws_rsrc(char* ws) { return __builtin_amdgcn_make_buffer_rsrc(ws, 0, (int)WS_END, 0x00020000); }
__device__ __forceinline__ void st16c(__amdgpu_buffer_rsrc_t r, unsigned off, unsigned soff, u32x4 v) { __builtin_amdgcn_raw_buffer_store_b128(v, r, off, soff, 16); asm volatile("s_nop 1" :: "v"(v)); }
__device__ __forceinline__ void st8c(__amdgpu_buffer_rsrc_t r, unsigned off, unsigned soff, u32x2 v) { __builtin_amdgcn_raw_buffer_store_b64(v, r, off, soff, 16); }
__device__ __forceinline__ u32x4 ld16c(__amdgpu_buffer_rsrc_t r, unsigned off, unsigned soff) { return __builtin_amdgcn_raw_buffer_load_b128(r, off, soff, 16); }
__device__ __forceinline__ unsigned ld4c(__amdgpu_buffer_rsrc_t r, unsigned off) { return __builtin_amdgcn_raw_buffer_load_b32(r, off, 0, 16); }
__device__ __forceinline__ unsigned ld2c(__amdgpu_buffer_rsrc_t r, unsigned off, unsigned soff) { return __builtin_amdgcn_raw_buffer_load_b16(r, off, soff, 16); }
__device__ __forceinline__ __amdgpu_buffer_rsrc_t in_rsrc(const void* p, unsigned bytes) { return __builtin_amdgcn_make_buffer_rsrc((void*)p, 0, (int)bytes, 0x00020000); }
__device__ __forceinline__ u32x4 mk_rsrc(const void* p, unsigned bytes) { const u64 a = (u64)p; return (u32x4){(unsigned)a, (unsigned)(a >> 32) & 0xffffu, bytes, 0x00020000u}; }
#define DMA_HEAD "s_add_i32 m0, %1, %5\n s_add_i32 %0, %4, %6\n buffer_load_dwordx4 %2, %3, %0 offen"
#define DMA_NEXT(o) "\n buffer_load_dwordx4 %2, %3, %0 offen offset:" #o " sc1 lds"
#define DMA_OPS : "=&s"(t) : "s"(lb), "v"(voff), "s"(r), "s"(sb), "n"(LI), "n"(SI) : "scc", "memory"
template <int LI, int SI> __device__ __forceinline__ void dma16(u32x4 r, unsigned lb, unsigned voff, unsigned sb) { unsigned t; asm volatile(DMA_HEAD " lds" DMA_OPS); }
template <int LI, int SI> __device__ __forceinline__ void dma16c(u32x4 r, unsigned lb, unsigned voff, unsigned sb) { unsigned t; asm volatile(DMA_HEAD " sc1 lds" DMA_OPS); }
template <int LI, int SI> __device__ __forceinline__ void dma16c3(u32x4 r, unsigned lb, unsigned voff, unsigned sb) { unsigned t; asm volatile(DMA_HEAD " sc1 lds" DMA_NEXT(1024) DMA_NEXT(2048) DMA_OPS); }
template <int LI, int SI> __device__ __forceinline__ void dma16c4(u32x4 r, unsigned lb, unsigned voff, unsigned sb) { unsigned t; asm volatile(DMA_HEAD " sc1 lds" DMA_NEXT(1024) DMA_NEXT(2048) DMA_NEXT(3072) DMA_OPS); }
template <int SI> __device__ __forceinline__ void pf4(u32x4 r, unsigned lb, unsigned voff, unsigned sb) {
    unsigned t;
    asm volatile("s_mov_b32 m0, %1\n s_add_i32 %0, %4, %5\n buffer_load_dword %2, %3, %0 offen lds" : "=&s"(t) : "s"(lb), "v"(voff), "s"(r), "s"(sb), "n"(SI) : "scc", "memory");
}
template <int SI> __device__ __forceinline__ void pf4c(u32x4 r, unsigned lb, unsigned voff, unsigned sb) {
    unsigned t;
    asm volatile("s_mov_b32 m0, %1\n s_add_i32 %0, %4, %5\n buffer_load_dword %2, %3, %0 offen sc1 lds" : "=&s"(t) : "s"(lb), "v"(voff), "s"(r), "s"(sb), "n"(SI) : "scc", "memory");
}
__device__ __forceinline__ void dma4v(u32x4 r, unsigned lb, const unsigned* v, unsigned sb) {
    asm volatile("s_mov_b32 m0, %0\n s_nop 0\n buffer_load_dwordx4 %1, %5, %6 offen lds\n buffer_load_dwordx4 %2, %5, %6 offen offset:1024 lds\n"
                 "buffer_load_dwordx4 %3, %5, %6 offen offset:2048 lds\n buffer_load_dwordx4 %4, %5, %6 offen offset:3072 lds"
                 :: "s"(lb), "v"(v[0]), "v"(v[1]), "v"(v[2]), "v"(v[3]), "s"(r), "s"(sb) : "memory");
}
__device__ __forceinline__ void dmah(u32x4 r, unsigned lb, unsigned v, unsigned sb) {
    asm volatile("s_mov_b32 m0, %0\n s_mov_b32 exec_hi, 0\n buffer_load_dwordx4 %1, %2, %3 offen lds\n s_mov_b32 exec_hi, -1" :: "s"(lb), "v"(v), "s"(r), "s"(sb) : "memory");
}
__device__ __forceinline__ void dmahc(u32x4 r, unsigned lb, unsigned v, unsigned sb) {
    asm volatile("s_mov_b32 m0, %0\n s_mov_b32 exec_hi, 0\n buffer_load_dwordx4 %1, %2, %3 offen sc1 lds\n s_mov_b32 exec_hi, -1" :: "s"(lb), "v"(v), "s"(r), "s"(sb) : "memory");
}
__device__ __forceinline__ u64 ldflag(__amdgpu_buffer_rsrc_t r, unsigned off) {
    asm volatile("" ::: "memory");
    return __builtin_bit_cast(u64, __builtin_amdgcn_raw_buffer_load_b64(r, off, 0, 16));
}
__device__ __forceinline__ void stflag(char* p, u64 v) { __hip_atomic_store((GAS u64*)p, v, RLX_AGENT); }
__device__ __forceinline__ bool allset(u64 v, u64 tag) { return __builtin_amdgcn_ballot_w64(v != tag) == 0ull; }

constexpr int SL_A = 16384, SL_B = 18432, SLOT = SL_A + SL_B;
constexpr unsigned SPIN_CAP = 1u << 20;
constexpr int L_ROWST = 145408;
constexpr int L_CS = L_ROWST + 1024;
constexpr int L_ZST = L_CS + 1024;
constexpr int L_WSCR = L_ZST + 1024;
constexpr int LDS_SZ = 163840;
constexpr int L_DUMP = LDS_SZ - 256;
constexpr int L_MISC = L_DUMP - 256;
constexpr int M_RL = L_MISC, M_FL = L_MISC + 16, M_RC = L_MISC + 32;
constexpr int CV_U = 512 + 4096, CV_HSTRIDE = 4160, CV_WAVE = CV_U + 4 * CV_HSTRIDE;
constexpr int RED_W = 18432, CV_BASE = L_MISC - 3 * CV_WAVE;
constexpr int Z8 = 5 * RED_W;
__host__ __device__ constexpr int warea(int j) { return 16384 + 18432 * (j == 0 || j == 7 ? 1 : j == 1 || j == 8 ? 0 : j == 2 ? 3 : j == 3 ? 5 : j == 4 ? 4 : j == 5 ? 6 : 2); }
constexpr int T18 = warea(2), T19 = warea(3), KVS = warea(6);
typedef unsigned u32x3 __attribute__((ext_vector_type(3)));

#define SCHED_IL(nr, nm) do { _Pragma("unroll") for (int i_ = 0; i_ < (nr); ++i_) { __builtin_amdgcn_sched_group_barrier(0x008, 1, 0); __builtin_amdgcn_sched_group_barrier(0x100, 1, 0); } \
        __builtin_amdgcn_sched_group_barrier(0x008, (nm) - (nr), 0); SCHED0(); } while (0)

__device__ __forceinline__ void spin_flags(__amdgpu_buffer_rsrc_t r, unsigned p, u64 tag) {
    unsigned spins = 0;
    while (!allset(ldflag(r, p), tag)) { if (++spins > SPIN_CAP) break; __builtin_amdgcn_s_sleep(1); }
    asm volatile("" ::: "memory");
}
__device__ __forceinline__ void dma1(int OFF, u32x4 r, unsigned lb, unsigned v, unsigned sb) {
    asm volatile("s_mov_b32 m0, %0\n s_nop 0\n buffer_load_dwordx4 %1, %2, %3 offen offset:%4 lds" :: "s"(lb), "v"(v), "s"(r), "s"(sb), "n"(OFF) : "memory");
}
#define WNT " nt"
#define KNT ""
__device__ __forceinline__ void dma1n(int OFF, u32x4 r, unsigned lb, unsigned v, unsigned sb) {
    asm volatile("s_mov_b32 m0, %0\n s_nop 0\n buffer_load_dwordx4 %1, %2, %3 offen offset:%4" WNT " lds" :: "s"(lb), "v"(v), "s"(r), "s"(sb), "n"(OFF) : "memory");
}
__device__ __forceinline__ void dma1c(int OFF, u32x4 r, unsigned lb, unsigned v, unsigned sb) {
    asm volatile("s_mov_b32 m0, %0\n s_nop 0\n buffer_load_dwordx4 %1, %2, %3 offen offset:%4 sc1 lds" :: "s"(lb), "v"(v), "s"(r), "s"(sb), "n"(OFF) : "memory");
}
__device__ __forceinline__ u32x4 rec4(float a, float b, float c, float d, unsigned tag) {
    const unsigned q = __float_as_uint(d);
    return (u32x4){__builtin_amdgcn_perm(__float_as_uint(a), q, 0x07060501u), __builtin_amdgcn_perm(__float_as_uint(b), q, 0x07060502u), __builtin_amdgcn_perm(__float_as_uint(c), q, 0x07060503u), tag};
}
__device__ __forceinline__ float half_sum(float a, float b) {
    asm("s_nop 1\n v_permlane32_swap_b32 %0, %1\n v_add_f32 %0, %0, %1\n v_mov_b32 %1, %0\n s_nop 1\n v_permlane16_swap_b32 %0, %1\n v_add_f32 %0, %0, %1" : "+v"(a), "+v"(b));
    return a;
}
template <int N> __device__ __forceinline__ f32x2 rec_consts(const u32x4* pv, float inv_n) {
    float a0, a1, a2, a3;
#pragma unroll
    for (int i = 0; i < N; ++i) {
        const float e = __uint_as_float(__builtin_amdgcn_perm(pv[i][2], __builtin_amdgcn_perm(pv[i][1], pv[i][0], 0x0c04000cu), 0x04020100u));
        if (i == 0) { a0 = __uint_as_float(pv[i][0]); a1 = __uint_as_float(pv[i][1]); a2 = __uint_as_float(pv[i][2]); a3 = e; }
        else { a0 = adds(a0, __uint_as_float(pv[i][0])); a1 = adds(a1, __uint_as_float(pv[i][1])); a2 = adds(a2, __uint_as_float(pv[i][2])); a3 = adds(a3, e); }
    }
    return ln_consts(half_sum(a0, a2), half_sum(a1, a3), inv_n);
}
__device__ __forceinline__ void wcheck(unsigned f, unsigned addr, unsigned need) {
    unsigned cnt;
    asm volatile("v_cmp_gt_u32 vcc, %3, %0\n s_cbranch_vccz 2f\n s_mov_b32 %1, 0x400000\n"
                 "1:\n s_sleep 1\n ds_read_b32 %0, %2\n s_sub_u32 %1, %1, 1\n s_waitcnt lgkmcnt(0)\n s_cbranch_scc1 2f\n v_cmp_gt_u32 vcc, %3, %0\n s_cbranch_vccnz 1b\n2:"
                 : "+v"(f), "=&s"(cnt) : "v"(addr), "n"(need) : "vcc", "scc", "memory");
}
__device__ __forceinline__ unsigned norm2s(f32x2 z, f32x4 st) {
    unsigned o, t;
    asm("v_fma_f32 %0, %2, %4, %6\n v_fma_f32 %1, %3, %5, %7\n v_cvt_pk_bf16_f32 %0, %0, %1" : "=&v"(o), "=&v"(t) : "v"(z.x), "v"(z.y), "v"(st[0]), "v"(st[1]), "v"(st[2]), "v"(st[3]));
    return o;
}
__device__ __forceinline__ float tap4s(float k0, float k1, float k2, float k3, const float* h, float v) {
    float o;
    asm("v_mul_f32 %0, %1, %8\n v_fma_f32 %0, %2, %7, %0\n v_fma_f32 %0, %3, %6, %0\n v_fma_f32 %0, %4, %5, %0\n v_mul_f32 %0, %0, %9"
        : "=&v"(o) : "v"(k0), "v"(k1), "v"(k2), "v"(k3), "v"(h[0]), "v"(h[1]), "v"(h[2]), "v"(h[3]), "v"(v));
    return o;
}

__device__ __forceinline__ float fmas(float a, float b, float c) { float o; asm("v_fma_f32 %0, %1, %2, %3" : "=v"(o) : "v"(a), "v"(b), "v"(c)); return o; }
__device__ __forceinline__ unsigned mulz(unsigned z, f32x2 a) {
    unsigned o, t;
    asm("v_lshlrev_b32 %0, 16, %2\n v_and_b32 %1, 0xffff0000, %2\n v_mul_f32 %0, %0, %3\n v_mul_f32 %1, %1, %4\n v_cvt_pk_bf16_f32 %0, %0, %1" : "=&v"(o), "=&v"(t) : "v"(z), "v"(a.x), "v"(a.y));
    return o;
}
__device__ __forceinline__ unsigned movs(unsigned a) { unsigned o; asm("v_mov_b32 %0, %1" : "=v"(o) : "v"(a)); return o; }
__device__ __forceinline__ unsigned algn(unsigned hi, unsigned lo) { unsigned o; asm("v_alignbit_b32 %0, %1, %2, 16" : "=v"(o) : "v"(hi), "v"(lo)); return o; }
__device__ __forceinline__ unsigned algn2(unsigned hi, unsigned lo) { unsigned o; asm("v_alignbit_b32 %0, %1, %2, 16 " : "=v"(o) : "v"(hi), "v"(lo)); return o; }

struct ConvIn { u32x4 uu[4], hh[4]; u32x2 hn[4]; unsigned dd; };
#define ALD(w_, dst, vo, r_, so, x_) asm volatile("buffer_load_dword" w_ " %0, %1, %2, %3 offen" x_ : "=v"(dst) : "v"(vo), "s"(r_), "s"(so) : "memory")
__device__ __forceinline__ void conv_load(ConvIn& c, const u16* __restrict__ hk, const u16* __restrict__ dv, u32x4 wr, int d, int lane) {
    const u32x4 rh = mk_rsrc(hk, SZ_X);
    const unsigned v = lane * 16, su = WS_KVT + d * (S * 2), sh = d * (S * 2);
    ALD("x4", c.uu[0], v, wr, su, " sc1"); ALD("x4", c.uu[1], v, wr, su, " offset:1024 sc1"); ALD("x4", c.uu[2], v, wr, su, " offset:2048 sc1"); ALD("x4", c.uu[3], v, wr, su, " offset:3072 sc1");
    ALD("x4", c.hh[0], v, rh, sh, ""); ALD("x4", c.hh[1], v, rh, sh, " offset:1024"); ALD("x4", c.hh[2], v, rh, sh, " offset:2048"); ALD("x4", c.hh[3], v, rh, sh, " offset:3072");
    ALD("x2", c.hn[0], v, rh, sh, " offset:16"); ALD("x2", c.hn[1], v, rh, sh, " offset:1040"); ALD("x2", c.hn[2], v, rh, sh, " offset:2064"); ALD("x2", c.hn[3], v, rh, sh, " offset:3088");
    c.dd = ((const unsigned*)dv)[d >> 1];
}
#define CONV_PIN(c) asm volatile("" : "+v"(c.uu[0]), "+v"(c.uu[1]), "+v"(c.uu[2]), "+v"(c.uu[3]), "+v"(c.hh[0]), "+v"(c.hh[1]), "+v"(c.hh[2]), "+v"(c.hh[3]), "+v"(c.hn[0]), "+v"(c.hn[1]), "+v"(c.hn[2]), "+v"(c.hn[3]))
__device__ __forceinline__ void conv_stage(ConvIn& c, LAS char* U, int lane) {
    LAS char* H = U + CV_U;
    if (lane == 63) c.hn[3] = (u32x2){0u, 0u};
    if (lane < 32) *(LAS u32x4*)(U + lane * 16) = (u32x4){0u, 0u, 0u, 0u};
    const unsigned h0 = RFL(c.hh[0][0]), h1 = RFL(c.hh[0][1]);
    if (lane < 8) {
        const int cc = lane >> 1, half = lane & 1;
        u32x4 z = {0u, 0u, 0u, 0u};
        if (half) {
            if (cc == 1) z[3] = h0 << 16;
            if (cc == 2) z[3] = h0;
            if (cc == 3) { z[2] = h0 << 16; z[3] = __builtin_amdgcn_alignbit(h1, h0, 16); }
        }
        *(LAS u32x4*)(H + cc * CV_HSTRIDE + half * 16) = z;
    }
#pragma unroll
    for (int i = 0; i < 4; ++i) {
        *(LAS u32x4*)(U + 512 + i * 1024 + lane * 16) = c.uu[i];
        const unsigned w0 = c.hh[i][0], w1 = c.hh[i][1], w2 = c.hh[i][2], w3 = c.hh[i][3], w4 = c.hn[i][0], w5 = c.hn[i][1];
        LAS char* p = H + 32 + i * 1024 + lane * 16;
        *(LAS u32x4*)(p) = c.hh[i];
        *(LAS u32x4*)(p + CV_HSTRIDE) = (u32x4){algn(w1, w0), algn(w2, w1), algn(w3, w2), algn(w4, w3)};
        *(LAS u32x4*)(p + 2 * CV_HSTRIDE) = (u32x4){movs(w1), movs(w2), movs(w3), movs(w4)};
        *(LAS u32x4*)(p + 3 * CV_HSTRIDE) = (u32x4){algn2(w2, w1), algn2(w3, w2), algn2(w4, w3), algn2(w5, w4)};
    }
}
template <class MID> __device__ __forceinline__ void conv_run(unsigned ddw, __amdgpu_buffer_rsrc_t rs, LAS char* U, int d, int lane, MID mid) {
    const int fr = lane & 15, g = lane >> 4;
    LAS char* H = U + CV_U;
    const int e0 = 16 * (g >> 1) - 8 * (g & 1) - 7 + fr + 16;
    const int ac = e0 & 3;
    LAS char* ap = H + ac * CV_HSTRIDE + (e0 - ac) * 2;
    LAS char* bp = U + 512 + 32 * (fr - (g >> 1)) + 16 * (g & 1) - 448;
    f32x4 acc[8];
#pragma unroll
    for (int n = 0; n < 8; ++n) acc[n] = (f32x4){0.f, 0.f, 0.f, 0.f};
    bf16x8 LA[2][4], LB[2][4], HA[4], HB[4];
#define CV_RD(A, B, a0, pa, pb) do { _Pragma("unroll") for (int a = 0; a < 4; ++a) { B[a] = *(LAS bf16x8*)((pb) + (a0 + a) * 512); \
        A[a] = cat8(*(LAS bf16x4*)((pa) + (a0 + a) * 512), *(LAS bf16x4*)((pa) + (a0 + a) * 512 + 8)); } } while (0)
#define CV_STEP(cur, nxt, pa, pb) do { \
        CV_RD(HA, HB, 4, pa, pb); \
        _Pragma("unroll") for (int s = 0; s < 4; ++s) { \
            _Pragma("unroll") for (int a = 0; a < s; ++a) { acc[a + s] = mfma16(LA[cur][a], LB[cur][s], acc[a + s]); acc[a + s] = mfma16(LA[cur][s], LB[cur][a], acc[a + s]); } \
            acc[2 * s] = mfma16(LA[cur][s], LB[cur][s], acc[2 * s]); } \
        SCHED_IL(12, 16); \
        CV_RD(LA[nxt], LB[nxt], 0, (pa) + 64, (pb) - 64); \
        _Pragma("unroll") for (int s = 0; s < 4; ++s) \
            _Pragma("unroll") for (int a = 0; a + s < 4; ++a) { acc[a + s + 4] = mfma16(LA[cur][a], HB[s], acc[a + s + 4]); acc[a + s + 4] = mfma16(HA[s], LB[cur][a], acc[a + s + 4]); } \
        SCHED_IL(12, 20); \
    } while (0)
    CV_RD(LA[0], LB[0], 0, ap, bp + 448);
#pragma unroll 1
    for (int bb = 0; bb < 8; bb += 2) {
        LAS char* pa = ap + bb * 64;
        LAS char* pb = bp + (7 - bb) * 64;
        CV_STEP(0, 1, pa, pb);
        CV_STEP(1, 0, pa + 64, pb - 64);
    }
    const float dd = (d & 1) ? bfhi(ddw) : bflo(ddw);
    unsigned yw[8][2];
    u32x2 uw[8];
#pragma unroll
    for (int nu = 0; nu < 8; ++nu) uw[nu] = *(LAS u32x2*)(U + 512 + (256 * nu + 16 * fr + 8 * (g >> 1)) * 2 + ((g & 1) ? 0 : 8));
    __builtin_amdgcn_s_waitcnt(0xC07F);
    SCHED0();
    mid();
    SCHED0();
#pragma unroll
    for (int nu = 0; nu < 8; ++nu) {
        yw[nu][0] = pk2(fmas(bfhi(uw[nu][1]), dd, acc[nu][0]), fmas(bflo(uw[nu][1]), dd, acc[nu][1]));
        yw[nu][1] = pk2(fmas(bfhi(uw[nu][0]), dd, acc[nu][2]), fmas(bflo(uw[nu][0]), dd, acc[nu][3]));
    }
#pragma unroll
    for (int p = 0; p < 4; ++p) {
        const u32x2 s0 = __builtin_amdgcn_permlane16_swap(yw[2 * p][0], yw[2 * p + 1][0], false, false);
        const u32x2 s1 = __builtin_amdgcn_permlane16_swap(yw[2 * p][1], yw[2 * p + 1][1], false, false);
        st16c(rs, (256 * (g & 1) + 16 * fr + 8 * (g >> 1)) * 2 + p * 1024, WS_YT + d * (S * 2), (u32x4){s0[0], s1[0], s0[1], s1[1]});
    }
}

constexpr int RB = 5, KST = 36 * 1024;
#define G3_LDB(i) do { _Pragma("unroll") for (int l = 0; l < 4; ++l) bq[i][l] = __builtin_bit_cast(bf16x8, ld16c(rs, lane * 16 + l * 1024, sB + (i) * KST)); \
        bq[i][4] = __builtin_bit_cast(bf16x8, ld16c(rs, lane * 16, sB4 + (i) * KST)); } while (0)
#define G3_LDA(i) do { _Pragma("unroll") for (int m = 0; m < 4; ++m) aq[i][m] = __builtin_bit_cast(bf16x8, ld16c(rs, lane * 16, sA + m * (72 * 1024) + (i) * 1024)); } while (0)
#define G3_RDB(i) do { _Pragma("unroll") for (int l = 0; l < 4; ++l) bq[i][l] = *(LAS bf16x8*)(lds + redo + ((i) - RB) * 4096 + l * 1024 + lane * 16); \
        bq[i][4] = *(LAS bf16x8*)(lds + redo + 16384 + ((i) - RB) * 512 + (g * 8 + (fr & 7)) * 16); } while (0)
#define G3_PRE() do { _Pragma("unroll") for (int i = 0; i < RB; ++i) G3_LDB(i); SCHED0(); } while (0)
#define G3_DMA(dst, sB, sB4) do { const unsigned ho = (lane >> 5) * KST + (((lane >> 3) & 3) * 16 + 8 * (ct & 1) + (lane & 7)) * 16; \
        dma16c4<0, 5 * KST>(wr, L0 + (dst), lane * 16, sB); dma16c4<4096, 6 * KST>(wr, L0 + (dst), lane * 16, sB); dma16c<16384, 5 * KST>(wr, L0 + (dst), ho, sB4); \
        dma16c4<8192, 7 * KST>(wr, L0 + (dst), lane * 16, sB); dma16c4<12288, 8 * KST>(wr, L0 + (dst), lane * 16, sB); dma16c<17408, 7 * KST>(wr, L0 + (dst), ho, sB4); } while (0)
#define FLAG_ISSUE(v_, o_) asm volatile("buffer_load_dwordx2 %0, %1, %2, 0 offen sc1" : "=v"(v_) : "v"(o_), "s"(wr) : "memory")
#define FP_ISSUE() FLAG_ISSUE(fpv, sF + (lane & 1) * 16 + (lane & 2) * 16)
#define FP_CHECK(NV) do { u64 ne; \
        asm volatile("s_waitcnt vmcnt(%3)\n v_cmp_ne_u64 %0, %1, %2" : "=s"(ne) : "s"(tag), "v"(fpv), "n"(NV) : "memory"); \
        if (ne != 0ull) spin_flags(rs, sF + (lane & 1) * 16 + (lane & 2) * 16, tag); \
        asm volatile("" ::: "memory"); } while (0)
#define XST(i) do { } while (0)
#define NOSTAT 0
#define ROT (pi + pj) % 9
#define CPRE 2
constexpr float GC1 = -2.0f * 0.7978845608028654f * 1.4426950408889634f, GC2 = GC1 * 0.044715f;
#define CPRIO 2
#define CPFW 2
#define T1W 1
#define KPF 1
#define GS 72
#define WBASE 0
#define WALK 2
#define ZD 6

__global__ __launch_bounds__(512, 2) void mega(const u16* __restrict__ x, const u16* __restrict__ kin, const u16* __restrict__ vin, const u16* __restrict__ shk,
                                               const u16* __restrict__ hk, const u16* __restrict__ dv, const u16* __restrict__ w_in, const u16* __restrict__ w_sp,
                                               const u16* __restrict__ b_sp, const u16* __restrict__ w_out, char* __restrict__ ws, u16* __restrict__ out) {
    extern __shared__ __attribute__((aligned(16))) char smem_[];
    LAS char* lds = (LAS char*)smem_;
    asm volatile(".Lmega_head:");
    const unsigned L0 = (unsigned)(size_t)lds;
    const int b = blockIdx.x, xcd = b & 7, r = b >> 3;
    const int pi = (xcd & 1) * 8 + (r & 7), pj = (xcd >> 1) * 4 + (r >> 3);
    const int tid = threadIdx.x, wid = RFL(tid >> 6), lane = tid & 63;
    const int fr = lane & 15, g = lane >> 4, q4 = (lane >> 2) & 3, p4 = lane & 3;
    const __amdgpu_buffer_rsrc_t rs = ws_rsrc(ws);
    const u32x4 wr = mk_rsrc(ws, WS_END);
    const f32x4 zero4 = {0.f, 0.f, 0.f, 0.f};
    STAMP(0);
#define TAILS() const int b_ = opqs(b), wid_ = opqs(wid), rg = (b_ & 1) * 16 + ((b_ & 7) >> 1) * 4 + ((b_ >> 3) & 3), ct = b_ >> 5; \
    const int nb3 = (9 * ct) >> 1, n30 = nb3 + (ct & 1), n34 = (ct & 1) ? nb3 : nb3 + 4; \
    const int redo = wid_ < 4 ? wid_ * RED_W : wid_ < 7 ? CV_BASE + (wid_ - 4) * CV_WAVE : 4 * RED_W
#define TAILS2() const unsigned sA = WS_P + (4 * rg * 72 + 9 * wid_) * 1024, sB = WS_WT + (9 * wid_ * 36 + n30) * 1024, sB4 = WS_WT + (9 * wid_ * 36 + n34) * 1024, \
    sF = WS_FP + (rg >> 1) * 512 + wid_ * 64 + (rg & 1) * 8
    u64 tag;
#define TAG_READY() do { asm volatile("s_load_dwordx2 %0, %1, 0x0\n s_waitcnt lgkmcnt(0)" : "=s"(tag) : "s"(ws + WS_E) : "memory"); tag += TAG_INC; } while (0)
#define WAIT_LB0() __builtin_amdgcn_s_waitcnt(0xC07F)
#define RD(p) (*(LAS bf16x8*)(p))
#define RDP(p, o) (*(LAS bf16x8*)(size_t)((p) + (o)))
#define TRF(p) cat8(trread(p), trread((p) + 2304))
#define TRP(p, o) TRF((LAS char*)(size_t)((p) + (o)))
#define ADV(p, d) asm("v_add_u32 %0, %1, %0" : "+v"(p) : "s"(d))
#define LDSW(o) (*(volatile LAS unsigned*)(lds + (o)))
#define FENCE() asm volatile("" ::: "memory")
#define SPIN(word, need) wcheck(0u, L0 + (word) + p4 * 4, need)
#define SGB(m, n) __builtin_amdgcn_sched_group_barrier(m, n, 0)
    const int w = wid & 3, l3 = lane >> 3;
    const unsigned ch = ((lane & 7) ^ (l3 >> 1)) * 16;
    const int bb = (q4 + 4 * (g & 1) + 16 * (g >> 1)) * 288 + p4 * 8;
    unsigned vA[4];
#define SET_VSP() do { const unsigned base = l3 * (S * 2) + ch; vA[0] = base; vA[1] = (base ^ 64) + 8 * S * 2 - 1024; vA[2] = base + 16 * S * 2 - 2048; vA[3] = (base ^ 64) + 24 * S * 2 - 3072; } while (0)
    unsigned vB[5];
#define SET_VB(i) do { const unsigned c = i < 4 ? (4 * w + i) * 64 + lane : 1024 + 32 * w + (lane & 31), rho = c / 18, cc = c - rho * 18; \
        const unsigned grow = (rho & ~12u) | ((rho & 4) << 1) | ((rho & 8) >> 1); vB[i] = grow * (F * 2) + cc * 16 - (i & 3) * 1024; } while (0)
    const unsigned lw = L0 + w * 4096, lh = L0 + 16384 + w * 512, w1 = pj * 288;
    const int rot = ROT;
#define KT(t) ((t) + rot >= 9 ? (t) + rot - 9 : (t) + rot)
#define DMA_W(dst, src) do { dma4v(rwi, lw + (dst), vB, w1 + (src)); dmah(rwi, lh + (dst), vB[4], w1 + (src)); } while (0)
#define DMA_W1(j) DMA_W(warea(j), KT(j) * (64 * F * 2))
    auto walk = [&](int i) {
        const int j = pj & 3, m = i ^ j, x = i - 1;
        const int q = (pi & ~3) | ((j + x + (x >= ((pi - j) & 3))) & 3);
        return i == 0 ? pi : i < 4 ? q : pi ^ (WALK == 2 && (m == 7 || m == 8) ? m ^ 15 : m);
    };
    u64 fpv;
    bf16x8 bq[9][5];
    bool hasconv = false;
    const int cd = w < 2 ? 2 * b + w : 512 + b;

    if (__builtin_expect(wid >= 4, 1)) {
        const u32x4 rx = mk_rsrc(x, SZ_X), rwi = mk_rsrc(w_in, D * F * 2);
        {
            const unsigned base = (8 * (l3 >> 2) + (l3 & 3)) * (D * 2) + ch;
            vA[0] = base; vA[1] = (base ^ 64) + 16 * D * 2 - 1024; vA[2] = (base ^ 64) + 4 * D * 2 - 2048; vA[3] = base + 20 * D * 2 - 3072;
        }
        const unsigned xs = (pi * 128 + 32 * w) * (D * 2);
        XST(26);
        dma4v(rx, lw, vA, xs + rot * 128);
        SCHED0();
#pragma unroll
        for (int i = 0; i < 5; ++i) {
            SET_VB(i);
            if (i < 4) dma1(i * 1024, rwi, lw + SL_A, vB[i], w1 + FH * 2 + rot * (64 * F * 2)); else dmah(rwi, lh + SL_A, vB[4], w1 + FH * 2 + rot * (64 * F * 2));
            SCHED0();
        }
        XST(27);
        TAG_READY();
        const int xi = (fr >> 2) & 1;
        const int n0 = (32 * w + 16 * xi + 4 * (fr >> 3) + (fr & 3)) * 128 + ((g ^ (2 * (fr >> 3) + ((fr >> 1) & 1)) ^ (4 * xi)) << 4), n1 = n0 ^ 64;
        const int c0 = bb + w * 64, c8 = bb + 256 + (w & 1) * 9216;
        bf16x8 ones = {0x3f80, 0x3f80, 0x3f80, 0x3f80, 0x3f80, 0x3f80, 0x3f80, 0x3f80};
        asm volatile("" : "+v"(ones));
        bf16x8 xf[9][4];
        const unsigned tag32 = (unsigned)tag;
        const unsigned go = g * 1024 + fr * 16, go2 = g == 0 ? 8192 + fr * 16 : 0x7fffff00u, tagx = g == 0 ? tag32 : 0u;
        const unsigned sr = WS_SR + pi * 12288 + w * 256, sc = WS_SC + pj * 12288 + w * 256;
#define COLSUM(base, dst) do { bf16x8 cf[5]; cf[0] = TRF((base) + c0); cf[1] = TRF((base) + c0 + 32); cf[2] = TRF((base) + c0 + 9216); cf[3] = TRF((base) + c0 + 9216 + 32); cf[4] = TRF((base) + c8); \
            const f32x4 s0_ = mfma16(ones, cf[2], mfma16(ones, cf[0], zero4)), s1_ = mfma16(ones, cf[3], mfma16(ones, cf[1], zero4)), s2_ = mfma16(ones, cf[4], zero4); \
            st16c(rs, g == 0 && pi < 9 ? fr * 16 : 0x7fffff00u, (dst) + pi * 1024, (u32x4){__float_as_uint(s0_[0]), __float_as_uint(s1_[0]), __float_as_uint(s2_[0]), tag32}); } while (0)
#define GATHER(pv, so) do { ALD("x4", pv[0], go, wr, so, " sc1"); ALD("x4", pv[1], go, wr, (so) + 4096, " sc1"); ALD("x4", pv[2], go2, wr, so, " sc1"); } while (0)
#define GBAD(pv) (__builtin_amdgcn_ballot_w64(((pv[0][3] ^ tag32) | (pv[1][3] ^ tag32) | (pv[2][3] ^ tagx)) != 0u) != 0ull)
#define GCHECK(pv, so) do { asm volatile("" : "+v"(pv[0]), "+v"(pv[1]), "+v"(pv[2])); \
            if (__builtin_expect(GBAD(pv), 0)) { unsigned sp_ = 0; \
                do { GATHER(pv, so); WAIT_V(0); asm volatile("" : "+v"(pv[0]), "+v"(pv[1]), "+v"(pv[2])); } while (GBAD(pv) && ++sp_ < SPIN_CAP); } } while (0)
#define CSUM(pv, k) adds(adds(__uint_as_float(pv[0][k]), __uint_as_float(pv[1][k])), __uint_as_float(pv[2][k]))
        u32x4 gr[3], gc[3];
        f32x2 lnc; float cs01, cs8;
        const u32x4 rsp = mk_rsrc(w_sp, S * S * 2);
        unsigned kpf = lane * (D * 2);
        __builtin_amdgcn_s_setprio(3);
#pragma unroll
        for (int t = 0; t < 9; ++t) {
            SCHED0();
            if (t == 0) {
                asm volatile("" :: "s"(KT(1)), "s"(KT(2)), "s"(KT(3)), "s"(KT(4)), "s"(KT(5)), "s"(KT(6)), "s"(KT(7)), "s"(KT(8)), "v"(n0), "v"(c0), "v"(c8), "v"(go), "v"(go2));
                XST(28);
                dma4v(rx, lw + SLOT, vA, xs + KT(1) * 128);
                if (T1W) DMA_W(SLOT + SL_A, FH * 2 + KT(1) * (64 * F * 2));
                XST(29);
            }
            WAIT_V(t == 0 ? (T1W ? 9 : 4) : t == 2 || t == 4 ? 10 : t == 3 ? 11 : t == 7 ? 15 : t == 8 ? 10 : 9);
            BAR();
            XST(40 + t);
            LAS char* sb = lds + (t & 3) * SLOT;
            if (t < 8) { xf[t][0] = RD(sb + n0); xf[t][1] = RD(sb + n1 + 1024); xf[t][2] = RD(sb + n1); xf[t][3] = RD(sb + n0 + 1024); }
            SCHED0();
            if (t == 0 && !T1W) DMA_W(SLOT + SL_A, FH * 2 + KT(1) * (64 * F * 2));
            if (t < 7) {
                dma4v(rx, lw + ((t + 2) & 3) * SLOT, vA, xs + KT(t + 2) * 128);
                DMA_W(((t + 2) & 3) * SLOT + SL_A, FH * 2 + KT(t + 2) * (64 * F * 2));
            }
            SCHED0();
            if (t == 1) COLSUM(lds + SL_A, sc);
            if (t == 2) {
                f32x4 rsm[2], rq[2];
#pragma unroll
                for (int m = 0; m < 2; ++m) {
                    rsm[m] = mfma16(ones, xf[0][2 + m], mfma16(ones, xf[0][m], zero4));
                    rq[m] = mfma16(xf[0][2 + m], xf[0][2 + m], mfma16(xf[0][m], xf[0][m], zero4));
                }
#define DIAG(q_) ((fr & 2) ? ((fr & 1) ? q_[3] : q_[2]) : ((fr & 1) ? q_[1] : q_[0]))
                st16c(rs, (fr >> 2) == g && pj < 9 ? fr * 16 : 0x7fffff00u, sr + pj * 1024, rec4(rsm[0][0], DIAG(rq[0]), rsm[1][0], DIAG(rq[1]), tag32));
            }
            SCHED0();
            if (t == 6) { GATHER(gr, sr); GATHER(gc, sc); }
            if (t == 7) { DMA_W1(0); DMA_W1(2); }
            if (t == 8) {
                if (KPF == 2) ALD("", kpf, kpf, mk_rsrc(w < 2 ? kin : vin, KPF ? SZ_X : 0), (b >> 3) * 64 * (D * 2) + (b & 7) * 144 + (w & 1) * 140, "");
                GCHECK(gr, sr); GCHECK(gc, sc);
                lnc = rec_consts<3>(gr, 1.0f / D);
                cs01 = half_sum(CSUM(gc, 0), CSUM(gc, 1)); cs8 = CSUM(gc, 2); cs8 = half_sum(cs8, cs8);
                asm volatile("" : "+v"(lnc), "+v"(cs01), "+v"(cs8));
            }
            if (t < 8) asm volatile("" : "+v"(xf[t][0]), "+v"(xf[t][1]), "+v"(xf[t][2]), "+v"(xf[t][3]));
        }
        {
            LAS float* p = (LAS float*)(lds + L_ROWST + (16 * w + 8 * (lane >> 5) + (fr >> 1)) * 16 + (fr & 1) * 4);
            p[0] = lnc.x; p[2] = lnc.y;
            LAS float* c = (LAS float*)(lds + L_CS) + fr * 12;
            c[2 * w + (lane >> 5)] = cs01;
            c[8 + w] = cs8;
        }
        WAIT_L0();
        BAR();
        STAMP(2);
        DMA_W1(1); DMA_W1(3); DMA_W1(4); DMA_W1(5); DMA_W1(6);
        if (KPF != 2) ALD("", kpf, kpf, mk_rsrc(w < 2 ? kin : vin, KPF ? SZ_X : 0), (b >> 3) * 64 * (D * 2) + (b & 7) * 144 + (w & 1) * 140, "");
        WAIT_V(KPF == 2 ? 15 : 16);
        LDSW(M_FL + w * 4) = 1u;
        XST(50);
        SPIN(M_FL, 1);
        XST(49);
        COLSUM(lds + warea(0), sc + 16 * 12288);
        st16c(rs, w == 3 && pj == 0 ? lane * 16 : 0x7fffff00u, WS_ST1 + pi * 1024, *(LAS u32x4*)(lds + L_ROWST + lane * 16));
        f32x4 az[2][9];
        unsigned rho = walk(lane >> 2);
        asm volatile("" : "+v"(rho));
        u64 fzv[2];
        const unsigned as0 = (pi * 128 + 32 * w) * (S * 2), zs0 = WS_Z2N + pj * 32 * SL_B + w * 4096;
        unsigned vl, fz;
#define KIDX(t) (2 * __builtin_amdgcn_readlane(rho, 4 * (((t) - 18) >> 1)) + ((t) & 1))
        auto stageA = [&](int t) { dma4v(rsp, lw + (t & 3) * SLOT, vA, as0 + KIDX(t) * 128); };
        auto stageB = [&](int t) {
            const unsigned zs = zs0 + KIDX(t) * SL_B;
            dma16c4<SL_A, 0>(wr, lw + (t & 3) * SLOT, vl, zs);
            dmahc(wr, lh + (t & 3) * SLOT + SL_A, vl, zs + 16384 - w * 3584);
        };
        u32x2 kw[11], vw[8]; u32x4 sk[2];
        unsigned ko, kom, ko4, kd8, kvo;
        {
            bf16x8 wf[162];
            unsigned fw;
            unsigned wb = L0 + bb;
#define WF_B(j) ((j) < 2 || (j) >= 6 ? 0 : (j) < 5 ? warea(2) : warea(5))
#define WF_P(s) ((LAS char*)(size_t)(wb + (warea((s) / 18) - WF_B((s) / 18) + ((s) % 9) * 32 + (((s) / 9) & 1) * 9216)))
#pragma unroll
            for (int s = 0; s < ZD; ++s) wf[s] = TRF(WF_P(s));
            SCHED0();
            XST(51);
            __builtin_amdgcn_s_setprio(0);
            wcheck(0u, L0 + M_RC + w * 4, 1);
            STAMP(3);
            SFOR(s, 0, 162)
                constexpr int j = s / 18, ks = (s / 9) & 1, n = s % 9;
                if (s == 72 - ZD - 4) fw = LDSW(M_FL + p4 * 4);
                if (s == 72 - ZD) wcheck(fw, L0 + M_FL + p4 * 4, 2);
                if (s == 126 - ZD - 4) fw = LDSW(M_RC + p4 * 4);
                if (s == 126 - ZD) wcheck(fw, L0 + M_RC + p4 * 4, 2);
                if (s == 18) { xf[8][0] = RD(lds + n0); xf[8][1] = RD(lds + n1 + 1024); }
                if (s == 19) { xf[8][2] = RD(lds + n1); xf[8][3] = RD(lds + n0 + 1024); }
                if (s == 25) asm volatile("" : "+v"(xf[8][0]), "+v"(xf[8][1]), "+v"(xf[8][2]), "+v"(xf[8][3]));
                if (s == 18 || s == 36 || s == 108 || s == 126) LDSW(M_RL + w * 4) = j;
                if (s == 46) {
                    const int ln = opqv(tid) & 63, it = w * 36 + (ln < 36 ? ln : 35), sg = it / 18;
                    kvo = sg; kd8 = it - sg * 18;
                    asm volatile("" : "+v"(kd8), "+v"(kvo));
                }
                if (s == 49) {
                    ko = (((b >> 3) * 64 + kvo * 8) * D + (b & 7) * 72 + kd8 * 4) * 2;
                    kom = b < 8 && kvo == 0 ? 0x7fffff00u : ko - 3 * (D * 2);
                    asm volatile("" : "+v"(ko), "+v"(kom));
                }
                if (s == 52) {
                    ko4 = ko + 4 * (D * 2); kvo = L0 + KVS + kd8 * 512 + ((kvo + kd8) & 7) * 16; kd8 = kd8 * 32 + (b & 7) * 576;
                    asm volatile("" : "+v"(ko4), "+v"(kd8), "+v"(kvo));
                }
                if (s == 152) fw = LDSW(M_RL + p4 * 4);
                if (s == 158) wcheck(fw, L0 + M_RL + p4 * 4, 7);
                if (s == 40) { WAIT_V(KPF == 2 ? 2 : 3); LDSW(M_FL + w * 4) = 2u; }
                if (s == 140) { vl = (opqv(tid) & 63) * 16; fz = WS_FZ + pj * 512 + rho * 32 + ((vl >> 1) & 24); asm volatile("" : "+v"(fz), "+v"(vl)); }
                if (s == 146) { const unsigned l3 = vl >> 7, ch = ((vl >> 4 & 7) ^ (l3 >> 1)) * 16; vA[0] = l3 * (S * 2) + ch; asm volatile("" : "+v"(vA[0])); }
                if (s == 150) { vA[1] = (vA[0] ^ 64) + 8 * S * 2 - 1024; vA[2] = vA[0] + 16 * S * 2 - 2048; vA[3] = vA[1] + 16 * S * 2 - 2048; asm volatile("" : "+v"(vA[1]), "+v"(vA[2]), "+v"(vA[3])); }
#define LD8(dst, vo, r_, imm) asm volatile("buffer_load_dwordx2 %0, %1, %2, 0 offen offset:%3" KNT : "=v"(dst) : "v"(vo), "s"(r_), "n"(imm) : "memory")
                if constexpr (s >= 54 && s < 65) { constexpr int i = s - 54; LD8(kw[i], i < 3 ? kom : i < 7 ? ko : ko4, mk_rsrc(kin, SZ_X), (i < 3 ? i : (i - 3) & 3) * 1152); }
                if constexpr (s >= 72 && s < 80) { constexpr int i = s - 72; LD8(vw[i], i < 4 ? ko : ko4, mk_rsrc(vin, SZ_X), (i & 3) * 1152); }
                if (s == 80) asm volatile("buffer_load_dwordx4 %0, %2, %3, 0 offen\n buffer_load_dwordx4 %1, %2, %3, 0 offen offset:16" : "=&v"(sk[0]), "=&v"(sk[1]) : "v"(kd8), "s"(mk_rsrc(shk, D * 8)) : "memory");
                if (s == 18) XST(56); if (s == 42) XST(57); if (s == 54) XST(58); if (s == 107) XST(59); if (s == 109) XST(60); if (s == 121) XST(61); if (s == 133) XST(62); if (s == 135) XST(63);
                SCHED0();
                if (s < 9) asm volatile("v_mfma_f32_16x16x32_bf16 %0, %1, %2, 0" : "=&v"(az[0][n]) : "v"(wf[s]), "v"(xf[j][2 * ks]));
                else asm volatile("v_mfma_f32_16x16x32_bf16 %0, %1, %2, %0" : "+v"(az[0][n]) : "v"(wf[s]), "v"(xf[j][2 * ks]));
                SCHED0();
                bf16x4 h0, h1;
                if constexpr ((s + ZD) % 18 == 0 && s + ZD < 162 && WF_B((s + ZD) / 18) != WF_B((s + ZD) / 18 - 1)) { unsigned& wb_ = wb; asm volatile("v_add_u32 %0, %1, %0" : "+v"(wb_) : "n"(WF_B((s + ZD) / 18) - WF_B((s + ZD) / 18 - 1))); }
                if constexpr (s + ZD < 162) h0 = trread(WF_P(s + ZD));
                SCHED0();
                if (s < 9) asm volatile("v_mfma_f32_16x16x32_bf16 %0, %1, %2, 0" : "=&v"(az[1][n]) : "v"(wf[s]), "v"(xf[j][2 * ks + 1]));
                else asm volatile("v_mfma_f32_16x16x32_bf16 %0, %1, %2, %0" : "+v"(az[1][n]) : "v"(wf[s]), "v"(xf[j][2 * ks + 1]));
                SCHED0();
                if constexpr (s + ZD < 162) { h1 = trread(WF_P(s + ZD) + 2304); wf[s + ZD] = cat8(h0, h1); }
                SCHED0();
            SEND
        }
        STAMP(4);
        WAIT_V(0);
        asm volatile("" : "+v"(kw[0]), "+v"(kw[1]), "+v"(kw[2]), "+v"(kw[3]), "+v"(kw[4]), "+v"(kw[5]), "+v"(kw[6]), "+v"(kw[7]), "+v"(kw[8]), "+v"(kw[9]), "+v"(kw[10]) : "v"(kpf));
        asm volatile("" : "+v"(vw[0]), "+v"(vw[1]), "+v"(vw[2]), "+v"(vw[3]), "+v"(vw[4]), "+v"(vw[5]), "+v"(vw[6]), "+v"(vw[7]), "+v"(sk[0]), "+v"(sk[1]));
        GATHER(gc, sc + 16 * 12288);
#define HALF(w_, c) (((c) & 1) ? bfhi(w_) : bflo(w_))
#pragma unroll
        for (int c = 0; c < 4; ++c) {
            const float h[4] = {bflo(sk[c >> 1][(c & 1) * 2]), bfhi(sk[c >> 1][(c & 1) * 2]), bflo(sk[c >> 1][(c & 1) * 2 + 1]), bfhi(sk[c >> 1][(c & 1) * 2 + 1])};
            float kk[11], o[8];
#pragma unroll
            for (int i = 0; i < 11; ++i) kk[i] = HALF(kw[i][c >> 1], c);
#pragma unroll
            for (int i = 0; i < 8; ++i) o[i] = tap4s(kk[i], kk[i + 1], kk[i + 2], kk[i + 3], h, HALF(vw[i][c >> 1], c));
            *(LAS u32x4*)(size_t)(kvo + c * 128) = (u32x4){pk2(o[7], o[6]), pk2(o[5], o[4]), pk2(o[3], o[2]), pk2(o[1], o[0])};
        }
        __builtin_amdgcn_s_setprio(3);
        WAIT_L0();
        STAMP(5);
        BAR();
        FLAG_ISSUE(fzv[0], fz);
        {
        const int lane = vl >> 4, fr = lane & 15, g = lane >> 4, q4 = (lane >> 2) & 3, p4 = lane & 3, l3 = lane >> 3;
        u64 miss;
#define NEEDZ(mask) do { if ((miss & (mask)) != 0ull) { unsigned sp_ = 0; \
            do { miss = __builtin_amdgcn_ballot_w64(ldflag(rs, fz) != tag); } while ((miss & (mask)) != 0ull && ++sp_ < SPIN_CAP); } FENCE(); } while (0)
        u32x4 zp[9];
        hasconv = w < 2 || (w == 2 && b < 64);
        const unsigned fk = WS_FK + w * 512 + lane * 8;
        {
            float nmb[2], k1[2], k2[2];
#pragma unroll
            for (int m = 0; m < 2; ++m) {
                LAS float* p = (LAS float*)(lds + L_ROWST + (16 * w + 8 * m + (fr >> 1)) * 16 + (fr & 1) * 4);
                const float rd = p[2];
                nmb[m] = -p[0]; k1[m] = muls(rd, GC1); k2[m] = muls(muls(rd, rd), muls(rd, GC2));
            }
            u32x4 kl[3];
            kl[0] = *(LAS u32x4*)(lds + KVS + w * 1024 + vl); kl[1] = *(LAS u32x4*)(lds + KVS + 4096 + w * 1024 + vl); kl[2] = *(LAS u32x4*)(lds + KVS + 8192 + vl);
            WAIT_V(1);
            GCHECK(gc, sc + 16 * 12288);
            *(LAS float*)(lds + L_CS + ((2 * w + (lane >> 5)) * 16 + fr) * 4) = half_sum(CSUM(gc, 0), CSUM(gc, 1));
            cs8 = CSUM(gc, 2);
            *(LAS float*)(lds + L_CS + ((8 + w) * 16 + fr) * 4) = half_sum(cs8, cs8);
            f32x4 cc = zero4;
            asm volatile("s_waitcnt vmcnt(0) lgkmcnt(0)\n v_cmp_ne_u64 %0, %1, %2" : "=s"(miss) : "s"(tag), "v"(fzv[0]), "v"(k1[0]), "v"(k1[1]), "v"(k2[0]), "v"(k2[1]), "v"(nmb[0]), "v"(nmb[1]), "v"(kl[0]), "v"(kl[1]), "v"(kl[2]) : "memory");
            asm volatile("" : "+v"(kl[0]), "+v"(kl[1]), "+v"(kl[2]));
            NEEDZ(0xf0ull);
            stageB(20); stageA(21);
#define XE(u) ((u) == 6 ? 2 : (u) >= 7 && (u) < 11 ? 1 : 0)
#define NW(u) ((u) == 0 ? 63 : (u) < 5 ? 9 : (u) == 5 ? 14 : 18 + XE((u) - 1) + XE((u) - 2) + XE((u) - 3))
            SFOR(u, 0, 19)
                constexpr int n = 19 + u, t = n + 3, tb = u < 4 ? t - 1 : t;
                const unsigned la = lw + (t & 3) * SLOT, lb = lw + (tb & 3) * SLOT + SL_A, as = as0 + KIDX(t) * 128, zs = zs0 + KIDX(tb) * SL_B;
                SCHED0();
                asm volatile("s_waitcnt vmcnt(%0)\n s_barrier" :: "n"(NW(u)), "s"(la), "s"(as), "s"(zs), "v"(cc) : "memory");
                if (u >= 1 && u < 5) { u64 nm_; asm volatile("v_cmp_ne_u64 %0, %1, %2" : "=s"(nm_) : "s"(tag), "v"(fzv[u & 1]) : "memory"); miss &= nm_; }
                if (u < 4) FLAG_ISSUE(fzv[(u + 1) & 1], fz);
                if (u == 4) { NEEDZ(~0ull); stageB(25); }
                if (u == 14 && w == 3 && lane == 0) stflag(ws + WS_FK + b * 8, tag);
                if (u == 16 && b == 0 && w == 3 && lane == 0) stflag(ws + WS_E, tag);
                if constexpr (u == 0) {
                    cc = *(LAS f32x4*)(lds + L_CS + g * 16);
                    stageA(22);
                    stageB(21);
                } else {
                constexpr int q = u - 1, m = q & 1, nn = q >> 1;
                bf16x4 wt0, wt1;
                if (u >= 8 && u < 11) { LAS char* p = lds + L_WSCR + w * 3072 + (4 * g + q4) * 96 + (u - 8) * 32 + p4 * 8; wt0 = trread(p); wt1 = trread(p + 1536); }
                f32x4 a = az[m][nn];
                float u0, u1, u2, u3;
                asm volatile("s_mov_b32 m0, %15\n v_fma_f32 %4, %8, %12, %4\n buffer_load_dwordx4 %16, %20, %21 offen lds\n"
                             "v_fma_f32 %5, %9, %12, %5\n v_fma_f32 %6, %10, %12, %6\n v_fma_f32 %7, %11, %12, %7\n"
                             "buffer_load_dwordx4 %17, %20, %21 offen offset:1024 lds\n"
                             "v_mul_f32 %0, %4, %4\n v_mul_f32 %1, %5, %5\n v_mul_f32 %2, %6, %6\n v_mul_f32 %3, %7, %7\n"
                             "buffer_load_dwordx4 %18, %20, %21 offen offset:2048 lds\n"
                             "v_fma_f32 %0, %0, %14, %13\n v_fma_f32 %1, %1, %14, %13\n v_fma_f32 %2, %2, %14, %13\n v_fma_f32 %3, %3, %14, %13\n"
                             "buffer_load_dwordx4 %19, %20, %21 offen offset:3072 lds\n"
                             "v_mul_f32 %0, %0, %4\n v_mul_f32 %1, %1, %5\n v_mul_f32 %2, %2, %6\n v_mul_f32 %3, %3, %7"
                             : "=&v"(u0), "=&v"(u1), "=&v"(u2), "=&v"(u3), "+v"(a[0]), "+v"(a[1]), "+v"(a[2]), "+v"(a[3])
                             : "v"(cc[0]), "v"(cc[1]), "v"(cc[2]), "v"(cc[3]), "v"(nmb[m]), "v"(k1[m]), "v"(k2[m]),
                               "s"(la), "v"(vA[0]), "v"(vA[1]), "v"(vA[2]), "v"(vA[3]), "s"(rsp), "s"(as) : "memory");
                f32x4 c9;
                if (m == 1 && nn < 8) cc = *(LAS f32x4*)(lds + L_CS + (nn + 1) * 64 + g * 16);
                if (q == 15) c9 = *(LAS f32x4*)(lds + L_CS + 9 * 64 + g * 16);
                if (u < 4) NEEDZ(3ull << (2 * (tb - 18)));
                asm volatile("s_mov_b32 m0, %8\n v_exp_f32 %0, %0\n buffer_load_dwordx4 %9, %10, %11 offen sc1 lds\n"
                             "v_exp_f32 %1, %1\n v_exp_f32 %2, %2\n v_exp_f32 %3, %3\n"
                             "buffer_load_dwordx4 %9, %10, %11 offen offset:1024 sc1 lds\n"
                             "v_add_f32 %0, 1.0, %0\n v_add_f32 %1, 1.0, %1\n v_add_f32 %2, 1.0, %2\n v_add_f32 %3, 1.0, %3\n"
                             "buffer_load_dwordx4 %9, %10, %11 offen offset:2048 sc1 lds\n"
                             "v_rcp_f32 %0, %0\n v_rcp_f32 %1, %1\n v_rcp_f32 %2, %2\n v_rcp_f32 %3, %3\n"
                             "buffer_load_dwordx4 %9, %10, %11 offen offset:3072 sc1 lds\n"
                             "v_mul_f32 %0, %0, %4\n v_mul_f32 %1, %1, %5\n v_mul_f32 %2, %2, %6\n v_mul_f32 %3, %3, %7\n"
                             "s_mov_b32 m0, %12\n s_mov_b32 exec_hi, 0\n buffer_load_dwordx4 %9, %10, %13 offen sc1 lds\n s_mov_b32 exec_hi, -1\n"
                             "v_cvt_pk_bf16_f32 %0, %0, %1\n v_cvt_pk_bf16_f32 %1, %2, %3"
                             : "+v"(u0), "+v"(u1), "+v"(u2), "+v"(u3)
                             : "v"(a[0]), "v"(a[1]), "v"(a[2]), "v"(a[3]), "s"(lb), "v"(vl), "s"(wr), "s"(zs), "s"(lh + (tb & 3) * SLOT + SL_A), "s"(zs + 16384 - w * 3584) : "memory");
                if (u >= 8 && u < 11) st16c(rs, b < 216 ? vl : 0x7fffff00u, WS_WT + (b * 12 + w * 3 + u - 8) * 1024, __builtin_bit_cast(u32x4, cat8(wt0, wt1)));
                if (u == 6 || u == 7) {
                    const unsigned so = WS_KVT + ((b & 7) * 72 * S + (b >> 3) * 64) * 2, ko_ = l3 * (S * 2) + ((lane - (l3 >> 2) - 2 * w) & 7) * 16;
                    if (u == 6) st16c(rs, ko_, so + w * (8 * S * 2), kl[0]);
                    if (u == 6) st16c(rs, ko_, so + (w + 4) * (8 * S * 2), kl[1]);
                    if (u == 7) st16c(rs, w == 0 ? ko_ : 0x7fffff00u, so + 64 * S * 2, kl[2]);
                }
                if (q == 15) { cc[0] = adds(cc[0], c9[0]); cc[1] = adds(cc[1], c9[1]); cc[2] = adds(cc[2], c9[2]); cc[3] = adds(cc[3], c9[3]); }
                const int n4 = nn - (nn > 4), zq = nn == 4 ? 8 : m * 4 + (n4 >> 1), h = nn == 4 ? m : n4 & 1;
                zp[zq][h * 2] = __float_as_uint(u0); zp[zq][h * 2 + 1] = __float_as_uint(u1);
                asm volatile("" : "+v"(zp[zq]));
                }
            SEND
        }
        SCHED0();
        STAMP(6);
#pragma unroll 1
        for (int n = 38; n < 41; ++n) {
            WAIT_V(18);
            BAR();
            stageA(n + 3); stageB(n + 3);
        }
        FLAG_ISSUE(fzv[0], fk);
#pragma unroll 1
        for (int n = 41; n < 43; ++n) {
            WAIT_V(19);
            BAR();
            stageA(n + 3); stageB(n + 3);
        }
        WAIT_V(18);
        asm volatile("" : "+v"(fzv[0]));
        if (!allset(fzv[0], tag)) spin_flags(rs, fk, tag);
        FENCE();
        TAILS(); TAILS2();
        ConvIn cin;
        BAR();
        stageA(46); stageB(46);
        if (hasconv) conv_load(cin, hk, dv, wr, cd, lane);
        SCHED0();
        if (hasconv) WAIT_V(30); else WAIT_V(18);
        BAR();
        XST(52);
        stageA(47); stageB(47);
        if (hasconv) WAIT_V(30); else WAIT_V(18);
        BAR();
        stageA(48); stageB(48);
        WAIT_V(18);
        BAR();
        XST(53);
        stageA(49); stageB(49);
        WAIT_V(18);
        BAR();
        XST(54);
        G3_LDB(0); G3_LDB(1);
        SCHED0();
        WAIT_V(19);
        BAR();
        XST(55);
        G3_LDB(2);
        SCHED0();
        if (hasconv) { CONV_PIN(cin); conv_stage(cin, lds + redo, lane); }
        SCHED0();
        G3_LDB(3);
        SCHED0();
        WAIT_V(20);
        STAMP(7);
        BAR();
#pragma unroll
        for (int q = 0; q < 8; ++q) *(LAS u32x4*)(lds + w * 8192 + q * 1024 + lane * 16) = zp[q];
        *(LAS u32x4*)(lds + Z8 + w * 1024 + lane * 16) = zp[8];
        SCHED0();
        G3_LDB(4);
        SCHED0();
        WAIT_L0();
        BAR();
        __builtin_amdgcn_s_setprio(0);
        STAMP(8);
        if (hasconv) { conv_run(cin.dd, rs, lds + redo, cd, lane, [&]() { STAMP(9); FP_ISSUE(); G3_DMA(redo, sB, sB4); }); STAMP(20); FP_CHECK(22); }
        else {
            if (w == 2) G3_DMA(redo, sB, sB4);
            else {
#pragma unroll 1
                for (int k = 0; k < 5; ++k) {
                    const int v = k < 2 ? k + 2 : k == 2 ? 7 : k - 3;
                    if (k == 3) SPIN(M_RC, 3);
                    G3_DMA((v == 7 ? 4 : v) * RED_W, sB + (v - 7) * (9 * KST), sB4 + (v - 7) * (9 * KST));
                }
                WAIT_V(0);
                LDSW(M_FL + 12) = 4u;
            }
            STAMP(20);
            FP_ISSUE(); FP_CHECK(0);
        }
        }
    } else {
        if (tid < 12) *(LAS unsigned*)(lds + L_MISC + tid * 4) = 0u;
        {
            const char* pc; unsigned len, len2, back;
            asm volatile("s_getpc_b64 %0\n.Lpf_here%=:\n s_mov_b32 %1, .Lmega_tail+1024-.Lmega_head\n s_mov_b32 %2, .Lpf_here%=-.Lmega_head\n s_mov_b32 %3, .Lmega_cend-.Lmega_head" : "=s"(pc), "=s"(len), "=s"(back), "=s"(len2));
            const u32x4 rc = mk_rsrc(pc - back, len > len2 ? len : len2);
            if (wid < CPFW) pf4<0>(rc, L0 + L_DUMP, ((r * CPFW + wid) & 7) * 8192 + lane * 128, 0);
        }
        u32x4 zp[9];
        unsigned bsw[4], vo[3];
        const int wm = w & 1, wn = w >> 1;
#pragma unroll
        for (int m = 0; m < 4; ++m) asm volatile("buffer_load_ushort %0, %1, %2, %3 offen" : "=v"(bsw[m]) : "v"(fr * 2), "s"(mk_rsrc(b_sp, S * 2)), "s"((pi * 128 + 64 * wm + 16 * ((m + 2 * wn) & 3)) * 2) : "memory");
        const int kap = b / 3, th = b - kap * 3, kq = kap / 9, ke = kap - kq * 9, pr = ke & 3;
        const unsigned wos = th * 384 + w * 96 + (ke == 8 ? 18 * kq + 4 : 18 * kq + (ke >> 2) * 9 + 2 * pr + (pr >> 1)) * (16 * D * 2);
#pragma unroll
        for (int i = 0; i < 3; ++i) { const unsigned c = i * 64 + lane, rr = c / 6; vo[i] = (rr + (rr >= 16 && ke == 8 ? 128 : 0)) * (D * 2) + (c - rr * 6) * 16 - i * 1024; }
#pragma unroll
        for (int i = 0; i < 5; ++i) SET_VB(i);
        SET_VSP();
        const u32x4 rsp = mk_rsrc(w_sp, S * S * 2);
        const unsigned as0 = (pi * 128 + 32 * w) * (S * 2), k20 = opqs(walk(1) * 256);
        asm volatile("" : "+v"(vB[0]), "+v"(vB[1]), "+v"(vB[2]), "+v"(vB[3]), "+v"(vB[4]), "+v"(vA[0]), "+v"(vA[1]), "+v"(vA[2]), "+v"(vA[3]));
        asm volatile("" : "+v"(vo[0]), "+v"(vo[1]), "+v"(vo[2]));
        {
            f32x4 az[2][9];
#pragma unroll
            for (int n = 0; n < 9; ++n) az[0][n] = az[1][n] = zero4;
            unsigned e0 = L0 + (32 * w + fr) * 128 + ((g ^ (fr >> 1)) << 4), e1 = e0 ^ 64, pw = L0 + SL_A + bb;
            bf16x8 ax[2], bx[9], ay[2], by[9];
#define ZRD_X() do { ax[0] = RDP(e0, 0); ax[1] = RDP(e1, 2048); _Pragma("unroll") for (int n = 0; n < 9; ++n) bx[n] = TRP(pw, n * 32); } while (0)
#define ZRD_Y() do { ay[0] = RDP(e1, 0); ay[1] = RDP(e0, 2048); _Pragma("unroll") for (int n = 0; n < 9; ++n) by[n] = TRP(pw, n * 32 + 9216); } while (0)
#define ZMM(A, B, n0_, n1_) do { _Pragma("unroll") for (int n = n0_; n < n1_; ++n) { az[0][n] = mfma16(A[0], B[n], az[0][n]); az[1][n] = mfma16(A[1], B[n], az[1][n]); } } while (0)
#define ZSCHED(rest) do { _Pragma("unroll") for (int i_ = 0; i_ < 6; ++i_) { SGB(0x008, 1); SGB(0x100, 2); } _Pragma("unroll") for (int i_ = 0; i_ < 8; ++i_) { SGB(0x008, 1); SGB(0x100, 1); } if (rest) SGB(0x008, rest); SCHED0(); } while (0)
            XST(26);
            TAG_READY();
            XST(27);
            __builtin_amdgcn_s_setprio(CPRIO);
            BAR();
            STAMP(1);
            ZRD_X();
            WAIT_LB0();
#pragma unroll 1
            for (int t = 0; t < 8; ++t) {
                const int dl = (t & 3) == 3 ? -3 * SLOT : SLOT;
                ZRD_Y();
                ZMM(ax, bx, 0, 7);
                ZSCHED(0);
                ADV(e0, dl); ADV(e1, dl); ADV(pw, dl);
                ZMM(ax, bx, 7, 9); ZMM(ay, by, 0, 2);
                SCHED0();
                WAIT_LB0();
                BAR();
                ZRD_X();
                ZMM(ay, by, 2, 9);
                ZSCHED(0);
            }
            ZRD_Y();
            ZMM(ax, bx, 0, 7);
            ZSCHED(0);
            ZMM(ax, bx, 7, 9); ZMM(ay, by, 0, 2);
            SCHED0();
            WAIT_LB0();
            BAR();
            f32x4 st[4], cq[3];
#pragma unroll
            for (int j = 0; j < 4; ++j) st[j] = *(LAS f32x4*)(lds + L_ROWST + (16 * w + 4 * g + j) * 16);
#pragma unroll
            for (int j = 0; j < 3; ++j) cq[j] = *(LAS f32x4*)(lds + L_CS + fr * 48 + j * 16);
            ZMM(ay, by, 2, 9);
            SCHED_IL(7, 14);
            STAMP(2);
            f32x2 sum[4], sq[4], k1[4], k2[4];
            cq[2][0] += cq[2][1];
#pragma unroll
            for (int j = 0; j < 4; ++j) { const f32x2 rd = hi2(st[j]); k1[j] = rd * GC1; k2[j] = rd * rd * rd * GC2; }
#pragma unroll
            for (int n = 0; n < 9; ++n) {
                const float cc = -cq[n >> 2][n & 3];
#pragma unroll
                for (int j = 0; j < 4; ++j) {
                    f32x4& a = az[j >> 1][n];
                    const f32x2 v = fma2(lo2(st[j]), (f32x2){cc, cc}, (j & 1) ? hi2(a) : lo2(a));
                    const f32x2 e = v * fma2(v * v, k2[j], k1[j]);
                    const f32x2 d = (f32x2){__builtin_amdgcn_exp2f(e.x), __builtin_amdgcn_exp2f(e.y)} + (f32x2){1.0f, 1.0f};
                    const f32x2 z = v * (f32x2){__builtin_amdgcn_rcpf(d.x), __builtin_amdgcn_rcpf(d.y)};
                    a[(j & 1) * 2] = z.x; a[(j & 1) * 2 + 1] = z.y;
                    if (n == 0) { sum[j] = z; sq[j] = z * z; } else { sum[j] += z; sq[j] = fma2(z, z, sq[j]); }
                }
            }
#pragma unroll
            for (int j = 0; j < 4; ++j) { const f32x2 rd = hi2(st[j]); sum[j] *= rd; sq[j] *= rd * rd; asm volatile("" : "+v"(sum[j]), "+v"(sq[j])); }
            SCHED0();
            LDSW(M_RC + w * 4) = 1u;
            SCHED0();
            float r0 = sum[0].x, r1 = sq[0].x, r2 = sum[0].y, r3 = sq[0].y, r4 = sum[1].x, r5 = sq[1].x, r6 = sum[1].y, r7 = sq[1].y;
            asm volatile(
                "s_nop 1\n"
                "v_add_f32_dpp %0, %0, %0 row_mirror row_mask:0xf bank_mask:0x3\n v_add_f32_dpp %1, %1, %1 row_mirror row_mask:0xf bank_mask:0x3\n"
                "v_add_f32_dpp %2, %2, %2 row_mirror row_mask:0xf bank_mask:0x3\n v_add_f32_dpp %3, %3, %3 row_mirror row_mask:0xf bank_mask:0x3\n"
                "v_add_f32_dpp %4, %4, %4 row_mirror row_mask:0xf bank_mask:0x3\n v_add_f32_dpp %5, %5, %5 row_mirror row_mask:0xf bank_mask:0x3\n"
                "v_add_f32_dpp %6, %6, %6 row_mirror row_mask:0xf bank_mask:0x3\n v_add_f32_dpp %7, %7, %7 row_mirror row_mask:0xf bank_mask:0x3\n"
                "v_add_f32_dpp %0, %8, %8 row_mirror row_mask:0xf bank_mask:0xc\n v_add_f32_dpp %1, %9, %9 row_mirror row_mask:0xf bank_mask:0xc\n"
                "v_add_f32_dpp %2, %10, %10 row_mirror row_mask:0xf bank_mask:0xc\n v_add_f32_dpp %3, %11, %11 row_mirror row_mask:0xf bank_mask:0xc\n"
                "v_add_f32_dpp %4, %12, %12 row_mirror row_mask:0xf bank_mask:0xc\n v_add_f32_dpp %5, %13, %13 row_mirror row_mask:0xf bank_mask:0xc\n"
                "v_add_f32_dpp %6, %14, %14 row_mirror row_mask:0xf bank_mask:0xc\n v_add_f32_dpp %7, %15, %15 row_mirror row_mask:0xf bank_mask:0xc\n"
                "v_add_f32_dpp %0, %0, %0 row_half_mirror row_mask:0xf bank_mask:0x5\n v_add_f32_dpp %1, %1, %1 row_half_mirror row_mask:0xf bank_mask:0x5\n"
                "v_add_f32_dpp %2, %2, %2 row_half_mirror row_mask:0xf bank_mask:0x5\n v_add_f32_dpp %3, %3, %3 row_half_mirror row_mask:0xf bank_mask:0x5\n"
                "v_add_f32_dpp %0, %4, %4 row_half_mirror row_mask:0xf bank_mask:0xa\n v_add_f32_dpp %1, %5, %5 row_half_mirror row_mask:0xf bank_mask:0xa\n"
                "v_add_f32_dpp %2, %6, %6 row_half_mirror row_mask:0xf bank_mask:0xa\n v_add_f32_dpp %3, %7, %7 row_half_mirror row_mask:0xf bank_mask:0xa\n"
                "v_add_f32_dpp %0, %0, %0 quad_perm:[1,0,3,2] row_mask:0xf bank_mask:0xf\n v_add_f32_dpp %1, %1, %1 quad_perm:[1,0,3,2] row_mask:0xf bank_mask:0xf\n"
                "v_add_f32_dpp %2, %2, %2 quad_perm:[1,0,3,2] row_mask:0xf bank_mask:0xf\n v_add_f32_dpp %3, %3, %3 quad_perm:[1,0,3,2] row_mask:0xf bank_mask:0xf\n"
                "v_add_f32_dpp %0, %0, %0 quad_perm:[2,3,0,1] row_mask:0xf bank_mask:0xf\n v_add_f32_dpp %1, %1, %1 quad_perm:[2,3,0,1] row_mask:0xf bank_mask:0xf\n"
                "v_add_f32_dpp %2, %2, %2 quad_perm:[2,3,0,1] row_mask:0xf bank_mask:0xf\n v_add_f32_dpp %3, %3, %3 quad_perm:[2,3,0,1] row_mask:0xf bank_mask:0xf\n"
                : "+v"(r0), "+v"(r1), "+v"(r2), "+v"(r3), "+v"(r4), "+v"(r5), "+v"(r6), "+v"(r7)
                : "v"(sum[2].x), "v"(sq[2].x), "v"(sum[2].y), "v"(sq[2].y), "v"(sum[3].x), "v"(sq[3].x), "v"(sum[3].y), "v"(sq[3].y));
            const unsigned tag32 = (unsigned)tag, so2 = WS_ST2 + pi * 16384 + w * 256;
            st16c(rs, p4 == 0 ? lane * 4 : 0x7fffff00u, so2 + pj * 1024, rec4(r0, r1, r2, r3, tag32));
            SCHED0();
            STAMP(3);
            {
                const u32x4 rwi = mk_rsrc(w_in, D * F * 2);
                SPIN(M_RL, 1);
                DMA_W1(7);
                SPIN(M_RL, 2);
                DMA_W1(8);
            }
            {
                u32x4 pv[4];
                unsigned spins = 0;
                for (;;) {
                    FENCE();
#pragma unroll
                    for (int i = 0; i < 4; ++i) pv[i] = ld16c(rs, g * 1024 + fr * 16, so2 + i * 4096);
                    const unsigned bad = ((pv[0][3] ^ tag32) | (pv[1][3] ^ tag32)) | ((pv[2][3] ^ tag32) | (pv[3][3] ^ tag32));
                    if (__builtin_amdgcn_ballot_w64(bad != 0u) == 0ull || ++spins > SPIN_CAP) break;
                }
                LDSW(M_RC + w * 4) = 2u;
                STAMP(5);
                const f32x2 c = rec_consts<4>(pv, 1.0f / FH);
                LAS float* p = (LAS float*)(lds + L_ZST + w * 256 + fr * 16 + (lane >> 5) * 4);
                p[0] = muls(c.y, *(LAS float*)(lds + L_ROWST + (16 * w + fr) * 16 + 8 + (lane >> 5) * 4)); p[2] = muls(-c.x, c.y);
            }
            FENCE();
            const unsigned zdst = WS_Z2N + ((pj * 32 + pi * 2 + (w >> 1)) * 18 + (w & 1)) * 1024;
#pragma unroll
            for (int j = 0; j < 4; ++j) st[j] = *(LAS f32x4*)(lds + L_ZST + w * 256 + (4 * g + j) * 16);
#pragma unroll
            for (int n = 0; n < 9; ++n) {
#pragma unroll
                for (int j = 0; j < 4; ++j) { const f32x4 a = az[j >> 1][n]; zp[n][j] = norm2s((j & 1) ? hi2(a) : lo2(a), st[j]); }
                st16c(rs, lane * 16, zdst + n * 2048, zp[n]);
                SCHED0();
            }
        }
        STAMP(6);
        SPIN(M_RL, 6);
#pragma unroll
        for (int n = 0; n < 9; ++n) *(LAS u32x4*)(lds + (w < 2 ? T18 : T19) + SL_A + n * 2048 + (w & 1) * 1024 + lane * 16) = zp[n];
        {
            const u32x4 rwo = mk_rsrc(w_out, b < 216 ? FH * D * 2 : 0);
            dma4v(rsp, lw + T18, vA, as0 + pi * 256);
            dma4v(rsp, lw + T19, vA, as0 + pi * 256 + 128);
            dma4v(rsp, lw, vA, as0 + k20);
#pragma unroll
            for (int i = 0; i < 3; ++i) dma1n(i * 1024, rwo, L0 + L_WSCR + w * 3072, vo[i], wos);
        }
        STAMP(4);
        WAIT_V(15);
        if (lane == 0) stflag(ws + WS_FZ + ((pj * 16 + pi) * 4 + w) * 8, tag);
        STAMP(7);
        asm volatile("" : "+v"(bsw[0]), "+v"(bsw[1]), "+v"(bsw[2]), "+v"(bsw[3]));
        f32x4 ac[4][4], a4[2];
#pragma unroll
        for (int m = 0; m < 4; ++m) {
            const float bs = bflo(bsw[m]);
#pragma unroll
            for (int l = 0; l < 4; ++l) ac[m][l] = (f32x4){bs, bs, bs, bs};
            if (m < 2) a4[m] = (f32x4){bs, bs, bs, bs};
            asm volatile("" : "+v"(ac[m][0]), "+v"(ac[m][1]), "+v"(ac[m][2]), "+v"(ac[m][3]));
        }
        asm volatile("" : "+v"(a4[0]), "+v"(a4[1]));
        const int swz = (g ^ (fr >> 1)) << 4;
        unsigned x01 = L0 + T18 + (64 * wm + 32 * wn + fr) * 128 + swz, x23 = L0 + T18 + (64 * wm + 32 * (1 - wn) + fr) * 128 + swz, y01 = x01 ^ 64, y23 = x23 ^ 64;
        unsigned pb = L0 + T18 + SL_A + 5 * wn * 2048 + lane * 16, p4_ = L0 + T18 + SL_A + 4 * 2048 + lane * 16;
        bf16x8 ax[4], bx[5], ay[4], by[5];
#define MMA_L(B, A, l) do { _Pragma("unroll") for (int m = 0; m < 4; ++m) ac[m][l] = mfma16(B[l], A[m], ac[m][l]); } while (0)
#define MMA_4(B, A) do { a4[0] = mfma16(B[4], A[0], a4[0]); a4[1] = mfma16(B[4], A[1], a4[1]); } while (0)
#define RDX_B() do { bx[0] = RDP(pb, 0); bx[1] = RDP(pb, 2048); bx[2] = RDP(pb, 4096); bx[3] = RDP(pb, 6144); } while (0)
#define RDX_A() do { ax[0] = RDP(x01, 0); ax[1] = RDP(x01, 2048); ax[2] = RDP(x23, 0); ax[3] = RDP(x23, 2048); bx[4] = RDP(p4_, 0); } while (0)
#define RDY() do { by[0] = RDP(pb, 1024); ay[0] = RDP(y01, 0); ay[1] = RDP(y01, 2048); ay[2] = RDP(y23, 0); ay[3] = RDP(y23, 2048); \
        by[1] = RDP(pb, 3072); by[2] = RDP(pb, 5120); by[3] = RDP(pb, 7168); by[4] = RDP(p4_, 1024); } while (0)
        asm volatile("" : "+v"(x01), "+v"(x23), "+v"(y01), "+v"(y23), "+v"(pb), "+v"(p4_));
        SCHED0();
        STAMP(8);
        WAIT_V(12);
        WAIT_L0();
        BAR();
        STAMP(9);
        RDX_B();
        RDX_A();
        int e0 = T19 - T18 - SLOT, e1 = 3 * SLOT - T19;
#pragma unroll 1
        for (int t = 18; t < 49; ++t) {
            const int dl = ((t & 3) == 3 ? -3 * SLOT : SLOT) + e0;
            e0 = opqs(e1); e1 = 0;
            RDY();
            MMA_L(bx, ax, 0); MMA_L(bx, ax, 1); MMA_L(bx, ax, 2);
            SCHED_IL(9, 12);
            ADV(x01, dl); ADV(x23, dl); ADV(y01, dl); ADV(y23, dl); ADV(pb, dl); ADV(p4_, dl);
            MMA_L(bx, ax, 3);
            SGB(0x002, 2); SGB(0x008, 1); SGB(0x002, 2); SGB(0x008, 1); SGB(0x002, 2); SGB(0x008, 2);
            SCHED0();
            __builtin_amdgcn_s_waitcnt(0x0071);
            BAR();
            RDX_B();
            MMA_4(bx, ax); MMA_L(by, ay, 0);
            SCHED_IL(4, 6);
            asm volatile("" : "+v"(a4[0]), "+v"(a4[1]));
            RDX_A();
            MMA_L(by, ay, 1); MMA_L(by, ay, 2); MMA_L(by, ay, 3); MMA_4(by, ay);
            SCHED_IL(5, 14);
        }
        STAMP(10);
        RDY();
        MMA_L(bx, ax, 0); MMA_L(bx, ax, 1); MMA_L(bx, ax, 2); MMA_L(bx, ax, 3); MMA_4(bx, ax);
        SCHED_IL(9, 18);
        WAIT_LB0();
        BAR();
#pragma unroll
        for (int q = 0; q < 8; ++q) zp[q] = *(LAS u32x4*)(lds + ((4 * wm + (((q >> 1) + 2 * wn) & 3)) * 4 + 2 * wn + (q & 1)) * 1024 + lane * 16);
        zp[8] = *(LAS u32x4*)(lds + Z8 + (2 * wm + wn) * 1024 + lane * 16);
        MMA_L(by, ay, 0); MMA_L(by, ay, 1); MMA_L(by, ay, 2); MMA_L(by, ay, 3); MMA_4(by, ay);
        SCHED_IL(9, 18);
        STAMP(11);
        {
            const int kap0 = 9 * (pj >> 1) + 4 * (pj & 1) + 2 * wn, kap4 = 9 * (pj >> 1) + 8;
#pragma unroll
            for (int lp = 0; lp < 2; ++lp)
#pragma unroll
                for (int m = 0; m < 4; ++m) {
                    const unsigned dst = WS_P + (pi * 8 + 4 * wm + ((m + 2 * wn) & 3)) * (72 * 1024);
                    const u32x4 z = zp[m * 2 + lp];
                    const f32x4 a = ac[m][2 * lp], c = ac[m][2 * lp + 1];
                    st16c(rs, lane * 16, dst + (kap0 + lp) * 1024, (u32x4){mulz(z[0], lo2(a)), mulz(z[1], hi2(a)), mulz(z[2], lo2(c)), mulz(z[3], hi2(c))});
                    SCHED0();
                }
#pragma unroll
            for (int m = 0; m < 2; ++m)
                st8c(rs, lane * 16, WS_P + (pi * 8 + 4 * wm + 2 * wn + m) * (72 * 1024) + kap4 * 1024 + (pj & 1) * 8, (u32x2){mulz(zp[8][2 * m], lo2(a4[m])), mulz(zp[8][2 * m + 1], hi2(a4[m]))});
        }
        SCHED0();
        LDSW(M_RC + w * 4) = 3u;
        STAMP(12);
        TAILS(); TAILS2();
#pragma unroll
        for (int i = 0; i < CPRE; ++i) G3_LDB(i);
        SCHED0();
        WAIT_V(5 * CPRE);
        if (lane == 0) stflag(ws + WS_FP + ((pi * 16 + pj) * 4 + w) * 8, tag);
        __builtin_amdgcn_s_setprio(0);
        STAMP(13);
        SCHED0();
#pragma unroll
        for (int i = CPRE; i < RB; ++i) G3_LDB(i);
        SCHED0();
        FP_ISSUE();
        STAMP(20);
        FP_CHECK(0);
        WAIT_VB(0);
        asm volatile(".Lmega_cend:");
    }
    {
        const int lane = opqv(tid) & 63, fr = lane & 15, g = lane >> 4;
        TAILS(); TAILS2();
        f32x4 acc[4][5];
        STAMP(21);
        {
            bf16x8 aq[9][4];
            unsigned fw;
            G3_LDA(0); G3_LDA(1); G3_LDA(2); G3_LDA(3);
            SCHED0();
#pragma unroll
            for (int i = 0; i < 9; ++i) {
#pragma unroll
                for (int m = 0; m < 4; ++m)
#pragma unroll
                    for (int l = 0; l < 5; ++l) acc[m][l] = mfma16(bq[i][l], aq[i][m], i == 0 ? zero4 : acc[m][l]);
                SCHED0();
                XST(30 + i);
                if (i == 0 && hasconv && lane == 0) stflag(ws + WS_FY + cd * 8, tag);
                if (2 * i + 4 < 9) G3_LDA(2 * i + 4);
                if (2 * i + 5 < 9) G3_LDA(2 * i + 5);
                if (i == 2) fw = LDSW(M_FL + 12);
                if (i == 3) wcheck(fw, L0 + M_FL + 12, 4);
                if (i >= 3 && i + 2 < 9) G3_RDB(i + 2);
                SCHED0();
            }
        }
        STAMP(22);
        const int m = wid >> 1, l0 = 2 * (wid & 1);
        const int tA = 64 * rg + 16 * m + fr, dA = 16 * (n30 + l0) + 4 * g;
        const int tH = 64 * rg + 32 * (wid & 1) + 16 * (g >> 1) + fr, dH = 16 * n34 + 8 * (ct & 1) + 4 * (g & 1);
        const unsigned fy = WS_FY + (lane < 32 ? 16 * (n30 + l0) + lane : 16 * n34 + 8 * (ct & 1) + (lane & 7)) * 8;
        const u64 fyv = ldflag(rs, fy);
        u32x2 xv[3]; unsigned yv[3][4]; float mb[2], rd[2];
        xv[0] = *(const u32x2*)((const char*)x + (unsigned)((tA * D + dA) * 2));
        xv[1] = *(const u32x2*)((const char*)x + (unsigned)((tA * D + dA + 16) * 2));
        xv[2] = *(const u32x2*)((const char*)x + (unsigned)((tH * D + dH) * 2));
        mb[0] = __uint_as_float(ld4c(rs, WS_ST1 + (tA >> 1) * 16 + (tA & 1) * 4)); rd[0] = __uint_as_float(ld4c(rs, WS_ST1 + (tA >> 1) * 16 + (tA & 1) * 4 + 8));
        mb[1] = __uint_as_float(ld4c(rs, WS_ST1 + (tH >> 1) * 16 + (tH & 1) * 4)); rd[1] = __uint_as_float(ld4c(rs, WS_ST1 + (tH >> 1) * 16 + (tH & 1) * 4 + 8));
        SCHED0();
        LAS char* red = lds + redo;
#pragma unroll
        for (int mm = 0; mm < 2; ++mm)
#pragma unroll
            for (int l = 0; l < 4; ++l) *(LAS f32x4*)(red + (mm * 4 + l) * 1024 + lane * 16) = acc[mm][l];
        SCHED0();
        if (!allset(fyv, tag)) spin_flags(rs, fy, tag);
        asm volatile("" ::: "memory");
#pragma unroll
        for (int rr = 0; rr < 4; ++rr) {
            yv[0][rr] = ld2c(rs, (dA * S + tA) * 2, WS_YT + rr * (S * 2));
            yv[1][rr] = ld2c(rs, (dA * S + tA) * 2, WS_YT + (16 + rr) * (S * 2));
            yv[2][rr] = ld2c(rs, (dH * S + tH) * 2, WS_YT + rr * (S * 2));
        }
        SCHED0();
#pragma unroll
        for (int mm = 2; mm < 4; ++mm)
#pragma unroll
            for (int l = 0; l < 4; ++l) *(LAS f32x4*)(red + (mm * 4 + l) * 1024 + lane * 16) = acc[mm][l];
        if ((g >> 1) == (ct & 1)) {
#pragma unroll
            for (int mm = 0; mm < 4; ++mm) *(LAS f32x4*)(red + 16384 + mm * 512 + (lane & 31) * 16) = acc[mm][4];
        }
        STAMP(23);
        asm volatile("s_waitcnt lgkmcnt(0)\n s_barrier\n.Lmega_tail:" ::: "memory");
        STAMP(24);
#pragma unroll
        for (int u = 0; u < 3; ++u) {
            if (u == 2 && wid >= 2) break;
            const int off = (u < 2 ? (2 * wid + u) * 1024 : 16384 + wid * 1024) + lane * 16;
            f32x4 o = zero4;
#pragma unroll
            for (int v = 0; v < 4; ++v) o += *(LAS f32x4*)(lds + v * RED_W + off) + *(LAS f32x4*)(lds + (v < 3 ? CV_BASE + v * CV_WAVE : 4 * RED_W) + off);
            float res[4];
#pragma unroll
            for (int rr = 0; rr < 4; ++rr) {
                const float ob = rbf(o[rr] * rd[u >> 1]);
                const float sg = rbf(ob * __builtin_amdgcn_rcpf(1.0f + __builtin_amdgcn_exp2f(-1.4426950408889634f * ob)));
                const float xx = (rr & 1) ? bfhi(xv[u][rr >> 1]) : bflo(xv[u][rr >> 1]);
                res[rr] = xx + rbf(rbf(rbf(rbf(xx - mb[u >> 1]) * rd[u >> 1]) * bflo(yv[u][rr])) * sg);
            }
            const unsigned oo = u < 2 ? (tA * D + dA + 16 * u) * 2 : (tH * D + dH) * 2;
            *(u32x2*)((char*)out + oo) = (u32x2){pk2(res[0], res[1]), pk2(res[2], res[3])};
        }
    }
}

extern "C" void kernel_launch(void* const* d_in, const int* in_sizes, int n_in,
                              void* d_out, int out_size, void* d_ws, size_t ws_size,
                              hipStream_t stream) {
    (void)hipFuncSetAttribute((const void*)mega, hipFuncAttributeMaxDynamicSharedMemorySize, LDS_SZ);
    mega<<<256, 512, LDS_SZ, stream>>>((const u16*)d_in[0], (const u16*)d_in[1], (const u16*)d_in[2], (const u16*)d_in[3], (const u16*)d_in[4], (const u16*)d_in[5],
                                       (const u16*)d_in[7], (const u16*)d_in[8], (const u16*)d_in[9], (const u16*)d_in[10], (char*)d_ws, (u16*)d_out);
}
```

```cpp
#include <hip/hip_runtime.h>
#include <stdint.h>
#define CODE_PF

#define LAS __attribute__((address_space(3)))
#define GAS __attribute__((address_space(1)))
typedef unsigned short u16;
typedef unsigned long long u64;
typedef short bf16x8 __attribute__((ext_vector_type(8)));
typedef short bf16x4 __attribute__((ext_vector_type(4)));
typedef float f32x4 __attribute__((ext_vector_type(4)));
typedef float f32x2 __attribute__((ext_vector_type(2)));
typedef unsigned u32x4 __attribute__((ext_vector_type(4)));
typedef unsigned u32x2 __attribute__((ext_vector_type(2)));
typedef __bf16 bf16x2_t __attribute__((ext_vector_type(2)));

constexpr int S = 2048, D = 576, F = 4608, FH = 2304;

__device__ __forceinline__ float bflo(unsigned w) { return __uint_as_float(w << 16); }
__device__ __forceinline__ float bfhi(unsigned w) { return __uint_as_float(w & 0xffff0000u); }
__device__ __forceinline__ unsigned pk2v(f32x2 p) { return __builtin_bit_cast(unsigned, __builtin_convertvector(p, bf16x2_t)); }
__device__ __forceinline__ unsigned pk2(float lo, float hi) { return pk2v((f32x2){lo, hi}); }
__device__ __forceinline__ f32x2 up2(unsigned w) { return (f32x2){bflo(w), bfhi(w)}; }
__device__ __forceinline__ float rbf(float x) { return bflo(pk2(x, x)); }
__device__ __forceinline__ f32x2 rbf2(f32x2 x) { return up2(pk2v(x)); }
__device__ __forceinline__ f32x2 fma2(f32x2 a, f32x2 b, f32x2 c) { return __builtin_elementwise_fma(a, b, c); }
__device__ __forceinline__ f32x2 lo2(f32x4 a) { return (f32x2){a[0], a[1]}; }
__device__ __forceinline__ f32x2 hi2(f32x4 a) { return (f32x2){a[2], a[3]}; }
__device__ __forceinline__ f32x4 mfma16(bf16x8 a, bf16x8 b, f32x4 c) { return __builtin_amdgcn_mfma_f32_16x16x32_bf16(a, b, c, 0, 0, 0); }
__device__ __forceinline__ bf16x4 trread(LAS char* p) { return __builtin_bit_cast(bf16x4, __builtin_amdgcn_ds_read_tr16_b64_v4i16((LAS bf16x4*)p)); }
__device__ __forceinline__ bf16x8 cat8(bf16x4 a, bf16x4 b) { return __builtin_shufflevector(a, b, 0, 1, 2, 3, 4, 5, 6, 7); }
#define WAIT_V(n) asm volatile("s_waitcnt vmcnt(%0)" ::"n"(n) : "memory")
#define WAIT_VB(n) __builtin_amdgcn_s_waitcnt(((n) & 15) | (7 << 4) | (15 << 8) | (((n) >> 4) << 14))
#define WAIT_L0() asm volatile("s_waitcnt lgkmcnt(0)" ::: "memory")
#define BAR() do { asm volatile("" ::: "memory"); __builtin_amdgcn_s_barrier(); asm volatile("" ::: "memory"); } while (0)
#define SCHED0() __builtin_amdgcn_sched_barrier(0)
#define RLX_AGENT __ATOMIC_RELAXED, __HIP_MEMORY_SCOPE_AGENT
#define PIN9(z) asm volatile("" : "+v"(z[0]), "+v"(z[1]), "+v"(z[2]), "+v"(z[3]), "+v"(z[4]), "+v"(z[5]), "+v"(z[6]), "+v"(z[7]), "+v"(z[8]))
__device__ __forceinline__ int opqs(int x) { asm volatile("" : "+s"(x)); return x; }
__device__ __forceinline__ int opqv(int x) { asm volatile("" : "+v"(x)); return x; }
template <int I> struct IC { static constexpr int v = I; };
template <int A, int B, class Fn> __device__ __forceinline__ void sfor(Fn&& f) { if constexpr (A < B) { f(IC<A>{}); sfor<A + 1, B>(f); } }
#define SFOR(i, a, b) sfor<a, b>([&](auto i##_) __attribute__((always_inline)) { constexpr int i = decltype(i##_)::v;
#define SEND });
#define RFL(x) __builtin_amdgcn_readfirstlane(x)

#define TIMER "s_memrealtime"
#define STAMP(i) do { } while (0)

__device__ __forceinline__ f32x2 gelu2(f32x2 v) {
    const float c1 = -2.0f * 0.7978845608028654f * 1.4426950408889634f;
    const float c2 = c1 * 0.044715f;
    f32x2 a = v * fma2(v * v, (f32x2){c2, c2}, (f32x2){c1, c1});
    f32x2 d = (f32x2){__builtin_amdgcn_exp2f(a.x), __builtin_amdgcn_exp2f(a.y)} + (f32x2){1.0f, 1.0f};
    return v * (f32x2){__builtin_amdgcn_rcpf(d.x), __builtin_amdgcn_rcpf(d.y)};
}
__device__ __forceinline__ float adds(float a, float b) { float o; asm("v_add_f32 %0, %1, %2" : "=v"(o) : "v"(a), "v"(b)); return o; }
__device__ __forceinline__ float muls(float a, float b) { float o; asm("v_mul_f32 %0, %1, %2" : "=v"(o) : "v"(a), "v"(b)); return o; }
__device__ __forceinline__ f32x2 ln_consts(float sm, float sq, float inv_n) {
    float mean, m2;
    asm("v_mul_f32 %0, %2, %3\n v_mul_f32 %1, %2, %4" : "=&v"(mean), "=v"(m2) : "s"(inv_n), "v"(sm), "v"(sq));
    const float var = fmaxf(fmaf(-mean, mean, m2), 0.f);
    return (f32x2){rbf(mean), __builtin_amdgcn_rcpf(rbf(__builtin_amdgcn_sqrtf(rbf(rbf(var) + 1e-5f))))};
}

constexpr unsigned SZ_Z = S * FH * 2, SZ_X = S * D * 2;
constexpr unsigned WS_Z2N = 0;
constexpr unsigned WS_P = WS_Z2N + SZ_Z;
constexpr unsigned WS_KVT = WS_P + SZ_Z;
constexpr unsigned WS_YT = WS_KVT + SZ_X;
constexpr unsigned WS_WT = WS_YT + SZ_X;
constexpr unsigned WS_ST2 = WS_WT + FH * D * 2;
constexpr unsigned WS_ST1 = WS_ST2 + 256 * 1024;
constexpr unsigned WS_SR = WS_ST1 + 16384;
constexpr unsigned WS_SC = WS_SR + 16 * 12288;
constexpr unsigned WS_FZ = WS_SC + 32 * 12288;
constexpr unsigned WS_FP = WS_FZ + 8192;
constexpr unsigned WS_FK = WS_FP + 8192;
constexpr unsigned WS_FY = WS_FK + 2048;
constexpr unsigned WS_E = WS_FY + 4608 + 128;
constexpr unsigned WS_END = WS_E + 128;
constexpr u64 TAG_INC = 0x9E3779B97F4A7C15ull;

__device__ __forceinline__ __amdgpu_buffer_rsrc_t ws_rsrc(char* ws) { return __builtin_amdgcn_make_buffer_rsrc(ws, 0, (int)WS_END, 0x00020000); }
__device__ __forceinline__ void st16c(__amdgpu_buffer_rsrc_t r, unsigned off, unsigned soff, u32x4 v) { __builtin_amdgcn_raw_buffer_store_b128(v, r, off, soff, 16); asm volatile("s_nop 1" :: "v"(v)); }
__device__ __forceinline__ void st8c(__amdgpu_buffer_rsrc_t r, unsigned off, unsigned soff, u32x2 v) { __builtin_amdgcn_raw_buffer_store_b64(v, r, off, soff, 16); }
__device__ __forceinline__ u32x4 ld16c(__amdgpu_buffer_rsrc_t r, unsigned off, unsigned soff) { return __builtin_amdgcn_raw_buffer_load_b128(r, off, soff, 16); }
__device__ __forceinline__ unsigned ld4c(__amdgpu_buffer_rsrc_t r, unsigned off) { return __builtin_amdgcn_raw_buffer_load_b32(r, off, 0, 16); }
__device__ __forceinline__ unsigned ld2c(__amdgpu_buffer_rsrc_t r, unsigned off, unsigned soff) { return __builtin_amdgcn_raw_buffer_load_b16(r, off, soff, 16); }
__device__ __forceinline__ __amdgpu_buffer_rsrc_t in_rsrc(const void* p, unsigned bytes) { return __builtin_amdgcn_make_buffer_rsrc((void*)p, 0, (int)bytes, 0x00020000); }
__device__ __forceinline__ u32x4 mk_rsrc(const void* p, unsigned bytes) { const u64 a = (u64)p; return (u32x4){(unsigned)a, (unsigned)(a >> 32) & 0xffffu, bytes, 0x00020000u}; }
#define DMA_HEAD "s_add_i32 m0, %1, %5\n s_add_i32 %0, %4, %6\n buffer_load_dwordx4 %2, %3, %0 offen"
#define DMA_NEXT(o) "\n buffer_load_dwordx4 %2, %3, %0 offen offset:" #o " sc1 lds"
#define DMA_OPS : "=&s"(t) : "s"(lb), "v"(voff), "s"(r), "s"(sb), "n"(LI), "n"(SI) : "scc", "memory"
template <int LI, int SI> __device__ __forceinline__ void dma16(u32x4 r, unsigned lb, unsigned voff, unsigned sb) { unsigned t; asm volatile(DMA_HEAD " lds" DMA_OPS); }
template <int LI, int SI> __device__ __forceinline__ void dma16c(u32x4 r, unsigned lb, unsigned voff, unsigned sb) { unsigned t; asm volatile(DMA_HEAD " sc1 lds" DMA_OPS); }
template <int LI, int SI> __device__ __forceinline__ void dma16c3(u32x4 r, unsigned lb, unsigned voff, unsigned sb) { unsigned t; asm volatile(DMA_HEAD " sc1 lds" DMA_NEXT(1024) DMA_NEXT(2048) DMA_OPS); }
template <int LI, int SI> __device__ __forceinline__ void dma16c4(u32x4 r, unsigned lb, unsigned voff, unsigned sb) { unsigned t; asm volatile(DMA_HEAD " sc1 lds" DMA_NEXT(1024) DMA_NEXT(2048) DMA_NEXT(3072) DMA_OPS); }
template <int SI> __device__ __forceinline__ void pf4(u32x4 r, unsigned lb, unsigned voff, unsigned sb) {
    unsigned t;
    asm volatile("s_mov_b32 m0, %1\n s_add_i32 %0, %4, %5\n buffer_load_dword %2, %3, %0 offen lds" : "=&s"(t) : "s"(lb), "v"(voff), "s"(r), "s"(sb), "n"(SI) : "scc", "memory");
}
template <int SI> __device__ __forceinline__ void pf4c(u32x4 r, unsigned lb, unsigned voff, unsigned sb) {
    unsigned t;
    asm volatile("s_mov_b32 m0, %1\n s_add_i32 %0, %4, %5\n buffer_load_dword %2, %3, %0 offen sc1 lds" : "=&s"(t) : "s"(lb), "v"(voff), "s"(r), "s"(sb), "n"(SI) : "scc", "memory");
}
__device__ __forceinline__ void dma4v(u32x4 r, unsigned lb, const unsigned* v, unsigned sb) {
    asm volatile("s_mov_b32 m0, %0\n s_nop 0\n buffer_load_dwordx4 %1, %5, %6 offen lds\n buffer_load_dwordx4 %2, %5, %6 offen offset:1024 lds\n"
                 "buffer_load_dwordx4 %3, %5, %6 offen offset:2048 lds\n buffer_load_dwordx4 %4, %5, %6 offen offset:3072 lds"
                 :: "s"(lb), "v"(v[0]), "v"(v[1]), "v"(v[2]), "v"(v[3]), "s"(r), "s"(sb) : "memory");
}
__device__ __forceinline__ void dmah(u32x4 r, unsigned lb, unsigned v, unsigned sb) {
    asm volatile("s_mov_b32 m0, %0\n s_mov_b32 exec_hi, 0\n buffer_load_dwordx4 %1, %2, %3 offen lds\n s_mov_b32 exec_hi, -1" :: "s"(lb), "v"(v), "s"(r), "s"(sb) : "memory");
}
__device__ __forceinline__ void dmahc(u32x4 r, unsigned lb, unsigned v, unsigned sb) {
    asm volatile("s_mov_b32 m0, %0\n s_mov_b32 exec_hi, 0\n buffer_load_dwordx4 %1, %2, %3 offen sc1 lds\n s_mov_b32 exec_hi, -1" :: "s"(lb), "v"(v), "s"(r), "s"(sb) : "memory");
}
__device__ __forceinline__ u64 ldflag(__amdgpu_buffer_rsrc_t r, unsigned off) {
    asm volatile("" ::: "memory");
    return __builtin_bit_cast(u64, __builtin_amdgcn_raw_buffer_load_b64(r, off, 0, 16));
}
__device__ __forceinline__ void stflag(char* p, u64 v) { __hip_atomic_store((GAS u64*)p, v, RLX_AGENT); }
__device__ __forceinline__ bool allset(u64 v, u64 tag) { return __builtin_amdgcn_ballot_w64(v != tag) == 0ull; }

constexpr int SL_A = 16384, SL_B = 18432, SLOT = SL_A + SL_B;
constexpr unsigned SPIN_CAP = 1u << 20;
constexpr int L_ROWST = 145408;
constexpr int L_CS = L_ROWST + 1024;
constexpr int L_ZST = L_CS + 1024;
constexpr int L_WSCR = L_ZST + 1024;
constexpr int LDS_SZ = 163840;
constexpr int L_DUMP = LDS_SZ - 256;
constexpr int L_MISC = L_DUMP - 256;
constexpr int M_RL = L_MISC, M_FL = L_MISC + 16, M_RC = L_MISC + 32;
constexpr int CV_U = 512 + 4096, CV_HSTRIDE = 4160, CV_WAVE = CV_U + 4 * CV_HSTRIDE;
constexpr int RED_W = 18432, ZS = 2 * SLOT;
__host__ __device__ constexpr int redoff(int v) { return v < 4 ? 256 + CV_WAVE + v * RED_W : v == 7 ? 256 + CV_WAVE + 4 * RED_W : v == 6 ? 256 : (v == 4 ? L_MISC : 4 * SLOT) - CV_WAVE; }
#define Z8OFF(w_) (ZS + 32768 + (w_) * 1024 + ((w_) >> 1) * SLOT)
constexpr u64 redtab(int v) { return (u64)(redoff(v) >> 8) | (u64)(redoff(v + 1) >> 8) << 16 | (u64)(redoff(v + 2) >> 8) << 32 | (u64)(redoff(v + 3) >> 8) << 48; }
__host__ __device__ constexpr int warea(int j) { return 16384 + 18432 * (j == 0 || j == 7 ? 1 : j == 1 || j == 8 ? 0 : j == 2 ? 3 : j == 3 ? 5 : j == 4 ? 4 : j == 5 ? 6 : 2); }
constexpr int T18 = warea(2), T19 = warea(3), KVS = warea(6);
typedef unsigned u32x3 __attribute__((ext_vector_type(3)));

#define SCHED_IL(nr, nm) do { _Pragma("unroll") for (int i_ = 0; i_ < (nr); ++i_) { __builtin_amdgcn_sched_group_barrier(0x008, 1, 0); __builtin_amdgcn_sched_group_barrier(0x100, 1, 0); } \
        __builtin_amdgcn_sched_group_barrier(0x008, (nm) - (nr), 0); SCHED0(); } while (0)

__device__ __forceinline__ void spin_flags(__amdgpu_buffer_rsrc_t r, unsigned p, u64 tag) {
    unsigned spins = 0;
    while (!allset(ldflag(r, p), tag)) { if (++spins > SPIN_CAP) break; __builtin_amdgcn_s_sleep(1); }
    asm volatile("" ::: "memory");
}
__device__ __forceinline__ void dma1(int OFF, u32x4 r, unsigned lb, unsigned v, unsigned sb) {
    asm volatile("s_mov_b32 m0, %0\n s_nop 0\n buffer_load_dwordx4 %1, %2, %3 offen offset:%4 lds" :: "s"(lb), "v"(v), "s"(r), "s"(sb), "n"(OFF) : "memory");
}
#define WNT " nt"
#define KNT ""
__device__ __forceinline__ void dma1n(int OFF, u32x4 r, unsigned lb, unsigned v, unsigned sb) {
    asm volatile("s_mov_b32 m0, %0\n s_nop 0\n buffer_load_dwordx4 %1, %2, %3 offen offset:%4" WNT " lds" :: "s"(lb), "v"(v), "s"(r), "s"(sb), "n"(OFF) : "memory");
}
__device__ __forceinline__ void dma1c(int OFF, u32x4 r, unsigned lb, unsigned v, unsigned sb) {
    asm volatile("s_mov_b32 m0, %0\n s_nop 0\n buffer_load_dwordx4 %1, %2, %3 offen offset:%4 sc1 lds" :: "s"(lb), "v"(v), "s"(r), "s"(sb), "n"(OFF) : "memory");
}
__device__ __forceinline__ u32x4 rec4(float a, float b, float c, float d, unsigned tag) {
    const unsigned q = __float_as_uint(d);
    return (u32x4){__builtin_amdgcn_perm(__float_as_uint(a), q, 0x07060501u), __builtin_amdgcn_perm(__float_as_uint(b), q, 0x07060502u), __builtin_amdgcn_perm(__float_as_uint(c), q, 0x07060503u), tag};
}
__device__ __forceinline__ float half_sum(float a, float b) {
    asm("s_nop 1\n v_permlane32_swap_b32 %0, %1\n v_add_f32 %0, %0, %1\n v_mov_b32 %1, %0\n s_nop 1\n v_permlane16_swap_b32 %0, %1\n v_add_f32 %0, %0, %1" : "+v"(a), "+v"(b));
    return a;
}
template <int N> __device__ __forceinline__ f32x2 rec_consts(const u32x4* pv, float inv_n) {
    float a0, a1, a2, a3;
#pragma unroll
    for (int i = 0; i < N; ++i) {
        const float e = __uint_as_float(__builtin_amdgcn_perm(pv[i][2], __builtin_amdgcn_perm(pv[i][1], pv[i][0], 0x0c04000cu), 0x04020100u));
        if (i == 0) { a0 = __uint_as_float(pv[i][0]); a1 = __uint_as_float(pv[i][1]); a2 = __uint_as_float(pv[i][2]); a3 = e; }
        else { a0 = adds(a0, __uint_as_float(pv[i][0])); a1 = adds(a1, __uint_as_float(pv[i][1])); a2 = adds(a2, __uint_as_float(pv[i][2])); a3 = adds(a3, e); }
    }
    return ln_consts(half_sum(a0, a2), half_sum(a1, a3), inv_n);
}
__device__ __forceinline__ void wcheck(unsigned f, unsigned addr, unsigned need) {
    unsigned cnt;
    asm volatile("v_cmp_gt_u32 vcc, %3, %0\n s_cbranch_vccz 2f\n s_mov_b32 %1, 0x400000\n"
                 "1:\n s_sleep 1\n ds_read_b32 %0, %2\n s_sub_u32 %1, %1, 1\n s_waitcnt lgkmcnt(0)\n s_cbranch_scc1 2f\n v_cmp_gt_u32 vcc, %3, %0\n s_cbranch_vccnz 1b\n2:"
                 : "+v"(f), "=&s"(cnt) : "v"(addr), "n"(need) : "vcc", "scc", "memory");
}
__device__ __forceinline__ unsigned norm2s(f32x2 z, f32x4 st) {
    unsigned o, t;
    asm("v_fma_f32 %0, %2, %4, %6\n v_fma_f32 %1, %3, %5, %7\n v_cvt_pk_bf16_f32 %0, %0, %1" : "=&v"(o), "=&v"(t) : "v"(z.x), "v"(z.y), "v"(st[0]), "v"(st[1]), "v"(st[2]), "v"(st[3]));
    return o;
}
__device__ __forceinline__ float tap4s(float k0, float k1, float k2, float k3, const float* h, float v) {
    float o;
    asm("v_mul_f32 %0, %1, %8\n v_fma_f32 %0, %2, %7, %0\n v_fma_f32 %0, %3, %6, %0\n v_fma_f32 %0, %4, %5, %0\n v_mul_f32 %0, %0, %9"
        : "=&v"(o) : "v"(k0), "v"(k1), "v"(k2), "v"(k3), "v"(h[0]), "v"(h[1]), "v"(h[2]), "v"(h[3]), "v"(v));
    return o;
}

__device__ __forceinline__ float fmas(float a, float b, float c) { float o; asm("v_fma_f32 %0, %1, %2, %3" : "=v"(o) : "v"(a), "v"(b), "v"(c)); return o; }
__device__ __forceinline__ unsigned mulz(unsigned z, f32x2 a) {
    unsigned o, t;
    asm("v_lshlrev_b32 %0, 16, %2\n v_and_b32 %1, 0xffff0000, %2\n v_mul_f32 %0, %0, %3\n v_mul_f32 %1, %1, %4\n v_cvt_pk_bf16_f32 %0, %0, %1" : "=&v"(o), "=&v"(t) : "v"(z), "v"(a.x), "v"(a.y));
    return o;
}
__device__ __forceinline__ unsigned movs(unsigned a) { unsigned o; asm("v_mov_b32 %0, %1" : "=v"(o) : "v"(a)); return o; }
__device__ __forceinline__ unsigned algn(unsigned hi, unsigned lo) { unsigned o; asm("v_alignbit_b32 %0, %1, %2, 16" : "=v"(o) : "v"(hi), "v"(lo)); return o; }
__device__ __forceinline__ unsigned algn2(unsigned hi, unsigned lo) { unsigned o; asm("v_alignbit_b32 %0, %1, %2, 16 " : "=v"(o) : "v"(hi), "v"(lo)); return o; }

#define YPRIO 0
struct ConvIn { u32x4 uu[4], hh[4]; u32x2 hn[4]; unsigned dd; };
#define ALD(w_, dst, vo, r_, so, x_) asm volatile("buffer_load_dword" w_ " %0, %1, %2, %3 offen" x_ : "=v"(dst) : "v"(vo), "s"(r_), "s"(so) : "memory")
__device__ __forceinline__ void conv_load(ConvIn& c, const u16* __restrict__ hk, const u16* __restrict__ dv, u32x4 wr, int d, int lane) {
    const u32x4 rh = mk_rsrc(hk, SZ_X);
    const unsigned v = lane * 16, su = WS_KVT + d * (S * 2), sh = d * (S * 2);
    ALD("x4", c.uu[0], v, wr, su, " sc1"); ALD("x4", c.uu[1], v, wr, su, " offset:1024 sc1"); ALD("x4", c.uu[2], v, wr, su, " offset:2048 sc1"); ALD("x4", c.uu[3], v, wr, su, " offset:3072 sc1");
    ALD("x4", c.hh[0], v, rh, sh, ""); ALD("x4", c.hh[1], v, rh, sh, " offset:1024"); ALD("x4", c.hh[2], v, rh, sh, " offset:2048"); ALD("x4", c.hh[3], v, rh, sh, " offset:3072");
    ALD("x2", c.hn[0], v, rh, sh, " offset:16"); ALD("x2", c.hn[1], v, rh, sh, " offset:1040"); ALD("x2", c.hn[2], v, rh, sh, " offset:2064"); ALD("x2", c.hn[3], v, rh, sh, " offset:3088");
    c.dd = ((const unsigned*)dv)[d >> 1];
}
#define CONV_PIN(c) asm volatile("" : "+v"(c.uu[0]), "+v"(c.uu[1]), "+v"(c.uu[2]), "+v"(c.uu[3]), "+v"(c.hh[0]), "+v"(c.hh[1]), "+v"(c.hh[2]), "+v"(c.hh[3]), "+v"(c.hn[0]), "+v"(c.hn[1]), "+v"(c.hn[2]), "+v"(c.hn[3]))
template <class MID> __device__ __forceinline__ void conv_stage(ConvIn& c, LAS char* U, int lane, MID mid) {
    LAS char* H = U + CV_U;
    if (lane == 63) c.hn[3] = (u32x2){0u, 0u};
    if (lane < 32) *(LAS u32x4*)(U + lane * 16) = (u32x4){0u, 0u, 0u, 0u};
    const unsigned h0 = RFL(c.hh[0][0]), h1 = RFL(c.hh[0][1]);
    if (lane < 8) {
        const int cc = lane >> 1, half = lane & 1;
        u32x4 z = {0u, 0u, 0u, 0u};
        if (half) {
            if (cc == 1) z[3] = h0 << 16;
            if (cc == 2) z[3] = h0;
            if (cc == 3) { z[2] = h0 << 16; z[3] = __builtin_amdgcn_alignbit(h1, h0, 16); }
        }
        *(LAS u32x4*)(H + cc * CV_HSTRIDE + half * 16) = z;
    }
    SFOR(i, 0, 4)
        SCHED0();
        mid(IC<i>{});
        SCHED0();
        *(LAS u32x4*)(U + 512 + i * 1024 + lane * 16) = c.uu[i];
        const unsigned w0 = c.hh[i][0], w1 = c.hh[i][1], w2 = c.hh[i][2], w3 = c.hh[i][3], w4 = c.hn[i][0], w5 = c.hn[i][1];
        LAS char* p = H + 32 + i * 1024 + lane * 16;
        *(LAS u32x4*)(p) = c.hh[i];
        *(LAS u32x4*)(p + CV_HSTRIDE) = (u32x4){algn(w1, w0), algn(w2, w1), algn(w3, w2), algn(w4, w3)};
        *(LAS u32x4*)(p + 2 * CV_HSTRIDE) = (u32x4){movs(w1), movs(w2), movs(w3), movs(w4)};
        *(LAS u32x4*)(p + 3 * CV_HSTRIDE) = (u32x4){algn2(w2, w1), algn2(w3, w2), algn2(w4, w3), algn2(w5, w4)};
    SEND
}
__device__ __forceinline__ void conv_run(unsigned ddw, __amdgpu_buffer_rsrc_t rs, LAS char* U, int d, int lane) {
    const int fr = lane & 15, g = lane >> 4;
    LAS char* H = U + CV_U;
    const int e0 = 16 * (g >> 1) - 8 * (g & 1) - 7 + fr + 16;
    const int ac = e0 & 3;
    LAS char* ap = H + ac * CV_HSTRIDE + (e0 - ac) * 2;
    LAS char* bp = U + 512 + 32 * (fr - (g >> 1)) + 16 * (g & 1) - 448;
    f32x4 acc[8];
#pragma unroll
    for (int n = 0; n < 8; ++n) acc[n] = (f32x4){0.f, 0.f, 0.f, 0.f};
    bf16x8 LA[2][4], LB[2][4], HA[4], HB[4];
#define CV_RD(A, B, a0, pa, pb) do { _Pragma("unroll") for (int a = 0; a < 4; ++a) { B[a] = *(LAS bf16x8*)((pb) + (a0 + a) * 512); \
        A[a] = cat8(*(LAS bf16x4*)((pa) + (a0 + a) * 512), *(LAS bf16x4*)((pa) + (a0 + a) * 512 + 8)); } } while (0)
#define CV_STEP(cur, nxt, pa, pb) do { \
        CV_RD(HA, HB, 4, pa, pb); \
        _Pragma("unroll") for (int s = 0; s < 4; ++s) { \
            _Pragma("unroll") for (int a = 0; a < s; ++a) { acc[a + s] = mfma16(LA[cur][a], LB[cur][s], acc[a + s]); acc[a + s] = mfma16(LA[cur][s], LB[cur][a], acc[a + s]); } \
            acc[2 * s] = mfma16(LA[cur][s], LB[cur][s], acc[2 * s]); } \
        SCHED_IL(12, 16); \
        CV_RD(LA[nxt], LB[nxt], 0, (pa) + 64, (pb) - 64); \
        _Pragma("unroll") for (int s = 0; s < 4; ++s) \
            _Pragma("unroll") for (int a = 0; a + s < 4; ++a) { acc[a + s + 4] = mfma16(LA[cur][a], HB[s], acc[a + s + 4]); acc[a + s + 4] = mfma16(HA[s], LB[cur][a], acc[a + s + 4]); } \
        SCHED_IL(12, 20); \
    } while (0)
    CV_RD(LA[0], LB[0], 0, ap, bp + 448);
#pragma unroll 1
    for (int bb = 0; bb < 8; bb += 2) {
        LAS char* pa = ap + bb * 64;
        LAS char* pb = bp + (7 - bb) * 64;
        CV_STEP(0, 1, pa, pb);
        CV_STEP(1, 0, pa + 64, pb - 64);
    }
    if (YPRIO) __builtin_amdgcn_s_setprio(YPRIO);
    const float dd = (d & 1) ? bfhi(ddw) : bflo(ddw);
    unsigned yw[8][2];
    u32x2 uw[8];
#pragma unroll
    for (int nu = 0; nu < 8; ++nu) uw[nu] = *(LAS u32x2*)(U + 512 + (256 * nu + 16 * fr + 8 * (g >> 1)) * 2 + ((g & 1) ? 0 : 8));
#pragma unroll
    for (int nu = 0; nu < 8; ++nu) {
        yw[nu][0] = pk2(fmas(bfhi(uw[nu][1]), dd, acc[nu][0]), fmas(bflo(uw[nu][1]), dd, acc[nu][1]));
        yw[nu][1] = pk2(fmas(bfhi(uw[nu][0]), dd, acc[nu][2]), fmas(bflo(uw[nu][0]), dd, acc[nu][3]));
    }
#pragma unroll
    for (int p = 0; p < 4; ++p) {
        const u32x2 s0 = __builtin_amdgcn_permlane16_swap(yw[2 * p][0], yw[2 * p + 1][0], false, false);
        const u32x2 s1 = __builtin_amdgcn_permlane16_swap(yw[2 * p][1], yw[2 * p + 1][1], false, false);
        st16c(rs, (256 * (g & 1) + 16 * fr + 8 * (g >> 1)) * 2 + p * 1024, WS_YT + d * (S * 2), (u32x4){s0[0], s1[0], s0[1], s1[1]});
    }
    SCHED0();
}

constexpr int RB = 5, KST = 36 * 1024;
#define KROT 0
#define SPOLL 0
#define KAP(i) ((i) + (KROT) >= 9 ? (i) + (KROT) - 9 : (i) + (KROT))
#define G3_LDB(i) do { _Pragma("unroll") for (int l = 0; l < 4; ++l) bq[i][l] = __builtin_bit_cast(bf16x8, ld16c(rs, lane * 16 + l * 1024, sB + KAP(i) * KST)); \
        bq[i][4] = __builtin_bit_cast(bf16x8, ld16c(rs, lane * 16, sB4 + KAP(i) * KST)); } while (0)
#define G3_LDA(i) do { _Pragma("unroll") for (int m = 0; m < 4; ++m) aq[i][m] = __builtin_bit_cast(bf16x8, ld16c(rs, lane * 16, sA + m * (72 * 1024) + KAP(i) * 1024)); } while (0)
#define G3_RDB(i) do { _Pragma("unroll") for (int l = 0; l < 4; ++l) bq[i][l] = *(LAS bf16x8*)(lds + redo + ((i) - RB) * 4096 + l * 1024 + lane * 16); \
        bq[i][4] = *(LAS bf16x8*)(lds + redo + 16384 + ((i) - RB) * 512 + (g * 8 + (fr & 7)) * 16); } while (0)
#define G3_PRE() do { _Pragma("unroll") for (int i = 0; i < RB; ++i) G3_LDB(i); SCHED0(); } while (0)
#define G3_HO ((((lane >> 3) & 3) * 16 + 8 * (ct & 1) + (lane & 7)) * 16)
#define G3_DMA1(dst, sB, sB4) do { dma16c4<0, 0>(wr, L0 + (dst), lane * 16, sB + KAP(5) * KST); dma16c4<4096, 0>(wr, L0 + (dst), lane * 16, sB + KAP(6) * KST); \
        dma16c<16384, 0>(wr, L0 + (dst), G3_HO + (lane < 32 ? KAP(5) : KAP(6)) * KST, sB4); } while (0)
#define G3_DMA2(dst, sB, sB4) do { dma16c4<8192, 0>(wr, L0 + (dst), lane * 16, sB + KAP(7) * KST); dma16c4<12288, 0>(wr, L0 + (dst), lane * 16, sB + KAP(8) * KST); \
        dma16c<17408, 0>(wr, L0 + (dst), G3_HO + (lane < 32 ? KAP(7) : KAP(8)) * KST, sB4); } while (0)
#define G3_DMA(dst, sB, sB4) do { G3_DMA1(dst, sB, sB4); G3_DMA2(dst, sB, sB4); } while (0)
#define FLAG_ISSUE(v_, o_) asm volatile("buffer_load_dwordx2 %0, %1, %2, 0 offen sc1" : "=v"(v_) : "v"(o_), "s"(wr) : "memory")
#define FP_SPIN() spin_flags(rs, sF + (lane & 1) * 16 + (lane & 2) * 16, tag)
#define FP_ISSUE() FLAG_ISSUE(fpv, sF + (lane & 1) * 16 + (lane & 2) * 16)
#define FP_CHECK(NV) do { u64 ne; \
        asm volatile("s_waitcnt vmcnt(%3)\n v_cmp_ne_u64 %0, %1, %2" : "=s"(ne) : "s"(tag), "v"(fpv), "n"(NV) : "memory"); \
        if (ne != 0ull) FP_SPIN(); \
        asm volatile("" ::: "memory"); } while (0)
#define XST(i) do { } while (0)
#define NOSTAT 0
#define ROT (pi + pj) % 9
#define CPRE 2
constexpr float GC1 = -2.0f * 0.7978845608028654f * 1.4426950408889634f, GC2 = GC1 * 0.044715f;
#define CPRIO 2
#define CPFW 2
#define T1W 1
#define KPF 1
#define GS 72
#define WBASE 0
#define WALK 2
#define WPF 0
#define CVD 0
#define YIS 7
#define YCK 8
#define PSTO(x) (x)
#define PSL 2
#define XIS 8
#define LPRIO 0
#define HPF 1
#define CVL 47
#define VPRIO 1
#define RGW 4
#define OAUX 0
#define ZD 6

__global__ __launch_bounds__(512, 2) void mega(const u16* __restrict__ x, const u16* __restrict__ kin, const u16* __restrict__ vin, const u16* __restrict__ shk,
                                               const u16* __restrict__ hk, const u16* __restrict__ dv, const u16* __restrict__ w_in, const u16* __restrict__ w_sp,
                                               const u16* __restrict__ b_sp, const u16* __restrict__ w_out, char* __restrict__ ws, u16* __restrict__ out) {
    extern __shared__ __attribute__((aligned(16))) char smem_[];
    LAS char* lds = (LAS char*)smem_;
    asm volatile(".Lmega_head:");
    const unsigned L0 = (unsigned)(size_t)lds;
    const int b = blockIdx.x, xcd = b & 7, r = b >> 3;
    const int pi = (xcd & 1) * 8 + (r & 7), pj = (xcd >> 1) * 4 + (r >> 3);
    const int tid = threadIdx.x, wid = RFL(tid >> 6), lane = tid & 63;
    const int fr = lane & 15, g = lane >> 4, q4 = (lane >> 2) & 3, p4 = lane & 3;
    const __amdgpu_buffer_rsrc_t rs = ws_rsrc(ws);
    const u32x4 wr = mk_rsrc(ws, WS_END);
    const f32x4 zero4 = {0.f, 0.f, 0.f, 0.f};
    STAMP(0);
#define TAILS() const int b_ = opqs(b), wid_ = opqs(wid), rg = (b_ & 1) * 16 + ((b_ & 7) >> 1) * 4 + ((b_ >> 3) & 3), ct = b_ >> 5; \
    const int nb3 = (9 * ct) >> 1, n30 = nb3 + (ct & 1), n34 = (ct & 1) ? nb3 : nb3 + 4; \
    const int redo = (int)(((wid_ < 4 ? redtab(0) : redtab(4)) >> (16 * (wid_ & 3))) & 0xffff) << 8
#define TAILS2() const unsigned sA = WS_P + (4 * rg * 72 + 9 * wid_) * 1024, sB = WS_WT + (9 * wid_ * 36 + n30) * 1024, sB4 = WS_WT + (9 * wid_ * 36 + n34) * 1024, \
    sF = WS_FP + (rg >> 1) * 512 + wid_ * 64 + (rg & 1) * 8
    u64 tag;
#define TAG_READY() do { asm volatile("s_load_dwordx2 %0, %1, 0x0\n s_waitcnt lgkmcnt(0)" : "=s"(tag) : "s"(ws + WS_E) : "memory"); tag += TAG_INC; } while (0)
#define WAIT_LB0() __builtin_amdgcn_s_waitcnt(0xC07F)
#define RD(p) (*(LAS bf16x8*)(p))
#define RDP(p, o) (*(LAS bf16x8*)(size_t)((p) + (o)))
#define TRF(p) cat8(trread(p), trread((p) + 2304))
#define TRP(p, o) TRF((LAS char*)(size_t)((p) + (o)))
#define ADV(p, d) asm("v_add_u32 %0, %1, %0" : "+v"(p) : "s"(d))
#define LDSW(o) (*(volatile LAS unsigned*)(lds + (o)))
#define FENCE() asm volatile("" ::: "memory")
#define SPIN(word, need) wcheck(0u, L0 + (word) + p4 * 4, need)
#define SGB(m, n) __builtin_amdgcn_sched_group_barrier(m, n, 0)
    const int w = wid & 3, l3 = lane >> 3;
    const unsigned ch = ((lane & 7) ^ (l3 >> 1)) * 16;
    const int bb = (q4 + 4 * (g & 1) + 16 * (g >> 1)) * 288 + p4 * 8;
    unsigned vA[4];
#define SET_VSP() do { const unsigned base = l3 * (S * 2) + ch; vA[0] = base; vA[1] = (base ^ 64) + 8 * S * 2 - 1024; vA[2] = base + 16 * S * 2 - 2048; vA[3] = (base ^ 64) + 24 * S * 2 - 3072; } while (0)
    unsigned vB[5];
#define SET_VB(i) do { const unsigned c = i < 4 ? (4 * w + i) * 64 + lane : 1024 + 32 * w + (lane & 31), rho = c / 18, cc = c - rho * 18; \
        const unsigned grow = (rho & ~12u) | ((rho & 4) << 1) | ((rho & 8) >> 1); vB[i] = grow * (F * 2) + cc * 16 - (i & 3) * 1024; } while (0)
    const unsigned lw = L0 + w * 4096, lh = L0 + 16384 + w * 512, w1 = pj * 288;
    const int rot = ROT;
#define KT(t) ((t) + rot >= 9 ? (t) + rot - 9 : (t) + rot)
#define DMA_W(dst, src) do { dma4v(rwi, lw + (dst), vB, w1 + (src)); dmah(rwi, lh + (dst), vB[4], w1 + (src)); } while (0)
#define DMA_W1(j) DMA_W(warea(j), KT(j) * (64 * F * 2))
    auto walk = [&](int i) {
        const int j = pj & 3, m = i ^ j, x = i - 1;
        const int q = (pi & ~3) | ((j + x + (x >= ((pi - j) & 3))) & 3);
        return i == 0 ? pi : i < 4 ? q : pi ^ (WALK == 2 && (m == 7 || m == 8) ? m ^ 15 : m);
    };
    u64 fpv;
    bf16x8 bq[9][5];
    bool hasconv = false;
    const int cd = w < 2 ? 2 * b + w : 512 + b;

    if (__builtin_expect(wid >= 4, 1)) {
        const u32x4 rx = mk_rsrc(x, SZ_X), rwi = mk_rsrc(w_in, D * F * 2);
        {
            const unsigned base = (8 * (l3 >> 2) + (l3 & 3)) * (D * 2) + ch;
            vA[0] = base; vA[1] = (base ^ 64) + 16 * D * 2 - 1024; vA[2] = (base ^ 64) + 4 * D * 2 - 2048; vA[3] = base + 20 * D * 2 - 3072;
        }
        const unsigned xs = (pi * 128 + 32 * w) * (D * 2);
        XST(26);
        dma4v(rx, lw, vA, xs + rot * 128);
        SCHED0();
#pragma unroll
        for (int i = 0; i < 5; ++i) {
            SET_VB(i);
            if (i < 4) dma1(i * 1024, rwi, lw + SL_A, vB[i], w1 + FH * 2 + rot * (64 * F * 2)); else dmah(rwi, lh + SL_A, vB[4], w1 + FH * 2 + rot * (64 * F * 2));
            SCHED0();
        }
        XST(27);
        TAG_READY();
        const int xi = (fr >> 2) & 1;
        const int n0 = (32 * w + 16 * xi + 4 * (fr >> 3) + (fr & 3)) * 128 + ((g ^ (2 * (fr >> 3) + ((fr >> 1) & 1)) ^ (4 * xi)) << 4), n1 = n0 ^ 64;
        const int c0 = bb + w * 64, c8 = bb + 256 + (w & 1) * 9216;
        bf16x8 ones = {0x3f80, 0x3f80, 0x3f80, 0x3f80, 0x3f80, 0x3f80, 0x3f80, 0x3f80};
        asm volatile("" : "+v"(ones));
        bf16x8 xf[9][4];
        const unsigned tag32 = (unsigned)tag;
        const unsigned go = g * 1024 + fr * 16, go2 = g == 0 ? 8192 + fr * 16 : 0x7fffff00u, tagx = g == 0 ? tag32 : 0u;
        const unsigned sr = WS_SR + pi * 12288 + w * 256, sc = WS_SC + pj * 12288 + w * 256;
#define COLSUM(base, dst) do { bf16x8 cf[5]; cf[0] = TRF((base) + c0); cf[1] = TRF((base) + c0 + 32); cf[2] = TRF((base) + c0 + 9216); cf[3] = TRF((base) + c0 + 9216 + 32); cf[4] = TRF((base) + c8); \
            const f32x4 s0_ = mfma16(ones, cf[2], mfma16(ones, cf[0], zero4)), s1_ = mfma16(ones, cf[3], mfma16(ones, cf[1], zero4)), s2_ = mfma16(ones, cf[4], zero4); \
            st16c(rs, g == 0 && pi < 9 ? fr * 16 : 0x7fffff00u, (dst) + pi * 1024, (u32x4){__float_as_uint(s0_[0]), __float_as_uint(s1_[0]), __float_as_uint(s2_[0]), tag32}); } while (0)
#define GATHER(pv, so) do { ALD("x4", pv[0], go, wr, so, " sc1"); ALD("x4", pv[1], go, wr, (so) + 4096, " sc1"); ALD("x4", pv[2], go2, wr, so, " sc1"); } while (0)
#define GBAD(pv) (__builtin_amdgcn_ballot_w64(((pv[0][3] ^ tag32) | (pv[1][3] ^ tag32) | (pv[2][3] ^ tagx)) != 0u) != 0ull)
#define GCHECK(pv, so) do { asm volatile("" : "+v"(pv[0]), "+v"(pv[1]), "+v"(pv[2])); \
            if (__builtin_expect(GBAD(pv), 0)) { unsigned sp_ = 0; \
                do { GATHER(pv, so); WAIT_V(0); asm volatile("" : "+v"(pv[0]), "+v"(pv[1]), "+v"(pv[2])); } while (GBAD(pv) && ++sp_ < SPIN_CAP); } } while (0)
#define CSUM(pv, k) adds(adds(__uint_as_float(pv[0][k]), __uint_as_float(pv[1][k])), __uint_as_float(pv[2][k]))
        u32x4 gr[3], gc[3];
        f32x2 lnc; float cs01, cs8;
        const u32x4 rsp = mk_rsrc(w_sp, S * S * 2);
        unsigned kpf = lane * (D * 2);
        __builtin_amdgcn_s_setprio(3);
#pragma unroll
        for (int t = 0; t < 9; ++t) {
            SCHED0();
            if (t == 0) {
                asm volatile("" :: "s"(KT(1)), "s"(KT(2)), "s"(KT(3)), "s"(KT(4)), "s"(KT(5)), "s"(KT(6)), "s"(KT(7)), "s"(KT(8)), "v"(n0), "v"(c0), "v"(c8), "v"(go), "v"(go2));
                XST(28);
                dma4v(rx, lw + SLOT, vA, xs + KT(1) * 128);
                if (T1W) DMA_W(SLOT + SL_A, FH * 2 + KT(1) * (64 * F * 2));
            }
            WAIT_V(t == 0 ? (T1W ? 9 : 4) : t == 2 || t == 4 ? 10 : t == 3 ? 11 : t == 7 ? 15 : t == 8 ? 10 : 9);
            BAR();
            XST(40 + t);
            LAS char* sb = lds + (t & 3) * SLOT;
            if (t < 8) { xf[t][0] = RD(sb + n0); xf[t][1] = RD(sb + n1 + 1024); xf[t][2] = RD(sb + n1); xf[t][3] = RD(sb + n0 + 1024); }
            SCHED0();
            if (t == 0 && !T1W) DMA_W(SLOT + SL_A, FH * 2 + KT(1) * (64 * F * 2));
            if (t < 7) {
                dma4v(rx, lw + ((t + 2) & 3) * SLOT, vA, xs + KT(t + 2) * 128);
                DMA_W(((t + 2) & 3) * SLOT + SL_A, FH * 2 + KT(t + 2) * (64 * F * 2));
            }
            SCHED0();
            if (t == 1) COLSUM(lds + SL_A, sc);
            if (t == 2) {
                f32x4 rsm[2], rq[2];
#pragma unroll
                for (int m = 0; m < 2; ++m) {
                    rsm[m] = mfma16(ones, xf[0][2 + m], mfma16(ones, xf[0][m], zero4));
                    rq[m] = mfma16(xf[0][2 + m], xf[0][2 + m], mfma16(xf[0][m], xf[0][m], zero4));
                }
#define DIAG(q_) ((fr & 2) ? ((fr & 1) ? q_[3] : q_[2]) : ((fr & 1) ? q_[1] : q_[0]))
                st16c(rs, (fr >> 2) == g && pj < 9 ? fr * 16 : 0x7fffff00u, sr + pj * 1024, rec4(rsm[0][0], DIAG(rq[0]), rsm[1][0], DIAG(rq[1]), tag32));
            }
            SCHED0();
            if (t == 6) { GATHER(gr, sr); GATHER(gc, sc); }
            if (t == 7) { DMA_W1(0); DMA_W1(2); }
            if (t == 8) {
                if (KPF == 2) ALD("", kpf, kpf, mk_rsrc(w < 2 ? kin : vin, KPF ? SZ_X : 0), (b >> 3) * 64 * (D * 2) + (b & 7) * 144 + (w & 1) * 140, "");
                GCHECK(gr, sr); GCHECK(gc, sc);
                lnc = rec_consts<3>(gr, 1.0f / D);
                cs01 = half_sum(CSUM(gc, 0), CSUM(gc, 1)); cs8 = CSUM(gc, 2); cs8 = half_sum(cs8, cs8);
                asm volatile("" : "+v"(lnc), "+v"(cs01), "+v"(cs8));
            }
            if (t < 8) asm volatile("" : "+v"(xf[t][0]), "+v"(xf[t][1]), "+v"(xf[t][2]), "+v"(xf[t][3]));
        }
        {
            LAS float* p = (LAS float*)(lds + L_ROWST + (16 * w + 8 * (lane >> 5) + (fr >> 1)) * 16 + (fr & 1) * 4);
            p[0] = lnc.x; p[2] = lnc.y;
            LAS float* c = (LAS float*)(lds + L_CS) + fr * 12;
            c[2 * w + (lane >> 5)] = cs01;
            c[8 + w] = cs8;
        }
        WAIT_L0();
        BAR();
        STAMP(2);
        DMA_W1(1); DMA_W1(3); DMA_W1(4); DMA_W1(5); DMA_W1(6);
        if (KPF != 2) ALD("", kpf, kpf, mk_rsrc(w < 2 ? kin : vin, KPF ? SZ_X : 0), (b >> 3) * 64 * (D * 2) + (b & 7) * 144 + (w & 1) * 140, "");
        pf4<0>(mk_rsrc(hk + cd * S, HPF && (w < 2 || (w == 2 && b < 64)) ? S * 2 : 0), L0 + L_DUMP, lane * 128, 0);
        WAIT_V(KPF == 2 ? 16 : 17);
        LDSW(M_FL + w * 4) = 1u;
        XST(50);
        SPIN(M_FL, 1);
        XST(49);
        COLSUM(lds + warea(0), sc + 16 * 12288);
        st16c(rs, w == 3 && pj == 0 ? lane * 16 : 0x7fffff00u, WS_ST1 + pi * 1024, *(LAS u32x4*)(lds + L_ROWST + lane * 16));
        f32x4 az[2][9];
        unsigned rho = walk(lane >> 2);
        asm volatile("" : "+v"(rho));
        u64 fzv[2];
        const unsigned as0 = (pi * 128 + 32 * w) * (S * 2), zs0 = WS_Z2N + pj * 32 * SL_B + w * 4096;
        unsigned vl, fz;
#define KIDX(t) (2 * __builtin_amdgcn_readlane(rho, 4 * (((t) - 18) >> 1)) + ((t) & 1))
        auto stageA = [&](int t) { dma4v(rsp, lw + (t & 3) * SLOT, vA, as0 + KIDX(t) * 128); };
        auto stageB = [&](int t) {
            const unsigned zs = zs0 + KIDX(t) * SL_B;
            dma16c4<SL_A, 0>(wr, lw + (t & 3) * SLOT, vl, zs);
            dmahc(wr, lh + (t & 3) * SLOT + SL_A, vl, zs + 16384 - w * 3584);
        };
        u32x2 kw[11], vw[8]; u32x4 sk[2];
        unsigned ko, kom, ko4, kd8, kvo;
        {
            bf16x8 wf[162];
            unsigned fw;
            unsigned wb = L0 + bb;
#define WF_B(j) ((j) < 2 || (j) >= 6 ? 0 : (j) < 5 ? warea(2) : warea(5))
#define WF_P(s) ((LAS char*)(size_t)(wb + (warea((s) / 18) - WF_B((s) / 18) + ((s) % 9) * 32 + (((s) / 9) & 1) * 9216)))
#pragma unroll
            for (int s = 0; s < ZD; ++s) wf[s] = TRF(WF_P(s));
            SCHED0();
            XST(51);
            __builtin_amdgcn_s_setprio(0);
            wcheck(0u, L0 + M_RC + w * 4, 1);
            STAMP(3);
            SFOR(s, 0, 162)
                constexpr int j = s / 18, ks = (s / 9) & 1, n = s % 9;
                if (s == 72 - ZD - 4) fw = LDSW(M_FL + p4 * 4);
                if (s == 72 - ZD) wcheck(fw, L0 + M_FL + p4 * 4, 2);
                if (s == 126 - ZD - 4) fw = LDSW(M_RC + p4 * 4);
                if (s == 126 - ZD) wcheck(fw, L0 + M_RC + p4 * 4, 2);
                if (s == 18) { xf[8][0] = RD(lds + n0); xf[8][1] = RD(lds + n1 + 1024); }
                if (s == 19) { xf[8][2] = RD(lds + n1); xf[8][3] = RD(lds + n0 + 1024); }
                if (s == 25) asm volatile("" : "+v"(xf[8][0]), "+v"(xf[8][1]), "+v"(xf[8][2]), "+v"(xf[8][3]));
                if (s == 18 || s == 36 || s == 108 || s == 126) LDSW(M_RL + w * 4) = j;
                if (s == 46) {
                    const int ln = opqv(tid) & 63, it = w * 36 + (ln < 36 ? ln : 35), sg = it / 18;
                    kvo = sg; kd8 = it - sg * 18;
                    asm volatile("" : "+v"(kd8), "+v"(kvo));
                }
                if (s == 49) {
                    ko = (((b >> 3) * 64 + kvo * 8) * D + (b & 7) * 72 + kd8 * 4) * 2;
                    kom = b < 8 && kvo == 0 ? 0x7fffff00u : ko - 3 * (D * 2);
                    asm volatile("" : "+v"(ko), "+v"(kom));
                }
                if (s == 52) {
                    ko4 = ko + 4 * (D * 2); kvo = L0 + KVS + kd8 * 512 + ((kvo + kd8) & 7) * 16; kd8 = kd8 * 32 + (b & 7) * 576;
                    asm volatile("" : "+v"(ko4), "+v"(kd8), "+v"(kvo));
                }
                if (s == 152) fw = LDSW(M_RL + p4 * 4);
                if (s == 158) wcheck(fw, L0 + M_RL + p4 * 4, 7);
                if (s == 40) { WAIT_V(KPF == 2 ? 3 : 4); LDSW(M_FL + w * 4) = 2u; }
                if (s == 140) { vl = (opqv(tid) & 63) * 16; fz = WS_FZ + pj * 512 + rho * 32 + ((vl >> 1) & 24); asm volatile("" : "+v"(fz), "+v"(vl)); }
                if (s == 146) { const unsigned l3 = vl >> 7, ch = ((vl >> 4 & 7) ^ (l3 >> 1)) * 16; vA[0] = l3 * (S * 2) + ch; asm volatile("" : "+v"(vA[0])); }
                if (s == 150) { vA[1] = (vA[0] ^ 64) + 8 * S * 2 - 1024; vA[2] = vA[0] + 16 * S * 2 - 2048; vA[3] = vA[1] + 16 * S * 2 - 2048; asm volatile("" : "+v"(vA[1]), "+v"(vA[2]), "+v"(vA[3])); }
#define LD8(dst, vo, r_, imm) asm volatile("buffer_load_dwordx2 %0, %1, %2, 0 offen offset:%3" KNT : "=v"(dst) : "v"(vo), "s"(r_), "n"(imm) : "memory")
                if constexpr (s >= 54 && s < 65) { constexpr int i = s - 54; LD8(kw[i], i < 3 ? kom : i < 7 ? ko : ko4, mk_rsrc(kin, SZ_X), (i < 3 ? i : (i - 3) & 3) * 1152); }
                if constexpr (s >= 72 && s < 80) { constexpr int i = s - 72; LD8(vw[i], i < 4 ? ko : ko4, mk_rsrc(vin, SZ_X), (i & 3) * 1152); }
                if (s == 80) asm volatile("buffer_load_dwordx4 %0, %2, %3, 0 offen\n buffer_load_dwordx4 %1, %2, %3, 0 offen offset:16" : "=&v"(sk[0]), "=&v"(sk[1]) : "v"(kd8), "s"(mk_rsrc(shk, D * 8)) : "memory");
                if (s == 18) XST(56); if (s == 42) XST(57); if (s == 54) XST(58); if (s == 107) XST(59); if (s == 109) XST(60); if (s == 121) XST(61); if (s == 133) XST(62); if (s == 135) XST(63);
                SCHED0();
                if (s < 9) asm volatile("v_mfma_f32_16x16x32_bf16 %0, %1, %2, 0" : "=&v"(az[0][n]) : "v"(wf[s]), "v"(xf[j][2 * ks]));
                else asm volatile("v_mfma_f32_16x16x32_bf16 %0, %1, %2, %0" : "+v"(az[0][n]) : "v"(wf[s]), "v"(xf[j][2 * ks]));
                SCHED0();
                bf16x4 h0, h1;
                if constexpr ((s + ZD) % 18 == 0 && s + ZD < 162 && WF_B((s + ZD) / 18) != WF_B((s + ZD) / 18 - 1)) { unsigned& wb_ = wb; asm volatile("v_add_u32 %0, %1, %0" : "+v"(wb_) : "n"(WF_B((s + ZD) / 18) - WF_B((s + ZD) / 18 - 1))); }
                if constexpr (s + ZD < 162) h0 = trread(WF_P(s + ZD));
                SCHED0();
                if (s < 9) asm volatile("v_mfma_f32_16x16x32_bf16 %0, %1, %2, 0" : "=&v"(az[1][n]) : "v"(wf[s]), "v"(xf[j][2 * ks + 1]));
                else asm volatile("v_mfma_f32_16x16x32_bf16 %0, %1, %2, %0" : "+v"(az[1][n]) : "v"(wf[s]), "v"(xf[j][2 * ks + 1]));
                SCHED0();
                if constexpr (s + ZD < 162) { h1 = trread(WF_P(s + ZD) + 2304); wf[s + ZD] = cat8(h0, h1); }
                SCHED0();
            SEND
        }
        STAMP(4);
        WAIT_V(0);
        asm volatile("" : "+v"(kw[0]), "+v"(kw[1]), "+v"(kw[2]), "+v"(kw[3]), "+v"(kw[4]), "+v"(kw[5]), "+v"(kw[6]), "+v"(kw[7]), "+v"(kw[8]), "+v"(kw[9]), "+v"(kw[10]) : "v"(kpf));
        asm volatile("" : "+v"(vw[0]), "+v"(vw[1]), "+v"(vw[2]), "+v"(vw[3]), "+v"(vw[4]), "+v"(vw[5]), "+v"(vw[6]), "+v"(vw[7]), "+v"(sk[0]), "+v"(sk[1]));
        GATHER(gc, sc + 16 * 12288);
#define HALF(w_, c) (((c) & 1) ? bfhi(w_) : bflo(w_))
#pragma unroll
        for (int c = 0; c < 4; ++c) {
            const float h[4] = {bflo(sk[c >> 1][(c & 1) * 2]), bfhi(sk[c >> 1][(c & 1) * 2]), bflo(sk[c >> 1][(c & 1) * 2 + 1]), bfhi(sk[c >> 1][(c & 1) * 2 + 1])};
            float kk[11], o[8];
#pragma unroll
            for (int i = 0; i < 11; ++i) kk[i] = HALF(kw[i][c >> 1], c);
#pragma unroll
            for (int i = 0; i < 8; ++i) o[i] = tap4s(kk[i], kk[i + 1], kk[i + 2], kk[i + 3], h, HALF(vw[i][c >> 1], c));
            *(LAS u32x4*)(size_t)(kvo + c * 128) = (u32x4){pk2(o[7], o[6]), pk2(o[5], o[4]), pk2(o[3], o[2]), pk2(o[1], o[0])};
        }
        __builtin_amdgcn_s_setprio(3);
        WAIT_L0();
        STAMP(5);
        BAR();
        FLAG_ISSUE(fzv[0], fz);
        {
        const int lane = vl >> 4, fr = lane & 15, g = lane >> 4, q4 = (lane >> 2) & 3, p4 = lane & 3, l3 = lane >> 3;
        u64 miss;
#define NEEDZ(mask) do { if ((miss & (mask)) != 0ull) { unsigned sp_ = 0; \
            do { miss = __builtin_amdgcn_ballot_w64(ldflag(rs, fz) != tag); } while ((miss & (mask)) != 0ull && ++sp_ < SPIN_CAP); } FENCE(); } while (0)
        u32x4 zp[9];
        hasconv = w < 2 || (w == 2 && b < 64);
        const unsigned fk = WS_FK + w * 512 + lane * 8;
        {
            float nmb[2], k1[2], k2[2];
#pragma unroll
            for (int m = 0; m < 2; ++m) {
                LAS float* p = (LAS float*)(lds + L_ROWST + (16 * w + 8 * m + (fr >> 1)) * 16 + (fr & 1) * 4);
                const float rd = p[2];
                nmb[m] = -p[0]; k1[m] = muls(rd, GC1); k2[m] = muls(muls(rd, rd), muls(rd, GC2));
            }
            u32x4 kl[3];
            kl[0] = *(LAS u32x4*)(lds + KVS + w * 1024 + vl); kl[1] = *(LAS u32x4*)(lds + KVS + 4096 + w * 1024 + vl); kl[2] = *(LAS u32x4*)(lds + KVS + 8192 + vl);
            WAIT_V(1);
            GCHECK(gc, sc + 16 * 12288);
            *(LAS float*)(lds + L_CS + ((2 * w + (lane >> 5)) * 16 + fr) * 4) = half_sum(CSUM(gc, 0), CSUM(gc, 1));
            cs8 = CSUM(gc, 2);
            *(LAS float*)(lds + L_CS + ((8 + w) * 16 + fr) * 4) = half_sum(cs8, cs8);
            f32x4 cc = zero4;
            asm volatile("s_waitcnt vmcnt(0) lgkmcnt(0)\n v_cmp_ne_u64 %0, %1, %2" : "=s"(miss) : "s"(tag), "v"(fzv[0]), "v"(k1[0]), "v"(k1[1]), "v"(k2[0]), "v"(k2[1]), "v"(nmb[0]), "v"(nmb[1]), "v"(kl[0]), "v"(kl[1]), "v"(kl[2]) : "memory");
            asm volatile("" : "+v"(kl[0]), "+v"(kl[1]), "+v"(kl[2]));
            NEEDZ(0xf0ull);
            stageB(20); stageA(21);
#define XE(u) ((u) == 6 ? 2 : (u) >= 7 && (u) < 11 ? 1 : 0)
#define NW(u) ((u) == 0 ? 63 : (u) < 5 ? 9 : (u) == 5 ? 14 : 18 + XE((u) - 1) + XE((u) - 2) + XE((u) - 3))
            SFOR(u, 0, 19)
                constexpr int n = 19 + u, t = n + 3, tb = u < 4 ? t - 1 : t;
                const unsigned la = lw + (t & 3) * SLOT, lb = lw + (tb & 3) * SLOT + SL_A, as = as0 + KIDX(t) * 128, zs = zs0 + KIDX(tb) * SL_B;
                SCHED0();
                asm volatile("s_waitcnt vmcnt(%0)\n s_barrier" :: "n"(NW(u)), "s"(la), "s"(as), "s"(zs), "v"(cc) : "memory");
                if (u >= 1 && u < 5) { u64 nm_; asm volatile("v_cmp_ne_u64 %0, %1, %2" : "=s"(nm_) : "s"(tag), "v"(fzv[u & 1]) : "memory"); miss &= nm_; }
                if (u < 4) FLAG_ISSUE(fzv[(u + 1) & 1], fz);
                if (u == 4) { NEEDZ(~0ull); stageB(25); }
                if (u == 14 && w == 3 && lane == 0) stflag(ws + WS_FK + b * 8, tag);
                if (u == 16 && b == 0 && w == 3 && lane == 0) stflag(ws + WS_E, tag);
                if constexpr (u == 0) {
                    cc = *(LAS f32x4*)(lds + L_CS + g * 16);
                    stageA(22);
                    stageB(21);
                } else {
                constexpr int q = u - 1, m = q & 1, nn = q >> 1;
                bf16x4 wt0, wt1;
                if (u >= 8 && u < 11) { LAS char* p = lds + L_WSCR + w * 3072 + (4 * g + q4) * 96 + (u - 8) * 32 + p4 * 8; wt0 = trread(p); wt1 = trread(p + 1536); }
                f32x4 a = az[m][nn];
                float u0, u1, u2, u3;
                asm volatile("s_mov_b32 m0, %15\n v_fma_f32 %4, %8, %12, %4\n buffer_load_dwordx4 %16, %20, %21 offen lds\n"
                             "v_fma_f32 %5, %9, %12, %5\n v_fma_f32 %6, %10, %12, %6\n v_fma_f32 %7, %11, %12, %7\n"
                             "buffer_load_dwordx4 %17, %20, %21 offen offset:1024 lds\n"
                             "v_mul_f32 %0, %4, %4\n v_mul_f32 %1, %5, %5\n v_mul_f32 %2, %6, %6\n v_mul_f32 %3, %7, %7\n"
                             "buffer_load_dwordx4 %18, %20, %21 offen offset:2048 lds\n"
                             "v_fma_f32 %0, %0, %14, %13\n v_fma_f32 %1, %1, %14, %13\n v_fma_f32 %2, %2, %14, %13\n v_fma_f32 %3, %3, %14, %13\n"
                             "buffer_load_dwordx4 %19, %20, %21 offen offset:3072 lds\n"
                             "v_mul_f32 %0, %0, %4\n v_mul_f32 %1, %1, %5\n v_mul_f32 %2, %2, %6\n v_mul_f32 %3, %3, %7"
                             : "=&v"(u0), "=&v"(u1), "=&v"(u2), "=&v"(u3), "+v"(a[0]), "+v"(a[1]), "+v"(a[2]), "+v"(a[3])
                             : "v"(cc[0]), "v"(cc[1]), "v"(cc[2]), "v"(cc[3]), "v"(nmb[m]), "v"(k1[m]), "v"(k2[m]),
                               "s"(la), "v"(vA[0]), "v"(vA[1]), "v"(vA[2]), "v"(vA[3]), "s"(rsp), "s"(as) : "memory");
                f32x4 c9;
                if (m == 1 && nn < 8) cc = *(LAS f32x4*)(lds + L_CS + (nn + 1) * 64 + g * 16);
                if (q == 15) c9 = *(LAS f32x4*)(lds + L_CS + 9 * 64 + g * 16);
                if (u < 4) NEEDZ(3ull << (2 * (tb - 18)));
                asm volatile("s_mov_b32 m0, %8\n v_exp_f32 %0, %0\n buffer_load_dwordx4 %9, %10, %11 offen sc1 lds\n"
                             "v_exp_f32 %1, %1\n v_exp_f32 %2, %2\n v_exp_f32 %3, %3\n"
                             "buffer_load_dwordx4 %9, %10, %11 offen offset:1024 sc1 lds\n"
                             "v_add_f32 %0, 1.0, %0\n v_add_f32 %1, 1.0, %1\n v_add_f32 %2, 1.0, %2\n v_add_f32 %3, 1.0, %3\n"
                             "buffer_load_dwordx4 %9, %10, %11 offen offset:2048 sc1 lds\n"
                             "v_rcp_f32 %0, %0\n v_rcp_f32 %1, %1\n v_rcp_f32 %2, %2\n v_rcp_f32 %3, %3\n"
                             "buffer_load_dwordx4 %9, %10, %11 offen offset:3072 sc1 lds\n"
                             "v_mul_f32 %0, %0, %4\n v_mul_f32 %1, %1, %5\n v_mul_f32 %2, %2, %6\n v_mul_f32 %3, %3, %7\n"
                             "s_mov_b32 m0, %12\n s_mov_b32 exec_hi, 0\n buffer_load_dwordx4 %9, %10, %13 offen sc1 lds\n s_mov_b32 exec_hi, -1\n"
                             "v_cvt_pk_bf16_f32 %0, %0, %1\n v_cvt_pk_bf16_f32 %1, %2, %3"
                             : "+v"(u0), "+v"(u1), "+v"(u2), "+v"(u3)
                             : "v"(a[0]), "v"(a[1]), "v"(a[2]), "v"(a[3]), "s"(lb), "v"(vl), "s"(wr), "s"(zs), "s"(lh + (tb & 3) * SLOT + SL_A), "s"(zs + 16384 - w * 3584) : "memory");
                if (u >= 8 && u < 11) st16c(rs, b < 216 ? vl : 0x7fffff00u, WS_WT + (b * 12 + w * 3 + u - 8) * 1024, __builtin_bit_cast(u32x4, cat8(wt0, wt1)));
                if (u == 6 || u == 7) {
                    const unsigned so = WS_KVT + ((b & 7) * 72 * S + (b >> 3) * 64) * 2, ko_ = l3 * (S * 2) + ((lane - (l3 >> 2) - 2 * w) & 7) * 16;
                    if (u == 6) st16c(rs, ko_, so + w * (8 * S * 2), kl[0]);
                    if (u == 6) st16c(rs, ko_, so + (w + 4) * (8 * S * 2), kl[1]);
                    if (u == 7) st16c(rs, w == 0 ? ko_ : 0x7fffff00u, so + 64 * S * 2, kl[2]);
                }
                if (q == 15) { cc[0] = adds(cc[0], c9[0]); cc[1] = adds(cc[1], c9[1]); cc[2] = adds(cc[2], c9[2]); cc[3] = adds(cc[3], c9[3]); }
                const int n4 = nn - (nn > 4), zq = nn == 4 ? 8 : m * 4 + (n4 >> 1), h = nn == 4 ? m : n4 & 1;
                zp[zq][h * 2] = __float_as_uint(u0); zp[zq][h * 2 + 1] = __float_as_uint(u1);
                asm volatile("" : "+v"(zp[zq]));
                }
            SEND
        }
        SCHED0();
        STAMP(6);
#define TST(i) do { } while (0)
#pragma unroll 1
        for (int n = 38; n < 42; ++n) {
            WAIT_V(18);
            BAR();
            stageA(n + 3); stageB(n + 3);
        }
        FLAG_ISSUE(fzv[0], fk);
#pragma unroll 1
        for (int n = 42; n < 44; ++n) {
            WAIT_V(19);
            BAR();
            stageA(n + 3); stageB(n + 3);
        }
        WAIT_V(18);
        asm volatile("" : "+v"(fzv[0]));
        if (!allset(fzv[0], tag)) spin_flags(rs, fk, tag);
        FENCE();
        TAILS(); TAILS2();
        ConvIn cin;
        BAR();
        TST(44);
        stageA(47); stageB(47);
        WAIT_V(18);
        BAR();
        TST(45);
        stageA(48); stageB(48);
        WAIT_V(18);
        BAR();
        TST(46);
        stageA(49); stageB(49);
        WAIT_V(18);
        BAR();
        TST(47);
        if (hasconv) conv_load(cin, hk, dv, wr, cd, lane); else G3_LDB(0);
        SCHED0();
        TST(49);
#pragma unroll
        for (int q = 0; q < 8; ++q) *(LAS u32x4*)(lds + ZS + w * 8192 + q * 1024 + lane * 16) = zp[q];
        *(LAS u32x4*)(lds + Z8OFF(w) + lane * 16) = zp[8];
        SCHED0();
        TST(50);
        if (hasconv) WAIT_V(12); else WAIT_V(5);
        WAIT_L0();
        TST(51);
        BAR();
        TST(48);
        if (hasconv) {
            __builtin_amdgcn_s_setprio(VPRIO);
            G3_LDB(0);
            SCHED0();
            WAIT_V(5);
            CONV_PIN(cin);
            if (w == 2) SPIN(M_RC, 3);
            STAMP(7);
            conv_stage(cin, lds + redo, lane, [&](auto i_) { G3_LDB(decltype(i_)::v + 1); });
            __builtin_amdgcn_s_setprio(0);
        } else {
            __builtin_amdgcn_s_setprio(0);
            G3_LDB(1); G3_LDB(2); G3_LDB(3); G3_LDB(4);
        }
        SCHED0();
        STAMP(8);
        if (hasconv) {
            if (CVD) wcheck(0u, L0 + M_RC + w * 4, 5);
            conv_run(cin.dd, rs, lds + redo, cd, lane);
            STAMP(9);
            FP_ISSUE(); G3_DMA1(redo, sB, sB4);
            WAIT_V(9);
            if (lane == 0) stflag(ws + WS_FY + cd * 8, tag);
            XST(29);
            G3_DMA2(redo, sB, sB4);
            STAMP(20);
            FP_CHECK(19);
        } else {
            SPIN(M_RC, RGW);
#pragma unroll 1
            for (int v = 5 - w; v < (b < 64 ? 4 : 6 - w); ++v) G3_DMA(redoff(0) + v * RED_W, sB + (v - 4 - w) * (9 * KST), sB4 + (v - 4 - w) * (9 * KST));
            G3_DMA(redo, sB, sB4);
            WAIT_V(18);
            LDSW(M_FL + w * 4) = 4u;
            for (int i_ = 0; i_ < PSL; ++i_) __builtin_amdgcn_s_sleep(16);
            STAMP(20);
            FP_ISSUE(); FP_CHECK(0);
        }
        }
    } else {
        if (tid < 12) *(LAS unsigned*)(lds + L_MISC + tid * 4) = 0u;
        {
            const char* pc; unsigned len, len2, back;
            asm volatile("s_getpc_b64 %0\n.Lpf_here%=:\n s_mov_b32 %1, .Lmega_tail+1024-.Lmega_head\n s_mov_b32 %2, .Lpf_here%=-.Lmega_head\n s_mov_b32 %3, .Lmega_cend-.Lmega_head" : "=s"(pc), "=s"(len), "=s"(back), "=s"(len2));
            const u32x4 rc = mk_rsrc(pc - back, len > len2 ? len : len2);
            if (wid < CPFW) pf4<0>(rc, L0 + L_DUMP, ((r * CPFW + wid) & 7) * 8192 + lane * 128, 0);
        }
        u32x4 zp[9];
        unsigned bsw[4], vo[3];
        const int wm = w & 1, wn = w >> 1;
#pragma unroll
        for (int m = 0; m < 4; ++m) asm volatile("buffer_load_ushort %0, %1, %2, %3 offen" : "=v"(bsw[m]) : "v"(fr * 2), "s"(mk_rsrc(b_sp, S * 2)), "s"((pi * 128 + 64 * wm + 16 * ((m + 2 * wn) & 3)) * 2) : "memory");
        const int kap = b / 3, th = b - kap * 3, kq = kap / 9, ke = kap - kq * 9, pr = ke & 3;
        const unsigned wos = th * 384 + w * 96 + (ke == 8 ? 18 * kq + 4 : 18 * kq + (ke >> 2) * 9 + 2 * pr + (pr >> 1)) * (16 * D * 2);
#pragma unroll
        for (int i = 0; i < 3; ++i) { const unsigned c = i * 64 + lane, rr = c / 6; vo[i] = (ke == 8 ? (rr >> 3 & 1) * 144 + (rr & 4) * 2 + (rr & 3) + (rr >> 4) * 4 : rr) * (D * 2) + (c - rr * 6) * 16 - i * 1024; }
#pragma unroll
        for (int i = 0; i < 5; ++i) SET_VB(i);
        SET_VSP();
        const u32x4 rsp = mk_rsrc(w_sp, S * S * 2);
        const unsigned as0 = (pi * 128 + 32 * w) * (S * 2), k20 = opqs(walk(1) * 256);
        asm volatile("" : "+v"(vB[0]), "+v"(vB[1]), "+v"(vB[2]), "+v"(vB[3]), "+v"(vB[4]), "+v"(vA[0]), "+v"(vA[1]), "+v"(vA[2]), "+v"(vA[3]));
        asm volatile("" : "+v"(vo[0]), "+v"(vo[1]), "+v"(vo[2]));
        {
            f32x4 az[2][9];
#pragma unroll
            for (int n = 0; n < 9; ++n) az[0][n] = az[1][n] = zero4;
            unsigned e0 = L0 + (32 * w + fr) * 128 + ((g ^ (fr >> 1)) << 4), e1 = e0 ^ 64, pw = L0 + SL_A + bb;
            bf16x8 ax[2], bx[9], ay[2], by[9];
#define ZRD_X() do { ax[0] = RDP(e0, 0); ax[1] = RDP(e1, 2048); _Pragma("unroll") for (int n = 0; n < 9; ++n) bx[n] = TRP(pw, n * 32); } while (0)
#define ZRD_Y() do { ay[0] = RDP(e1, 0); ay[1] = RDP(e0, 2048); _Pragma("unroll") for (int n = 0; n < 9; ++n) by[n] = TRP(pw, n * 32 + 9216); } while (0)
#define ZMM(A, B, n0_, n1_) do { _Pragma("unroll") for (int n = n0_; n < n1_; ++n) { az[0][n] = mfma16(A[0], B[n], az[0][n]); az[1][n] = mfma16(A[1], B[n], az[1][n]); } } while (0)
#define ZSCHED(rest) do { _Pragma("unroll") for (int i_ = 0; i_ < 6; ++i_) { SGB(0x008, 1); SGB(0x100, 2); } _Pragma("unroll") for (int i_ = 0; i_ < 8; ++i_) { SGB(0x008, 1); SGB(0x100, 1); } if (rest) SGB(0x008, rest); SCHED0(); } while (0)
            XST(26);
            TAG_READY();
            XST(27);
            __builtin_amdgcn_s_setprio(CPRIO);
            BAR();
            STAMP(1);
            ZRD_X();
            WAIT_LB0();
#pragma unroll 1
            for (int t = 0; t < 8; ++t) {
                const int dl = (t & 3) == 3 ? -3 * SLOT : SLOT;
                ZRD_Y();
                ZMM(ax, bx, 0, 7);
                ZSCHED(0);
                ADV(e0, dl); ADV(e1, dl); ADV(pw, dl);
                ZMM(ax, bx, 7, 9); ZMM(ay, by, 0, 2);
                SCHED0();
                WAIT_LB0();
                BAR();
                ZRD_X();
                ZMM(ay, by, 2, 9);
                ZSCHED(0);
            }
            ZRD_Y();
            ZMM(ax, bx, 0, 7);
            ZSCHED(0);
            ZMM(ax, bx, 7, 9); ZMM(ay, by, 0, 2);
            SCHED0();
            WAIT_LB0();
            BAR();
            f32x4 st[4], cq[3];
#pragma unroll
            for (int j = 0; j < 4; ++j) st[j] = *(LAS f32x4*)(lds + L_ROWST + (16 * w + 4 * g + j) * 16);
#pragma unroll
            for (int j = 0; j < 3; ++j) cq[j] = *(LAS f32x4*)(lds + L_CS + fr * 48 + j * 16);
            ZMM(ay, by, 2, 9);
            SCHED_IL(7, 14);
            STAMP(2);
            f32x2 sum[4], sq[4], k1[4], k2[4];
            cq[2][0] += cq[2][1];
#pragma unroll
            for (int j = 0; j < 4; ++j) { const f32x2 rd = hi2(st[j]); k1[j] = rd * GC1; k2[j] = rd * rd * rd * GC2; }
#pragma unroll
            for (int n = 0; n < 9; ++n) {
                const float cc = -((n & 1) ? __uint_as_float(movs(__float_as_uint(cq[n >> 2][n & 3]))) : cq[n >> 2][n & 3]);
#pragma unroll
                for (int j = 0; j < 4; ++j) {
                    f32x4& a = az[j >> 1][n];
                    const f32x2 v = fma2(lo2(st[j]), (f32x2){cc, cc}, (j & 1) ? hi2(a) : lo2(a));
                    const f32x2 e = v * fma2(v * v, k2[j], k1[j]);
                    const f32x2 d = (f32x2){__builtin_amdgcn_exp2f(e.x), __builtin_amdgcn_exp2f(e.y)} + (f32x2){1.0f, 1.0f};
                    const f32x2 z = v * (f32x2){__builtin_amdgcn_rcpf(d.x), __builtin_amdgcn_rcpf(d.y)};
                    a[(j & 1) * 2] = z.x; a[(j & 1) * 2 + 1] = z.y;
                    if (n == 0) { sum[j] = z; sq[j] = z * z; } else { sum[j] += z; sq[j] = fma2(z, z, sq[j]); }
                }
            }
#pragma unroll
            for (int j = 0; j < 4; ++j) { const f32x2 rd = hi2(st[j]); sum[j] *= rd; sq[j] *= rd * rd; asm volatile("" : "+v"(sum[j]), "+v"(sq[j])); }
            SCHED0();
            LDSW(M_RC + w * 4) = 1u;
            SCHED0();
            float r0 = sum[0].x, r1 = sq[0].x, r2 = sum[0].y, r3 = sq[0].y, r4 = sum[1].x, r5 = sq[1].x, r6 = sum[1].y, r7 = sq[1].y;
            asm volatile(
                "s_nop 1\n"
                "v_add_f32_dpp %0, %0, %0 row_mirror row_mask:0xf bank_mask:0x3\n v_add_f32_dpp %1, %1, %1 row_mirror row_mask:0xf bank_mask:0x3\n"
                "v_add_f32_dpp %2, %2, %2 row_mirror row_mask:0xf bank_mask:0x3\n v_add_f32_dpp %3, %3, %3 row_mirror row_mask:0xf bank_mask:0x3\n"
                "v_add_f32_dpp %4, %4, %4 row_mirror row_mask:0xf bank_mask:0x3\n v_add_f32_dpp %5, %5, %5 row_mirror row_mask:0xf bank_mask:0x3\n"
                "v_add_f32_dpp %6, %6, %6 row_mirror row_mask:0xf bank_mask:0x3\n v_add_f32_dpp %7, %7, %7 row_mirror row_mask:0xf bank_mask:0x3\n"
                "v_add_f32_dpp %0, %8, %8 row_mirror row_mask:0xf bank_mask:0xc\n v_add_f32_dpp %1, %9, %9 row_mirror row_mask:0xf bank_mask:0xc\n"
                "v_add_f32_dpp %2, %10, %10 row_mirror row_mask:0xf bank_mask:0xc\n v_add_f32_dpp %3, %11, %11 row_mirror row_mask:0xf bank_mask:0xc\n"
                "v_add_f32_dpp %4, %12, %12 row_mirror row_mask:0xf bank_mask:0xc\n v_add_f32_dpp %5, %13, %13 row_mirror row_mask:0xf bank_mask:0xc\n"
                "v_add_f32_dpp %6, %14, %14 row_mirror row_mask:0xf bank_mask:0xc\n v_add_f32_dpp %7, %15, %15 row_mirror row_mask:0xf bank_mask:0xc\n"
                "v_add_f32_dpp %0, %0, %0 row_half_mirror row_mask:0xf bank_mask:0x5\n v_add_f32_dpp %1, %1, %1 row_half_mirror row_mask:0xf bank_mask:0x5\n"
                "v_add_f32_dpp %2, %2, %2 row_half_mirror row_mask:0xf bank_mask:0x5\n v_add_f32_dpp %3, %3, %3 row_half_mirror row_mask:0xf bank_mask:0x5\n"
                "v_add_f32_dpp %0, %4, %4 row_half_mirror row_mask:0xf bank_mask:0xa\n v_add_f32_dpp %1, %5, %5 row_half_mirror row_mask:0xf bank_mask:0xa\n"
                "v_add_f32_dpp %2, %6, %6 row_half_mirror row_mask:0xf bank_mask:0xa\n v_add_f32_dpp %3, %7, %7 row_half_mirror row_mask:0xf bank_mask:0xa\n"
                "v_add_f32_dpp %0, %0, %0 quad_perm:[1,0,3,2] row_mask:0xf bank_mask:0xf\n v_add_f32_dpp %1, %1, %1 quad_perm:[1,0,3,2] row_mask:0xf bank_mask:0xf\n"
                "v_add_f32_dpp %2, %2, %2 quad_perm:[1,0,3,2] row_mask:0xf bank_mask:0xf\n v_add_f32_dpp %3, %3, %3 quad_perm:[1,0,3,2] row_mask:0xf bank_mask:0xf\n"
                "v_add_f32_dpp %0, %0, %0 quad_perm:[2,3,0,1] row_mask:0xf bank_mask:0xf\n v_add_f32_dpp %1, %1, %1 quad_perm:[2,3,0,1] row_mask:0xf bank_mask:0xf\n"
                "v_add_f32_dpp %2, %2, %2 quad_perm:[2,3,0,1] row_mask:0xf bank_mask:0xf\n v_add_f32_dpp %3, %3, %3 quad_perm:[2,3,0,1] row_mask:0xf bank_mask:0xf\n"
                : "+v"(r0), "+v"(r1), "+v"(r2), "+v"(r3), "+v"(r4), "+v"(r5), "+v"(r6), "+v"(r7)
                : "v"(sum[2].x), "v"(sq[2].x), "v"(sum[2].y), "v"(sq[2].y), "v"(sum[3].x), "v"(sq[3].x), "v"(sum[3].y), "v"(sq[3].y));
            const unsigned tag32 = (unsigned)tag, so2 = WS_ST2 + pi * 16384 + w * 256;
            st16c(rs, p4 == 0 ? lane * 4 : 0x7fffff00u, so2 + pj * 1024, rec4(r0, r1, r2, r3, tag32));
            SCHED0();
            STAMP(3);
            {
                const u32x4 rwi = mk_rsrc(w_in, D * F * 2);
                SPIN(M_RL, 1);
                DMA_W1(7);
                SPIN(M_RL, 2);
                DMA_W1(8);
            }
            {
                u32x4 pv[4];
                unsigned spins = 0;
                for (;;) {
                    FENCE();
#pragma unroll
                    for (int i = 0; i < 4; ++i) pv[i] = ld16c(rs, g * 1024 + fr * 16, so2 + i * 4096);
                    const unsigned bad = ((pv[0][3] ^ tag32) | (pv[1][3] ^ tag32)) | ((pv[2][3] ^ tag32) | (pv[3][3] ^ tag32));
                    if (__builtin_amdgcn_ballot_w64(bad != 0u) == 0ull || ++spins > SPIN_CAP) break;
                }
                LDSW(M_RC + w * 4) = 2u;
                STAMP(5);
                const f32x2 c = rec_consts<4>(pv, 1.0f / FH);
                LAS float* p = (LAS float*)(lds + L_ZST + w * 256 + fr * 16 + (lane >> 5) * 4);
                p[0] = muls(c.y, *(LAS float*)(lds + L_ROWST + (16 * w + fr) * 16 + 8 + (lane >> 5) * 4)); p[2] = muls(-c.x, c.y);
            }
            FENCE();
            const unsigned zdst = WS_Z2N + ((pj * 32 + pi * 2 + (w >> 1)) * 18 + (w & 1)) * 1024;
#pragma unroll
            for (int j = 0; j < 4; ++j) st[j] = *(LAS f32x4*)(lds + L_ZST + w * 256 + (4 * g + j) * 16);
#pragma unroll
            for (int n = 0; n < 9; ++n) {
#pragma unroll
                for (int j = 0; j < 4; ++j) { const f32x4 a = az[j >> 1][n]; zp[n][j] = norm2s((j & 1) ? hi2(a) : lo2(a), st[j]); }
                st16c(rs, lane * 16, zdst + n * 2048, zp[n]);
                SCHED0();
            }
        }
        STAMP(6);
        SPIN(M_RL, 6);
#pragma unroll
        for (int n = 0; n < 9; ++n) *(LAS u32x4*)(lds + (w < 2 ? T18 : T19) + SL_A + n * 2048 + (w & 1) * 1024 + lane * 16) = zp[n];
        {
            const u32x4 rwo = mk_rsrc(w_out, b < 216 ? FH * D * 2 : 0);
            dma4v(rsp, lw + T18, vA, as0 + pi * 256);
            dma4v(rsp, lw + T19, vA, as0 + pi * 256 + 128);
            dma4v(rsp, lw, vA, as0 + k20);
#pragma unroll
            for (int i = 0; i < 3; ++i) dma1n(i * 1024, rwo, L0 + L_WSCR + w * 3072, vo[i], wos);
        }
        STAMP(4);
        WAIT_V(15);
        if (lane == 0) stflag(ws + WS_FZ + ((pj * 16 + pi) * 4 + w) * 8, tag);
        STAMP(7);
        asm volatile("" : "+v"(bsw[0]), "+v"(bsw[1]), "+v"(bsw[2]), "+v"(bsw[3]));
        f32x4 ac[4][4], a4[2];
#pragma unroll
        for (int m = 0; m < 4; ++m) {
            const float bs = bflo(bsw[m]);
#pragma unroll
            for (int l = 0; l < 4; ++l) ac[m][l] = (f32x4){bs, bs, bs, bs};
            if (m < 2) a4[m] = (f32x4){bs, bs, bs, bs};
            asm volatile("" : "+v"(ac[m][0]), "+v"(ac[m][1]), "+v"(ac[m][2]), "+v"(ac[m][3]));
        }
        asm volatile("" : "+v"(a4[0]), "+v"(a4[1]));
        const int swz = (g ^ (fr >> 1)) << 4;
        unsigned x01 = L0 + T18 + (64 * wm + 32 * wn + fr) * 128 + swz, x23 = L0 + T18 + (64 * wm + 32 * (1 - wn) + fr) * 128 + swz, y01 = x01 ^ 64, y23 = x23 ^ 64;
        unsigned pb = L0 + T18 + SL_A + 5 * wn * 2048 + lane * 16, p4_ = L0 + T18 + SL_A + 4 * 2048 + lane * 16;
        bf16x8 ax[4], bx[5], ay[4], by[5];
#define MMA_L(B, A, l) do { _Pragma("unroll") for (int m = 0; m < 4; ++m) ac[m][l] = mfma16(B[l], A[m], ac[m][l]); } while (0)
#define MMA_4(B, A) do { a4[0] = mfma16(B[4], A[0], a4[0]); a4[1] = mfma16(B[4], A[1], a4[1]); } while (0)
#define RDX_B() do { bx[0] = RDP(pb, 0); bx[1] = RDP(pb, 2048); bx[2] = RDP(pb, 4096); bx[3] = RDP(pb, 6144); } while (0)
#define RDX_A() do { ax[0] = RDP(x01, 0); ax[1] = RDP(x01, 2048); ax[2] = RDP(x23, 0); ax[3] = RDP(x23, 2048); bx[4] = RDP(p4_, 0); } while (0)
#define RDY() do { by[0] = RDP(pb, 1024); ay[0] = RDP(y01, 0); ay[1] = RDP(y01, 2048); ay[2] = RDP(y23, 0); ay[3] = RDP(y23, 2048); \
        by[1] = RDP(pb, 3072); by[2] = RDP(pb, 5120); by[3] = RDP(pb, 7168); by[4] = RDP(p4_, 1024); } while (0)
        asm volatile("" : "+v"(x01), "+v"(x23), "+v"(y01), "+v"(y23), "+v"(pb), "+v"(p4_));
        SCHED0();
        STAMP(8);
        WAIT_V(12);
        WAIT_L0();
        BAR();
        STAMP(9);
        RDX_B();
        RDX_A();
        int e0 = T19 - T18 - SLOT, e1 = 3 * SLOT - T19;
#define CSTEP(sync) do { \
            RDY(); \
            MMA_L(bx, ax, 0); MMA_L(bx, ax, 1); MMA_L(bx, ax, 2); \
            SCHED_IL(9, 12); \
            ADV(x01, dl); ADV(x23, dl); ADV(y01, dl); ADV(y23, dl); ADV(pb, dl); ADV(p4_, dl); \
            MMA_L(bx, ax, 3); \
            SGB(0x002, 2); SGB(0x008, 1); SGB(0x002, 2); SGB(0x008, 1); SGB(0x002, 2); SGB(0x008, 2); \
            SCHED0(); \
            sync; \
            RDX_B(); \
            MMA_4(bx, ax); MMA_L(by, ay, 0); \
            SCHED_IL(4, 6); \
            asm volatile("" : "+v"(a4[0]), "+v"(a4[1])); \
            RDX_A(); \
            MMA_L(by, ay, 1); MMA_L(by, ay, 2); MMA_L(by, ay, 3); MMA_4(by, ay); \
            SCHED_IL(5, 14); } while (0)
#pragma unroll 1
        for (int t = 18; t < 48; ++t) {
            const int dl = ((t & 3) == 3 ? -3 * SLOT : SLOT) + e0;
            e0 = opqs(e1); e1 = 0;
            CSTEP(__builtin_amdgcn_s_waitcnt(0x0071); BAR());
        }
        {
            const int dl = SLOT;
            CSTEP(LDSW(M_RC + w * 4) = 3u; SCHED0());
        }
        STAMP(10);
        RDY();
#pragma unroll
        for (int q = 0; q < 8; ++q) zp[q] = *(LAS u32x4*)(lds + ZS + ((4 * wm + (((q >> 1) + 2 * wn) & 3)) * 4 + 2 * wn + (q & 1)) * 1024 + lane * 16);
        zp[8] = *(LAS u32x4*)(lds + Z8OFF(2 * wm + wn) + lane * 16);
        MMA_L(bx, ax, 0); MMA_L(bx, ax, 1); MMA_L(bx, ax, 2); MMA_L(bx, ax, 3); MMA_4(bx, ax);
        SCHED_IL(18, 18);
        LDSW(M_RC + w * 4) = 4u;
        SCHED0();
        MMA_L(by, ay, 0); MMA_L(by, ay, 1); MMA_L(by, ay, 2); MMA_L(by, ay, 3); MMA_4(by, ay);
        SCHED0();
        STAMP(11);
        {
            const int kap0 = 9 * (pj >> 1) + 4 * (pj & 1) + 2 * wn, kap4 = 9 * (pj >> 1) + 8;
#pragma unroll
            for (int lp = 0; lp < 2; ++lp)
#pragma unroll
                for (int m = 0; m < 4; ++m) {
                    const unsigned dst = WS_P + (pi * 8 + 4 * wm + ((m + 2 * wn) & 3)) * (72 * 1024);
                    const u32x4 z = zp[m * 2 + lp];
                    const f32x4 a = ac[m][2 * lp], c = ac[m][2 * lp + 1];
                    st16c(rs, PSTO(lane * 16), dst + (kap0 + lp) * 1024, (u32x4){mulz(z[0], lo2(a)), mulz(z[1], hi2(a)), mulz(z[2], lo2(c)), mulz(z[3], hi2(c))});
                    SCHED0();
                }
#pragma unroll
            for (int m = 0; m < 2; ++m)
                st8c(rs, PSTO((lane >> 5) * 256 + fr * 16 + (g & 1) * 8), WS_P + (pi * 8 + 4 * wm + 2 * wn + m) * (72 * 1024) + kap4 * 1024 + (pj & 1) * 512, (u32x2){mulz(zp[8][2 * m], lo2(a4[m])), mulz(zp[8][2 * m + 1], hi2(a4[m]))});
        }
        SCHED0();
        if (CVD || RGW == 5) LDSW(M_RC + w * 4) = 5u;
        STAMP(12);
        TAILS(); TAILS2();
#pragma unroll
        for (int i = 0; i < CPRE; ++i) G3_LDB(i);
        SCHED0();
        WAIT_V(5 * CPRE);
        if (lane == 0) stflag(ws + WS_FP + ((pi * 16 + pj) * 4 + w) * 8, tag);
        __builtin_amdgcn_s_setprio(0);
        STAMP(13);
        SCHED0();
#pragma unroll
        for (int i = CPRE; i < RB; ++i) G3_LDB(i);
        SCHED0();
        if (w < 2) { SPIN(M_RC, 4); G3_DMA(redo, sB, sB4); }
        FP_ISSUE();
        STAMP(20);
        FP_CHECK(0);
        WAIT_VB(0);
        asm volatile(".Lmega_cend:");
    }
    {
        const int lane = opqv(tid) & 63, fr = lane & 15, g = lane >> 4;
        TAILS(); TAILS2();
        f32x4 acc[4][5];
        if (LPRIO) { if (wid >= 4) __builtin_amdgcn_s_setprio(LPRIO); }
        STAMP(21);
        const int l0 = 2 * (wid & 1);
#define OUTPOS() const int lane = opqv(tid) & 63, fr = lane & 15, g = lane >> 4, tA = 64 * rg + 16 * (wid >> 1) + fr, dA = 16 * (n30 + l0) + 4 * g, \
        tH = 64 * rg + 32 * (wid & 1) + 16 * (g >> 1) + fr, dH = 16 * n34 + 8 * (ct & 1) + 4 * (g & 1)
        u32x2 xv[3]; unsigned yv[3][4], fy; float mb[2], rd[2];
        bool ylate;
#define LOADY() do { OUTPOS(); _Pragma("unroll") for (int rr = 0; rr < 4; ++rr) { yv[0][rr] = ld2c(rs, (dA * S + tA) * 2, WS_YT + rr * (S * 2)); \
        yv[1][rr] = ld2c(rs, (dA * S + tA) * 2, WS_YT + (16 + rr) * (S * 2)); yv[2][rr] = ld2c(rs, (dH * S + tH) * 2, WS_YT + rr * (S * 2)); } } while (0)
        {
            bf16x8 aq[9][4];
            unsigned fw;
            const int fwo = wid_ == 2 || (wid_ == 3 && b_ < 64) ? M_FL + 12 : wid_ == 3 ? M_FL + 8 : M_RC;
            u64 fyv;
            G3_LDA(0); G3_LDA(1); G3_LDA(2); G3_LDA(3);
            SCHED0();
#pragma unroll
            for (int i = 0; i < 9; ++i) {
                if (i == YCK) {
                    asm volatile("" : "+v"(fyv) :: "memory");
                    ylate = !allset(fyv, tag);
                    LOADY();
                    SCHED0();
                }
#pragma unroll
                for (int m = 0; m < 4; ++m)
#pragma unroll
                    for (int l = 0; l < 5; ++l) acc[m][l] = mfma16(bq[i][l], aq[i][m], i == 0 ? zero4 : acc[m][l]);
                SCHED0();
                XST(30 + i);
                if (2 * i + 4 < 9) G3_LDA(2 * i + 4);
                if (2 * i + 5 < 9) G3_LDA(2 * i + 5);
                if (i == YIS) { const int lane = opqv(tid) & 63; fy = WS_FY + (lane < 32 ? 16 * (n30 + l0) + lane : 16 * n34 + 8 * (ct & 1) + (lane & 7)) * 8; fyv = ldflag(rs, fy); }
                if (i == XIS) {
                    OUTPOS();
                    xv[0] = *(const u32x2*)((const char*)x + (unsigned)((tA * D + dA) * 2));
                    xv[1] = *(const u32x2*)((const char*)x + (unsigned)((tA * D + dA + 16) * 2));
                    xv[2] = *(const u32x2*)((const char*)x + (unsigned)((tH * D + dH) * 2));
                    mb[0] = __uint_as_float(ld4c(rs, WS_ST1 + (tA >> 1) * 16 + (tA & 1) * 4)); rd[0] = __uint_as_float(ld4c(rs, WS_ST1 + (tA >> 1) * 16 + (tA & 1) * 4 + 8));
                    mb[1] = __uint_as_float(ld4c(rs, WS_ST1 + (tH >> 1) * 16 + (tH & 1) * 4)); rd[1] = __uint_as_float(ld4c(rs, WS_ST1 + (tH >> 1) * 16 + (tH & 1) * 4 + 8));
                }
                if (i == 2) fw = LDSW(fwo);
                if (i == 3) wcheck(fw, L0 + fwo, 4);
                if (i >= 3 && i + 2 < 9) G3_RDB(i + 2);
                SCHED0();
            }
        }
        STAMP(22);
        LAS char* red = lds + redo;
#pragma unroll
        for (int mm = 0; mm < 2; ++mm)
#pragma unroll
            for (int l = 0; l < 4; ++l) *(LAS f32x4*)(red + (mm * 4 + l) * 1024 + lane * 16) = acc[mm][l];
#pragma unroll
        for (int mm = 2; mm < 4; ++mm)
#pragma unroll
            for (int l = 0; l < 4; ++l) *(LAS f32x4*)(red + (mm * 4 + l) * 1024 + lane * 16) = acc[mm][l];
        if ((g >> 1) == (ct & 1)) {
#pragma unroll
            for (int mm = 0; mm < 4; ++mm) *(LAS f32x4*)(red + 16384 + mm * 512 + (lane & 31) * 16) = acc[mm][4];
        }
        STAMP(23);
        asm volatile("s_waitcnt lgkmcnt(0)\n s_barrier\n.Lmega_tail:" ::: "memory");
        STAMP(24);
        if (ylate) { spin_flags(rs, fy, tag); LOADY(); XST(39); }
        {
        OUTPOS();
#pragma unroll
        for (int u = 0; u < 3; ++u) {
            if (u == 2 && wid >= 2) break;
            const int off = (u < 2 ? (2 * wid + u) * 1024 : 16384 + wid * 1024) + lane * 16;
            f32x4 o = zero4;
#pragma unroll
            for (int v = 0; v < 4; ++v) o += *(LAS f32x4*)(lds + redoff(v) + off) + *(LAS f32x4*)(lds + redoff(v + 4) + off);
            float res[4];
#pragma unroll
            for (int rr = 0; rr < 4; ++rr) {
                const float ob = rbf(o[rr] * rd[u >> 1]);
                const float sg = rbf(ob * __builtin_amdgcn_rcpf(1.0f + __builtin_amdgcn_exp2f(-1.4426950408889634f * ob)));
                const float xx = (rr & 1) ? bfhi(xv[u][rr >> 1]) : bflo(xv[u][rr >> 1]);
                res[rr] = xx + rbf(rbf(rbf(rbf(xx - mb[u >> 1]) * rd[u >> 1]) * bflo(yv[u][rr])) * sg);
            }
            const unsigned oo = u < 2 ? (tA * D + dA + 16 * u) * 2 : (tH * D + dH) * 2;
            __builtin_amdgcn_raw_buffer_store_b64((u32x2){pk2(res[0], res[1]), pk2(res[2], res[3])}, in_rsrc(out, SZ_X), oo, 0, OAUX);
        }
        }
    }
}

extern "C" void kernel_launch(void* const* d_in, const int* in_sizes, int n_in,
                              void* d_out, int out_size, void* d_ws, size_t ws_size,
                              hipStream_t stream) {
    (void)hipFuncSetAttribute((const void*)mega, hipFuncAttributeMaxDynamicSharedMemorySize, LDS_SZ);
    mega<<<256, 512, LDS_SZ, stream>>>((const u16*)d_in[0], (const u16*)d_in[1], (const u16*)d_in[2], (const u16*)d_in[3], (const u16*)d_in[4], (const u16*)d_in[5],
                                       (const u16*)d_in[7], (const u16*)d_in[8], (const u16*)d_in[9], (const u16*)d_in[10], (char*)d_ws, (u16*)d_out);
}
```

```cpp
#include <hip/hip_runtime.h>
#include <stdint.h>
#define CODE_PF

#define LAS __attribute__((address_space(3)))
#define GAS __attribute__((address_space(1)))
typedef unsigned short u16;
typedef unsigned long long u64;
typedef short bf16x8 __attribute__((ext_vector_type(8)));
typedef short bf16x4 __attribute__((ext_vector_type(4)));
typedef float f32x4 __attribute__((ext_vector_type(4)));
typedef float f32x2 __attribute__((ext_vector_type(2)));
typedef unsigned u32x4 __attribute__((ext_vector_type(4)));
typedef unsigned u32x2 __attribute__((ext_vector_type(2)));
typedef __bf16 bf16x2_t __attribute__((ext_vector_type(2)));

constexpr int S = 2048, D = 576, F = 4608, FH = 2304;

__device__ __forceinline__ float bflo(unsigned w) { return __uint_as_float(w << 16); }
__device__ __forceinline__ float bfhi(unsigned w) { return __uint_as_float(w & 0xffff0000u); }
__device__ __forceinline__ unsigned pk2v(f32x2 p) { return __builtin_bit_cast(unsigned, __builtin_convertvector(p, bf16x2_t)); }
__device__ __forceinline__ unsigned pk2(float lo, float hi) { return pk2v((f32x2){lo, hi}); }
__device__ __forceinline__ f32x2 up2(unsigned w) { return (f32x2){bflo(w), bfhi(w)}; }
__device__ __forceinline__ float rbf(float x) { return bflo(pk2(x, x)); }
__device__ __forceinline__ f32x2 rbf2(f32x2 x) { return up2(pk2v(x)); }
__device__ __forceinline__ f32x2 fma2(f32x2 a, f32x2 b, f32x2 c) { return __builtin_elementwise_fma(a, b, c); }
__device__ __forceinline__ f32x2 lo2(f32x4 a) { return (f32x2){a[0], a[1]}; }
__device__ __forceinline__ f32x2 hi2(f32x4 a) { return (f32x2){a[2], a[3]}; }
__device__ __forceinline__ f32x4 mfma16(bf16x8 a, bf16x8 b, f32x4 c) { return __builtin_amdgcn_mfma_f32_16x16x32_bf16(a, b, c, 0, 0, 0); }
__device__ __forceinline__ bf16x4 trread(LAS char* p) { return __builtin_bit_cast(bf16x4, __builtin_amdgcn_ds_read_tr16_b64_v4i16((LAS bf16x4*)p)); }
__device__ __forceinline__ bf16x8 cat8(bf16x4 a, bf16x4 b) { return __builtin_shufflevector(a, b, 0, 1, 2, 3, 4, 5, 6, 7); }
#define WAIT_V(n) asm volatile("s_waitcnt vmcnt(%0)" ::"n"(n) : "memory")
#define WAIT_VB(n) __builtin_amdgcn_s_waitcnt(((n) & 15) | (7 << 4) | (15 << 8) | (((n) >> 4) << 14))
#define WAIT_L0() asm volatile("s_waitcnt lgkmcnt(0)" ::: "memory")
#define BAR() do { asm volatile("" ::: "memory"); __builtin_amdgcn_s_barrier(); asm volatile("" ::: "memory"); } while (0)
#define SCHED0() __builtin_amdgcn_sched_barrier(0)
#define RLX_AGENT __ATOMIC_RELAXED, __HIP_MEMORY_SCOPE_AGENT
#define PIN9(z) asm volatile("" : "+v"(z[0]), "+v"(z[1]), "+v"(z[2]), "+v"(z[3]), "+v"(z[4]), "+v"(z[5]), "+v"(z[6]), "+v"(z[7]), "+v"(z[8]))
__device__ __forceinline__ int opqs(int x) { asm volatile("" : "+s"(x)); return x; }
__device__ __forceinline__ int opqv(int x) { asm volatile("" : "+v"(x)); return x; }
template <int I> struct IC { static constexpr int v = I; };
template <int A, int B, class Fn> __device__ __forceinline__ void sfor(Fn&& f) { if constexpr (A < B) { f(IC<A>{}); sfor<A + 1, B>(f); } }
#define SFOR(i, a, b) sfor<a, b>([&](auto i##_) __attribute__((always_inline)) { constexpr int i = decltype(i##_)::v;
#define SEND });
#define RFL(x) __builtin_amdgcn_readfirstlane(x)

#define TIMER "s_memrealtime"
#define STAMP(i) do { } while (0)

__device__ __forceinline__ f32x2 gelu2(f32x2 v) {
    const float c1 = -2.0f * 0.7978845608028654f * 1.4426950408889634f;
    const float c2 = c1 * 0.044715f;
    f32x2 a = v * fma2(v * v, (f32x2){c2, c2}, (f32x2){c1, c1});
    f32x2 d = (f32x2){__builtin_amdgcn_exp2f(a.x), __builtin_amdgcn_exp2f(a.y)} + (f32x2){1.0f, 1.0f};
    return v * (f32x2){__builtin_amdgcn_rcpf(d.x), __builtin_amdgcn_rcpf(d.y)};
}
__device__ __forceinline__ float adds(float a, float b) { float o; asm("v_add_f32 %0, %1, %2" : "=v"(o) : "v"(a), "v"(b)); return o; }
__device__ __forceinline__ float muls(float a, float b) { float o; asm("v_mul_f32 %0, %1, %2" : "=v"(o) : "v"(a), "v"(b)); return o; }
__device__ __forceinline__ f32x2 ln_consts(float sm, float sq, float inv_n) {
    float mean, m2;
    asm("v_mul_f32 %0, %2, %3\n v_mul_f32 %1, %2, %4" : "=&v"(mean), "=v"(m2) : "s"(inv_n), "v"(sm), "v"(sq));
    const float var = fmaxf(fmaf(-mean, mean, m2), 0.f);
    return (f32x2){rbf(mean), __builtin_amdgcn_rcpf(rbf(__builtin_amdgcn_sqrtf(rbf(rbf(var) + 1e-5f))))};
}

constexpr unsigned SZ_Z = S * FH * 2, SZ_X = S * D * 2;
constexpr unsigned WS_Z2N = 0;
constexpr unsigned WS_P = WS_Z2N + SZ_Z;
constexpr unsigned WS_KVT = WS_P + SZ_Z;
constexpr unsigned WS_YT = WS_KVT + SZ_X;
constexpr unsigned WS_WT = WS_YT + SZ_X;
constexpr unsigned WS_ST2 = WS_WT + FH * D * 2;
constexpr unsigned WS_ST1 = WS_ST2 + 256 * 1024;
constexpr unsigned WS_SR = WS_ST1 + 16384;
constexpr unsigned WS_SC = WS_SR + 16 * 12288;
constexpr unsigned WS_FZ = WS_SC + 32 * 12288;
constexpr unsigned WS_FP = WS_FZ + 8192;
constexpr unsigned WS_FK = WS_FP + 8192;
constexpr unsigned WS_FY = WS_FK + 2048;
constexpr unsigned WS_E = WS_FY + 4608 + 128;
constexpr unsigned WS_END = WS_E + 128;
constexpr u64 TAG_INC = 0x9E3779B97F4A7C15ull;

__device__ __forceinline__ __amdgpu_buffer_rsrc_t ws_rsrc(char* ws) { return __builtin_amdgcn_make_buffer_rsrc(ws, 0, (int)WS_END, 0x00020000); }
__device__ __forceinline__ void st16c(__amdgpu_buffer_rsrc_t r, unsigned off, unsigned soff, u32x4 v) { __builtin_amdgcn_raw_buffer_store_b128(v, r, off, soff, 16); asm volatile("s_nop 1" :: "v"(v)); }
__device__ __forceinline__ void st8c(__amdgpu_buffer_rsrc_t r, unsigned off, unsigned soff, u32x2 v) { __builtin_amdgcn_raw_buffer_store_b64(v, r, off, soff, 16); }
__device__ __forceinline__ u32x4 ld16c(__amdgpu_buffer_rsrc_t r, unsigned off, unsigned soff) { return __builtin_amdgcn_raw_buffer_load_b128(r, off, soff, 16); }
__device__ __forceinline__ unsigned ld4c(__amdgpu_buffer_rsrc_t r, unsigned off) { return __builtin_amdgcn_raw_buffer_load_b32(r, off, 0, 16); }
__device__ __forceinline__ unsigned ld2c(__amdgpu_buffer_rsrc_t r, unsigned off, unsigned soff) { return __builtin_amdgcn_raw_buffer_load_b16(r, off, soff, 16); }
__device__ __forceinline__ __amdgpu_buffer_rsrc_t in_rsrc(const void* p, unsigned bytes) { return __builtin_amdgcn_make_buffer_rsrc((void*)p, 0, (int)bytes, 0x00020000); }
__device__ __forceinline__ u32x4 mk_rsrc(const void* p, unsigned bytes) { const u64 a = (u64)p; return (u32x4){(unsigned)a, (unsigned)(a >> 32) & 0xffffu, bytes, 0x00020000u}; }
#define DMA_HEAD "s_add_i32 m0, %1, %5\n s_add_i32 %0, %4, %6\n buffer_load_dwordx4 %2, %3, %0 offen"
#define DMA_NEXT(o) "\n buffer_load_dwordx4 %2, %3, %0 offen offset:" #o " sc1 lds"
#define DMA_OPS : "=&s"(t) : "s"(lb), "v"(voff), "s"(r), "s"(sb), "n"(LI), "n"(SI) : "scc", "memory"
template <int LI, int SI> __device__ __forceinline__ void dma16(u32x4 r, unsigned lb, unsigned voff, unsigned sb) { unsigned t; asm volatile(DMA_HEAD " lds" DMA_OPS); }
template <int LI, int SI> __device__ __forceinline__ void dma16c(u32x4 r, unsigned lb, unsigned voff, unsigned sb) { unsigned t; asm volatile(DMA_HEAD " sc1 lds" DMA_OPS); }
template <int LI, int SI> __device__ __forceinline__ void dma16c3(u32x4 r, unsigned lb, unsigned voff, unsigned sb) { unsigned t; asm volatile(DMA_HEAD " sc1 lds" DMA_NEXT(1024) DMA_NEXT(2048) DMA_OPS); }
template <int LI, int SI> __device__ __forceinline__ void dma16c4(u32x4 r, unsigned lb, unsigned voff, unsigned sb) { unsigned t; asm volatile(DMA_HEAD " sc1 lds" DMA_NEXT(1024) DMA_NEXT(2048) DMA_NEXT(3072) DMA_OPS); }
template <int SI> __device__ __forceinline__ void pf4(u32x4 r, unsigned lb, unsigned voff, unsigned sb) {
    unsigned t;
    asm volatile("s_mov_b32 m0, %1\n s_add_i32 %0, %4, %5\n buffer_load_dword %2, %3, %0 offen lds" : "=&s"(t) : "s"(lb), "v"(voff), "s"(r), "s"(sb), "n"(SI) : "scc", "memory");
}
template <int SI> __device__ __forceinline__ void pf4c(u32x4 r, unsigned lb, unsigned voff, unsigned sb) {
    unsigned t;
    asm volatile("s_mov_b32 m0, %1\n s_add_i32 %0, %4, %5\n buffer_load_dword %2, %3, %0 offen sc1 lds" : "=&s"(t) : "s"(lb), "v"(voff), "s"(r), "s"(sb), "n"(SI) : "scc", "memory");
}
__device__ __forceinline__ void dma4v(u32x4 r, unsigned lb, const unsigned* v, unsigned sb) {
    asm volatile("s_mov_b32 m0, %0\n s_nop 0\n buffer_load_dwordx4 %1, %5, %6 offen lds\n buffer_load_dwordx4 %2, %5, %6 offen offset:1024 lds\n"
                 "buffer_load_dwordx4 %3, %5, %6 offen offset:2048 lds\n buffer_load_dwordx4 %4, %5, %6 offen offset:3072 lds"
                 :: "s"(lb), "v"(v[0]), "v"(v[1]), "v"(v[2]), "v"(v[3]), "s"(r), "s"(sb) : "memory");
}
__device__ __forceinline__ void dmah(u32x4 r, unsigned lb, unsigned v, unsigned sb) {
    asm volatile("s_mov_b32 m0, %0\n s_mov_b32 exec_hi, 0\n buffer_load_dwordx4 %1, %2, %3 offen lds\n s_mov_b32 exec_hi, -1" :: "s"(lb), "v"(v), "s"(r), "s"(sb) : "memory");
}
__device__ __forceinline__ void dmahc(u32x4 r, unsigned lb, unsigned v, unsigned sb) {
    asm volatile("s_mov_b32 m0, %0\n s_mov_b32 exec_hi, 0\n buffer_load_dwordx4 %1, %2, %3 offen sc1 lds\n s_mov_b32 exec_hi, -1" :: "s"(lb), "v"(v), "s"(r), "s"(sb) : "memory");
}
__device__ __forceinline__ u64 ldflag(__amdgpu_buffer_rsrc_t r, unsigned off) {
    asm volatile("" ::: "memory");
    return __builtin_bit_cast(u64, __builtin_amdgcn_raw_buffer_load_b64(r, off, 0, 16));
}
__device__ __forceinline__ void stflag(char* p, u64 v) { __hip_atomic_store((GAS u64*)p, v, RLX_AGENT); }
__device__ __forceinline__ bool allset(u64 v, u64 tag) { return __builtin_amdgcn_ballot_w64(v != tag) == 0ull; }

constexpr int SL_A = 16384, SL_B = 18432, SLOT = SL_A + SL_B;
constexpr unsigned SPIN_CAP = 1u << 20;
constexpr int L_ROWST = 145408;
constexpr int L_CS = L_ROWST + 1024;
constexpr int L_ZST = L_CS + 1024;
constexpr int L_WSCR = L_ZST + 1024;
constexpr int LDS_SZ = 163840;
constexpr int L_DUMP = LDS_SZ - 256;
constexpr int L_MISC = L_DUMP - 256;
constexpr int M_RL = L_MISC, M_FL = L_MISC + 16, M_RC = L_MISC + 32, M_RS = L_MISC + 48;
constexpr int CV_U = 512 + 4096, CV_HSTRIDE = 4160, CV_WAVE = CV_U + 4 * CV_HSTRIDE;
constexpr int RED_W = 18432, ZS = 2 * SLOT;
__host__ __device__ constexpr int redoff(int v) { return v < 4 ? 256 + CV_WAVE + v * RED_W : v == 7 ? 256 + CV_WAVE + 4 * RED_W : v == 6 ? 256 : (v == 4 ? L_MISC : 4 * SLOT) - CV_WAVE; }
#define Z8OFF(w_) (ZS + 32768 + (w_) * 1024 + ((w_) >> 1) * SLOT)
constexpr u64 redtab(int v) { return (u64)(redoff(v) >> 8) | (u64)(redoff(v + 1) >> 8) << 16 | (u64)(redoff(v + 2) >> 8) << 32 | (u64)(redoff(v + 3) >> 8) << 48; }
__host__ __device__ constexpr int warea(int j) { return 16384 + 18432 * (j == 0 || j == 7 ? 1 : j == 1 || j == 8 ? 0 : j == 2 ? 3 : j == 3 ? 5 : j == 4 ? 4 : j == 5 ? 6 : 2); }
constexpr int T18 = warea(2), T19 = warea(3), KVS = warea(6);
typedef unsigned u32x3 __attribute__((ext_vector_type(3)));

#define SCHED_IL(nr, nm) do { _Pragma("unroll") for (int i_ = 0; i_ < (nr); ++i_) { __builtin_amdgcn_sched_group_barrier(0x008, 1, 0); __builtin_amdgcn_sched_group_barrier(0x100, 1, 0); } \
        __builtin_amdgcn_sched_group_barrier(0x008, (nm) - (nr), 0); SCHED0(); } while (0)

__device__ __forceinline__ void spin_flags(__amdgpu_buffer_rsrc_t r, unsigned p, u64 tag) {
    unsigned spins = 0;
    while (!allset(ldflag(r, p), tag)) { if (++spins > SPIN_CAP) break; __builtin_amdgcn_s_sleep(1); }
    asm volatile("" ::: "memory");
}
__device__ __forceinline__ void dma1(int OFF, u32x4 r, unsigned lb, unsigned v, unsigned sb) {
    asm volatile("s_mov_b32 m0, %0\n s_nop 0\n buffer_load_dwordx4 %1, %2, %3 offen offset:%4 lds" :: "s"(lb), "v"(v), "s"(r), "s"(sb), "n"(OFF) : "memory");
}
#define WNT " nt"
#define KNT ""
__device__ __forceinline__ void dma1n(int OFF, u32x4 r, unsigned lb, unsigned v, unsigned sb) {
    asm volatile("s_mov_b32 m0, %0\n s_nop 0\n buffer_load_dwordx4 %1, %2, %3 offen offset:%4" WNT " lds" :: "s"(lb), "v"(v), "s"(r), "s"(sb), "n"(OFF) : "memory");
}
__device__ __forceinline__ void dma1c(int OFF, u32x4 r, unsigned lb, unsigned v, unsigned sb) {
    asm volatile("s_mov_b32 m0, %0\n s_nop 0\n buffer_load_dwordx4 %1, %2, %3 offen offset:%4 sc1 lds" :: "s"(lb), "v"(v), "s"(r), "s"(sb), "n"(OFF) : "memory");
}
__device__ __forceinline__ u32x4 rec4(float a, float b, float c, float d, unsigned tag) {
    const unsigned q = __float_as_uint(d);
    return (u32x4){__builtin_amdgcn_perm(__float_as_uint(a), q, 0x07060501u), __builtin_amdgcn_perm(__float_as_uint(b), q, 0x07060502u), __builtin_amdgcn_perm(__float_as_uint(c), q, 0x07060503u), tag};
}
__device__ __forceinline__ float half_sum(float a, float b) {
    asm("s_nop 1\n v_permlane32_swap_b32 %0, %1\n v_add_f32 %0, %0, %1\n v_mov_b32 %1, %0\n s_nop 1\n v_permlane16_swap_b32 %0, %1\n v_add_f32 %0, %0, %1" : "+v"(a), "+v"(b));
    return a;
}
template <int N> __device__ __forceinline__ f32x2 rec_consts(const u32x4* pv, float inv_n) {
    float a0, a1, a2, a3;
#pragma unroll
    for (int i = 0; i < N; ++i) {
        const float e = __uint_as_float(__builtin_amdgcn_perm(pv[i][2], __builtin_amdgcn_perm(pv[i][1], pv[i][0], 0x0c04000cu), 0x04020100u));
        if (i == 0) { a0 = __uint_as_float(pv[i][0]); a1 = __uint_as_float(pv[i][1]); a2 = __uint_as_float(pv[i][2]); a3 = e; }
        else { a0 = adds(a0, __uint_as_float(pv[i][0])); a1 = adds(a1, __uint_as_float(pv[i][1])); a2 = adds(a2, __uint_as_float(pv[i][2])); a3 = adds(a3, e); }
    }
    return ln_consts(half_sum(a0, a2), half_sum(a1, a3), inv_n);
}
__device__ __forceinline__ void wcheck(unsigned f, unsigned addr, unsigned need) {
    unsigned cnt;
    asm volatile("v_cmp_gt_u32 vcc, %3, %0\n s_cbranch_vccz 2f\n s_mov_b32 %1, 0x400000\n"
                 "1:\n s_sleep 1\n ds_read_b32 %0, %2\n s_sub_u32 %1, %1, 1\n s_waitcnt lgkmcnt(0)\n s_cbranch_scc1 2f\n v_cmp_gt_u32 vcc, %3, %0\n s_cbranch_vccnz 1b\n2:"
                 : "+v"(f), "=&s"(cnt) : "v"(addr), "n"(need) : "vcc", "scc", "memory");
}
__device__ __forceinline__ unsigned norm2s(f32x2 z, f32x4 st) {
    unsigned o, t;
    asm("v_fma_f32 %0, %2, %4, %6\n v_fma_f32 %1, %3, %5, %7\n v_cvt_pk_bf16_f32 %0, %0, %1" : "=&v"(o), "=&v"(t) : "v"(z.x), "v"(z.y), "v"(st[0]), "v"(st[1]), "v"(st[2]), "v"(st[3]));
    return o;
}
__device__ __forceinline__ void tap4d(float& o0, float& o1, const float* k, const float* h, float v0, float v1) {
    asm("v_mul_f32 %0, %2, %10\n v_mul_f32 %1, %3, %10\n v_fma_f32 %0, %3, %9, %0\n v_fma_f32 %1, %4, %9, %1\n v_fma_f32 %0, %4, %8, %0\n v_fma_f32 %1, %5, %8, %1\n"
        "v_fma_f32 %0, %5, %7, %0\n v_fma_f32 %1, %6, %7, %1\n v_mul_f32 %0, %0, %11\n v_mul_f32 %1, %1, %12"
        : "=&v"(o0), "=&v"(o1) : "v"(k[0]), "v"(k[1]), "v"(k[2]), "v"(k[3]), "v"(k[4]), "v"(h[0]), "v"(h[1]), "v"(h[2]), "v"(h[3]), "v"(v0), "v"(v1));
}

__device__ __forceinline__ float fmas(float a, float b, float c) { float o; asm("v_fma_f32 %0, %1, %2, %3" : "=v"(o) : "v"(a), "v"(b), "v"(c)); return o; }
__device__ __forceinline__ unsigned mulz(unsigned z, f32x2 a) {
    unsigned o, t;
    asm("v_lshlrev_b32 %0, 16, %2\n v_and_b32 %1, 0xffff0000, %2\n v_mul_f32 %0, %0, %3\n v_mul_f32 %1, %1, %4\n v_cvt_pk_bf16_f32 %0, %0, %1" : "=&v"(o), "=&v"(t) : "v"(z), "v"(a.x), "v"(a.y));
    return o;
}
__device__ __forceinline__ unsigned movs(unsigned a) { unsigned o; asm("v_mov_b32 %0, %1" : "=v"(o) : "v"(a)); return o; }
__device__ __forceinline__ unsigned algn(unsigned hi, unsigned lo) { unsigned o; asm("v_alignbit_b32 %0, %1, %2, 16" : "=v"(o) : "v"(hi), "v"(lo)); return o; }
__device__ __forceinline__ unsigned algn2(unsigned hi, unsigned lo) { unsigned o; asm("v_alignbit_b32 %0, %1, %2, 16 " : "=v"(o) : "v"(hi), "v"(lo)); return o; }

#define YPRIO 0
struct ConvIn { u32x4 uu[4], hh[4]; u32x2 hn[4]; unsigned dd; };
#define ALD(w_, dst, vo, r_, so, x_) asm volatile("buffer_load_dword" w_ " %0, %1, %2, %3 offen" x_ : "=v"(dst) : "v"(vo), "s"(r_), "s"(so) : "memory")
__device__ __forceinline__ void conv_load(ConvIn& c, const u16* __restrict__ hk, const u16* __restrict__ dv, u32x4 wr, int d, int lane) {
    const u32x4 rh = mk_rsrc(hk, SZ_X);
    const unsigned v = lane * 16, su = WS_KVT + d * (S * 2), sh = d * (S * 2);
    ALD("x4", c.uu[0], v, wr, su, " sc1"); ALD("x4", c.uu[1], v, wr, su, " offset:1024 sc1"); ALD("x4", c.uu[2], v, wr, su, " offset:2048 sc1"); ALD("x4", c.uu[3], v, wr, su, " offset:3072 sc1");
    ALD("x4", c.hh[0], v, rh, sh, ""); ALD("x4", c.hh[1], v, rh, sh, " offset:1024"); ALD("x4", c.hh[2], v, rh, sh, " offset:2048"); ALD("x4", c.hh[3], v, rh, sh, " offset:3072");
    ALD("x2", c.hn[0], v, rh, sh, " offset:16"); ALD("x2", c.hn[1], v, rh, sh, " offset:1040"); ALD("x2", c.hn[2], v, rh, sh, " offset:2064"); ALD("x2", c.hn[3], v, rh, sh, " offset:3088");
    c.dd = ((const unsigned*)dv)[d >> 1];
}
#define CONV_PIN(c) asm volatile("" : "+v"(c.uu[0]), "+v"(c.uu[1]), "+v"(c.uu[2]), "+v"(c.uu[3]), "+v"(c.hh[0]), "+v"(c.hh[1]), "+v"(c.hh[2]), "+v"(c.hh[3]), "+v"(c.hn[0]), "+v"(c.hn[1]), "+v"(c.hn[2]), "+v"(c.hn[3]))
template <class MID> __device__ __forceinline__ void conv_stage(ConvIn& c, LAS char* U, int lane, MID mid) {
    LAS char* H = U + CV_U;
    if (lane == 63) c.hn[3] = (u32x2){0u, 0u};
    if (lane < 32) *(LAS u32x4*)(U + lane * 16) = (u32x4){0u, 0u, 0u, 0u};
    const unsigned h0 = RFL(c.hh[0][0]), h1 = RFL(c.hh[0][1]);
    if (lane < 8) {
        const int cc = lane >> 1, half = lane & 1;
        u32x4 z = {0u, 0u, 0u, 0u};
        if (half) {
            if (cc == 1) z[3] = h0 << 16;
            if (cc == 2) z[3] = h0;
            if (cc == 3) { z[2] = h0 << 16; z[3] = __builtin_amdgcn_alignbit(h1, h0, 16); }
        }
        *(LAS u32x4*)(H + cc * CV_HSTRIDE + half * 16) = z;
    }
    SFOR(i, 0, 4)
        SCHED0();
        mid(IC<i>{});
        SCHED0();
        *(LAS u32x4*)(U + 512 + i * 1024 + lane * 16) = c.uu[i];
        const unsigned w0 = c.hh[i][0], w1 = c.hh[i][1], w2 = c.hh[i][2], w3 = c.hh[i][3], w4 = c.hn[i][0], w5 = c.hn[i][1];
        LAS char* p = H + 32 + i * 1024 + lane * 16;
        *(LAS u32x4*)(p) = c.hh[i];
        *(LAS u32x4*)(p + CV_HSTRIDE) = (u32x4){algn(w1, w0), algn(w2, w1), algn(w3, w2), algn(w4, w3)};
        *(LAS u32x4*)(p + 2 * CV_HSTRIDE) = (u32x4){movs(w1), movs(w2), movs(w3), movs(w4)};
        *(LAS u32x4*)(p + 3 * CV_HSTRIDE) = (u32x4){algn2(w2, w1), algn2(w3, w2), algn2(w4, w3), algn2(w5, w4)};
    SEND
}
__device__ __forceinline__ void conv_run(unsigned ddw, __amdgpu_buffer_rsrc_t rs, LAS char* U, int d, int lane) {
    const int fr = lane & 15, g = lane >> 4;
    LAS char* H = U + CV_U;
    const int e0 = 16 * (g >> 1) - 8 * (g & 1) - 7 + fr + 16;
    const int ac = e0 & 3;
    LAS char* ap = H + ac * CV_HSTRIDE + (e0 - ac) * 2;
    LAS char* bp = U + 512 + 32 * (fr - (g >> 1)) + 16 * (g & 1) - 448;
    f32x4 acc[8];
#pragma unroll
    for (int n = 0; n < 8; ++n) acc[n] = (f32x4){0.f, 0.f, 0.f, 0.f};
    bf16x8 LA[2][4], LB[2][4], HA[4], HB[4];
#define CV_RD(A, B, a0, pa, pb) do { _Pragma("unroll") for (int a = 0; a < 4; ++a) { B[a] = *(LAS bf16x8*)((pb) + (a0 + a) * 512); \
        A[a] = cat8(*(LAS bf16x4*)((pa) + (a0 + a) * 512), *(LAS bf16x4*)((pa) + (a0 + a) * 512 + 8)); } } while (0)
#define CV_STEP(cur, nxt, pa, pb) do { \
        CV_RD(HA, HB, 4, pa, pb); \
        _Pragma("unroll") for (int s = 0; s < 4; ++s) { \
            _Pragma("unroll") for (int a = 0; a < s; ++a) { acc[a + s] = mfma16(LA[cur][a], LB[cur][s], acc[a + s]); acc[a + s] = mfma16(LA[cur][s], LB[cur][a], acc[a + s]); } \
            acc[2 * s] = mfma16(LA[cur][s], LB[cur][s], acc[2 * s]); } \
        SCHED_IL(12, 16); \
        CV_RD(LA[nxt], LB[nxt], 0, (pa) + 64, (pb) - 64); \
        _Pragma("unroll") for (int s = 0; s < 4; ++s) \
            _Pragma("unroll") for (int a = 0; a + s < 4; ++a) { acc[a + s + 4] = mfma16(LA[cur][a], HB[s], acc[a + s + 4]); acc[a + s + 4] = mfma16(HA[s], LB[cur][a], acc[a + s + 4]); } \
        SCHED_IL(12, 20); \
    } while (0)
    CV_RD(LA[0], LB[0], 0, ap, bp + 448);
#pragma unroll 1
    for (int bb = 0; bb < 8; bb += 2) {
        LAS char* pa = ap + bb * 64;
        LAS char* pb = bp + (7 - bb) * 64;
        CV_STEP(0, 1, pa, pb);
        CV_STEP(1, 0, pa + 64, pb - 64);
    }
    if (YPRIO) __builtin_amdgcn_s_setprio(YPRIO);
    const float dd = (d & 1) ? bfhi(ddw) : bflo(ddw);
    unsigned yw[8][2];
    u32x2 uw[8];
#pragma unroll
    for (int nu = 0; nu < 8; ++nu) uw[nu] = *(LAS u32x2*)(U + 512 + (256 * nu + 16 * fr + 8 * (g >> 1)) * 2 + ((g & 1) ? 0 : 8));
#pragma unroll
    for (int nu = 0; nu < 8; ++nu) {
        yw[nu][0] = pk2(fmas(bfhi(uw[nu][1]), dd, acc[nu][0]), fmas(bflo(uw[nu][1]), dd, acc[nu][1]));
        yw[nu][1] = pk2(fmas(bfhi(uw[nu][0]), dd, acc[nu][2]), fmas(bflo(uw[nu][0]), dd, acc[nu][3]));
    }
#pragma unroll
    for (int p = 0; p < 4; ++p) {
        const u32x2 s0 = __builtin_amdgcn_permlane16_swap(yw[2 * p][0], yw[2 * p + 1][0], false, false);
        const u32x2 s1 = __builtin_amdgcn_permlane16_swap(yw[2 * p][1], yw[2 * p + 1][1], false, false);
        st16c(rs, (256 * (g & 1) + 16 * fr + 8 * (g >> 1)) * 2 + p * 1024, WS_YT + d * (S * 2), (u32x4){s0[0], s1[0], s0[1], s1[1]});
    }
    SCHED0();
}

constexpr int RB = 5, KST = 36 * 1024;
#define KROT 0
#define SPOLL 0
#define KAP(i) ((i) + (KROT) >= 9 ? (i) + (KROT) - 9 : (i) + (KROT))
#define G3_LDB(i) do { _Pragma("unroll") for (int l = 0; l < 4; ++l) bq[i][l] = __builtin_bit_cast(bf16x8, ld16c(rs, lane * 16 + l * 1024, sB + KAP(i) * KST)); \
        bq[i][4] = __builtin_bit_cast(bf16x8, ld16c(rs, lane * 16, sB4 + KAP(i) * KST)); } while (0)
#define G3_LDA(i) do { _Pragma("unroll") for (int m = 0; m < 4; ++m) aq[i][m] = __builtin_bit_cast(bf16x8, ld16c(rs, lane * 16, sA + m * (72 * 1024) + KAP(i) * 1024)); } while (0)
#define G3_RDB(i) do { _Pragma("unroll") for (int l = 0; l < 4; ++l) bq[i][l] = *(LAS bf16x8*)(lds + redo + ((i) - RB) * 4096 + l * 1024 + lane * 16); \
        bq[i][4] = *(LAS bf16x8*)(lds + redo + 16384 + ((i) - RB) * 512 + (g * 8 + (fr & 7)) * 16); } while (0)
#define G3_PRE() do { _Pragma("unroll") for (int i = 0; i < RB; ++i) G3_LDB(i); SCHED0(); } while (0)
#define G3_HO ((((lane >> 3) & 3) * 16 + 8 * (ct & 1) + (lane & 7)) * 16)
#define G3_DMA1(dst, sB, sB4) do { dma16c4<0, 0>(wr, L0 + (dst), lane * 16, sB + KAP(5) * KST); dma16c4<4096, 0>(wr, L0 + (dst), lane * 16, sB + KAP(6) * KST); \
        dma16c<16384, 0>(wr, L0 + (dst), G3_HO + (lane < 32 ? KAP(5) : KAP(6)) * KST, sB4); } while (0)
#define G3_DMA2(dst, sB, sB4) do { dma16c4<8192, 0>(wr, L0 + (dst), lane * 16, sB + KAP(7) * KST); dma16c4<12288, 0>(wr, L0 + (dst), lane * 16, sB + KAP(8) * KST); \
        dma16c<17408, 0>(wr, L0 + (dst), G3_HO + (lane < 32 ? KAP(7) : KAP(8)) * KST, sB4); } while (0)
#define G3_DMA(dst, sB, sB4) do { G3_DMA1(dst, sB, sB4); G3_DMA2(dst, sB, sB4); } while (0)
#define FLAG_ISSUE(v_, o_) asm volatile("buffer_load_dwordx2 %0, %1, %2, 0 offen sc1" : "=v"(v_) : "v"(o_), "s"(wr) : "memory")
#define FP_SPIN() spin_flags(rs, sF + (lane & 1) * 16 + (lane & 2) * 16, tag)
#define FP_ISSUE() FLAG_ISSUE(fpv, sF + (lane & 1) * 16 + (lane & 2) * 16)
#define FP_CHECK(NV) do { u64 ne; \
        asm volatile("s_waitcnt vmcnt(%3)\n v_cmp_ne_u64 %0, %1, %2" : "=s"(ne) : "s"(tag), "v"(fpv), "n"(NV) : "memory"); \
        if (ne != 0ull) FP_SPIN(); \
        asm volatile("" ::: "memory"); } while (0)
#define XST(i) do { } while (0)
#define NOSTAT 0
#define ROT (pi + pj) % 9
#define CPRE 2
constexpr float GC1 = -2.0f * 0.7978845608028654f * 1.4426950408889634f, GC2 = GC1 * 0.044715f;
#define CPRIO 2
#define CPFW 2
#define T1W 1
#define KPF 1
#define GS 72
#define WBASE 0
#define WALK 2
#define WPF 4
#define CVD 0
#define YIS 7
#define YCK 8
#define PSTO(x) (x)
#define PSL 2
#define XIS 8
#define LPRIO 0
#define HPF 1
#define CVL 47
#define VPRIO 1
#define RGW 4
#define OAUX 0
#define TE 0
#define KXC 1
#define KC ((b >> 3) & 7)
#define KR ((b & 7) * 4 + (b >> 6))
#define KVE 1
#define HKE 17
#define FE 2
#define GCS 84
#define GPRIO 3
#define A21 1
#define KPRIO 0
#define KS1 1
#define KIL 0
#define WOS 0
#define ZD 6

__global__ __launch_bounds__(512, 2) void mega(const u16* __restrict__ x, const u16* __restrict__ kin, const u16* __restrict__ vin, const u16* __restrict__ shk,
                                               const u16* __restrict__ hk, const u16* __restrict__ dv, const u16* __restrict__ w_in, const u16* __restrict__ w_sp,
                                               const u16* __restrict__ b_sp, const u16* __restrict__ w_out, char* __restrict__ ws, u16* __restrict__ out) {
    extern __shared__ __attribute__((aligned(16))) char smem_[];
    LAS char* lds = (LAS char*)smem_;
    asm volatile(".Lmega_head:");
    const unsigned L0 = (unsigned)(size_t)lds;
    const int b = blockIdx.x, xcd = b & 7, r = b >> 3;
    const int pi = (xcd & 1) * 8 + (r & 7), pj = (xcd >> 1) * 4 + (r >> 3);
    const int tid = threadIdx.x, wid = RFL(tid >> 6), lane = tid & 63;
    const int fr = lane & 15, g = lane >> 4, q4 = (lane >> 2) & 3, p4 = lane & 3;
    const __amdgpu_buffer_rsrc_t rs = ws_rsrc(ws);
    const u32x4 wr = mk_rsrc(ws, WS_END);
    const f32x4 zero4 = {0.f, 0.f, 0.f, 0.f};
    STAMP(0);
#define TAILS() const int b_ = opqs(b), wid_ = opqs(wid), rg = (b_ & 1) * 16 + ((b_ & 7) >> 1) * 4 + ((b_ >> 3) & 3), ct = b_ >> 5; \
    const int nb3 = (9 * ct) >> 1, n30 = nb3 + (ct & 1), n34 = (ct & 1) ? nb3 : nb3 + 4; \
    const int redo = (int)(((wid_ < 4 ? redtab(0) : redtab(4)) >> (16 * (wid_ & 3))) & 0xffff) << 8
#define KX 0
#define TAILS2() const int kx = (KX) * 4, kw_ = wid_ ^ kx; const unsigned sA = WS_P + (4 * rg * 72 + 9 * kw_) * 1024, sB = WS_WT + (9 * kw_ * 36 + n30) * 1024, sB4 = WS_WT + (9 * kw_ * 36 + n34) * 1024, \
    sF = WS_FP + (rg >> 1) * 512 + kw_ * 64 + (rg & 1) * 8
    u64 tag;
#define TAG_READY() do { asm volatile("s_load_dwordx2 %0, %1, 0x0\n s_waitcnt lgkmcnt(0)" : "=s"(tag) : "s"(ws + WS_E) : "memory"); tag += TAG_INC; } while (0)
#define WAIT_LB0() __builtin_amdgcn_s_waitcnt(0xC07F)
#define RD(p) (*(LAS bf16x8*)(p))
#define RDP(p, o) (*(LAS bf16x8*)(size_t)((p) + (o)))
#define TRF(p) cat8(trread(p), trread((p) + 2304))
#define TRP(p, o) TRF((LAS char*)(size_t)((p) + (o)))
#define ADV(p, d) asm("v_add_u32 %0, %1, %0" : "+v"(p) : "s"(d))
#define LDSW(o) (*(volatile LAS unsigned*)(lds + (o)))
#define FENCE() asm volatile("" ::: "memory")
#define SPIN(word, need) wcheck(0u, L0 + (word) + p4 * 4, need)
#define SGB(m, n) __builtin_amdgcn_sched_group_barrier(m, n, 0)
    const int w = wid & 3, l3 = lane >> 3;
    const unsigned ch = ((lane & 7) ^ (l3 >> 1)) * 16;
    const int bb = (q4 + 4 * (g & 1) + 16 * (g >> 1)) * 288 + p4 * 8;
    unsigned vA[4];
#define SET_VSP() do { const unsigned base = l3 * (S * 2) + ch; vA[0] = base; vA[1] = (base ^ 64) + 8 * S * 2 - 1024; vA[2] = base + 16 * S * 2 - 2048; vA[3] = (base ^ 64) + 24 * S * 2 - 3072; } while (0)
    unsigned vB[5];
#define SET_VB(i) do { const unsigned c = i < 4 ? (4 * w + i) * 64 + lane : 1024 + 32 * w + (lane & 31), rho = c / 18, cc = c - rho * 18; \
        const unsigned grow = (rho & ~12u) | ((rho & 4) << 1) | ((rho & 8) >> 1); vB[i] = grow * (F * 2) + cc * 16 - (i & 3) * 1024; } while (0)
    const unsigned lw = L0 + w * 4096, lh = L0 + 16384 + w * 512, w1 = pj * 288;
    const int rot = ROT;
#define KT(t) ((t) + rot >= 9 ? (t) + rot - 9 : (t) + rot)
#define DMA_W(dst, src) do { dma4v(rwi, lw + (dst), vB, w1 + (src)); dmah(rwi, lh + (dst), vB[4], w1 + (src)); } while (0)
#define DMA_W1(j) DMA_W(warea(j), KT(j) * (64 * F * 2))
    auto walk = [&](int i) {
        const int j = pj & 3, m = i ^ j, x = i - 1;
        const int q = (pi & ~3) | ((j + x + (x >= ((pi - j) & 3))) & 3);
        int e = (WALK & 2) && (m == 7 || m == 8) ? m ^ 15 : m;
        if (WALK & 4) { e += pj & 12; e = e >= 28 ? e - 24 : e >= 16 ? e - 12 : e; }
        return i == 0 ? pi : i < 4 ? q : pi ^ e;
    };
    u64 fpv;
    bf16x8 bq[9][5];
    bool hasconv = false;
    const int cd = w < 2 ? 2 * b + w : 512 + b;

    if (__builtin_expect(wid >= 4, 1)) {
        const u32x4 rx = mk_rsrc(x, SZ_X), rwi = mk_rsrc(w_in, D * F * 2);
        {
            const unsigned base = (8 * (l3 >> 2) + (l3 & 3)) * (D * 2) + ch;
            vA[0] = base; vA[1] = (base ^ 64) + 16 * D * 2 - 1024; vA[2] = (base ^ 64) + 4 * D * 2 - 2048; vA[3] = base + 20 * D * 2 - 3072;
        }
        const unsigned xs = (pi * 128 + 32 * w) * (D * 2);
        XST(26);
        dma4v(rx, lw, vA, xs + rot * 128);
        SCHED0();
#pragma unroll
        for (int i = 0; i < 5; ++i) {
            SET_VB(i);
            if (i < 4) dma1(i * 1024, rwi, lw + SL_A, vB[i], w1 + FH * 2 + rot * (64 * F * 2)); else dmah(rwi, lh + SL_A, vB[4], w1 + FH * 2 + rot * (64 * F * 2));
            SCHED0();
        }
        XST(27);
        TAG_READY();
        const int xi = (fr >> 2) & 1;
        const int n0 = (32 * w + 16 * xi + 4 * (fr >> 3) + (fr & 3)) * 128 + ((g ^ (2 * (fr >> 3) + ((fr >> 1) & 1)) ^ (4 * xi)) << 4), n1 = n0 ^ 64;
        const int c0 = bb + w * 64, c8 = bb + 256 + (w & 1) * 9216;
        bf16x8 ones = {0x3f80, 0x3f80, 0x3f80, 0x3f80, 0x3f80, 0x3f80, 0x3f80, 0x3f80};
        asm volatile("" : "+v"(ones));
        bf16x8 xf[9][4];
        const unsigned tag32 = (unsigned)tag;
        const unsigned go = g * 1024 + fr * 16, go2 = g == 0 ? 8192 + fr * 16 : 0x7fffff00u, tagx = g == 0 ? tag32 : 0u;
        const unsigned sr = WS_SR + pi * 12288 + w * 256, sc = WS_SC + pj * 12288 + w * 256;
#define COLSUM(base, dst) do { bf16x8 cf[5]; cf[0] = TRF((base) + c0); cf[1] = TRF((base) + c0 + 32); cf[2] = TRF((base) + c0 + 9216); cf[3] = TRF((base) + c0 + 9216 + 32); cf[4] = TRF((base) + c8); \
            const f32x4 s0_ = mfma16(ones, cf[2], mfma16(ones, cf[0], zero4)), s1_ = mfma16(ones, cf[3], mfma16(ones, cf[1], zero4)), s2_ = mfma16(ones, cf[4], zero4); \
            st16c(rs, g == 0 && pi < 9 ? fr * 16 : 0x7fffff00u, (dst) + pi * 1024, (u32x4){__float_as_uint(s0_[0]), __float_as_uint(s1_[0]), __float_as_uint(s2_[0]), tag32}); } while (0)
#define GATHER(pv, so) do { ALD("x4", pv[0], go, wr, so, " sc1"); ALD("x4", pv[1], go, wr, (so) + 4096, " sc1"); ALD("x4", pv[2], go2, wr, so, " sc1"); } while (0)
#define GBAD(pv) (__builtin_amdgcn_ballot_w64(((pv[0][3] ^ tag32) | (pv[1][3] ^ tag32) | (pv[2][3] ^ tagx)) != 0u) != 0ull)
#define GCHECK(pv, so) do { asm volatile("" : "+v"(pv[0]), "+v"(pv[1]), "+v"(pv[2])); \
            if (__builtin_expect(GBAD(pv), 0)) { unsigned sp_ = 0; \
                do { GATHER(pv, so); WAIT_V(0); asm volatile("" : "+v"(pv[0]), "+v"(pv[1]), "+v"(pv[2])); } while (GBAD(pv) && ++sp_ < SPIN_CAP); } } while (0)
#define CSUM(pv, k) adds(adds(__uint_as_float(pv[0][k]), __uint_as_float(pv[1][k])), __uint_as_float(pv[2][k]))
        u32x4 gr[3], gc[3];
        f32x2 lnc; float cs01, cs8;
        const u32x4 rsp = mk_rsrc(w_sp, S * S * 2);
        __builtin_amdgcn_s_setprio(3);
#pragma unroll
        for (int t = 0; t < 9; ++t) {
            SCHED0();
            if (t == 0) {
                asm volatile("" :: "s"(KT(1)), "s"(KT(2)), "s"(KT(3)), "s"(KT(4)), "s"(KT(5)), "s"(KT(6)), "s"(KT(7)), "s"(KT(8)), "v"(n0), "v"(c0), "v"(c8), "v"(go), "v"(go2));
                XST(28);
                dma4v(rx, lw + SLOT, vA, xs + KT(1) * 128);
                if (T1W) DMA_W(SLOT + SL_A, FH * 2 + KT(1) * (64 * F * 2));
            }
            WAIT_V(t == 0 ? (T1W ? 9 : 4) : t == 2 || t == 4 ? 10 : t == 3 ? 11 : t == 7 ? 15 : t == 8 ? 10 : 9);
            BAR();
            XST(40 + t);
            LAS char* sb = lds + (t & 3) * SLOT;
            if (t < 8) { xf[t][0] = RD(sb + n0); xf[t][1] = RD(sb + n1 + 1024); xf[t][2] = RD(sb + n1); xf[t][3] = RD(sb + n0 + 1024); }
            SCHED0();
            if (t == 0 && !T1W) DMA_W(SLOT + SL_A, FH * 2 + KT(1) * (64 * F * 2));
            if (t < 7) {
                dma4v(rx, lw + ((t + 2) & 3) * SLOT, vA, xs + KT(t + 2) * 128);
                DMA_W(((t + 2) & 3) * SLOT + SL_A, FH * 2 + KT(t + 2) * (64 * F * 2));
            }
            SCHED0();
            if (t == 1) COLSUM(lds + SL_A, sc);
            if (t == 2) {
                f32x4 rsm[2], rq[2];
#pragma unroll
                for (int m = 0; m < 2; ++m) {
                    rsm[m] = mfma16(ones, xf[0][2 + m], mfma16(ones, xf[0][m], zero4));
                    rq[m] = mfma16(xf[0][2 + m], xf[0][2 + m], mfma16(xf[0][m], xf[0][m], zero4));
                }
#define DIAG(q_) ((fr & 2) ? ((fr & 1) ? q_[3] : q_[2]) : ((fr & 1) ? q_[1] : q_[0]))
                st16c(rs, (fr >> 2) == g && pj < 9 ? fr * 16 : 0x7fffff00u, sr + pj * 1024, rec4(rsm[0][0], DIAG(rq[0]), rsm[1][0], DIAG(rq[1]), tag32));
            }
            SCHED0();
            if (t == 6) { GATHER(gr, sr); GATHER(gc, sc); }
            if (t == 7) { DMA_W1(0); DMA_W1(2); }
            if (t == 8) {
                GCHECK(gr, sr); GCHECK(gc, sc);
                lnc = rec_consts<3>(gr, 1.0f / D);
                cs01 = half_sum(CSUM(gc, 0), CSUM(gc, 1)); cs8 = CSUM(gc, 2); cs8 = half_sum(cs8, cs8);
                asm volatile("" : "+v"(lnc), "+v"(cs01), "+v"(cs8));
            }
            if (t < 8) asm volatile("" : "+v"(xf[t][0]), "+v"(xf[t][1]), "+v"(xf[t][2]), "+v"(xf[t][3]));
        }
        {
            LAS float* p = (LAS float*)(lds + L_ROWST + (16 * w + 8 * (lane >> 5) + (fr >> 1)) * 16 + (fr & 1) * 4);
            p[0] = lnc.x; p[2] = lnc.y;
            LAS float* c = (LAS float*)(lds + L_CS) + fr * 12;
            c[2 * w + (lane >> 5)] = cs01;
            c[8 + w] = cs8;
        }
        WAIT_L0();
        BAR();
        STAMP(2);
        DMA_W1(1); DMA_W1(3); DMA_W1(4); DMA_W1(5); DMA_W1(6);
#define PF_KV() pf4<0>(mk_rsrc(w < 2 ? kin : vin, KPF ? SZ_X : 0), L0 + L_DUMP, lane * (D * 2), KR * 64 * (D * 2) + KC * 144 + (w & 1) * 140)
#define PF_HK() pf4<0>(mk_rsrc(hk + cd * S, HPF && (w < 2 || (w == 2 && b < 64)) ? S * 2 : 0), L0 + L_DUMP, (opqv(tid) & 63) * 128, 0)
        if (KVE) PF_KV();
        if (HKE == 1) PF_HK();
        if (WPF & 4) pf4<0>(w == 0 ? rsp : mk_rsrc(w_sp, 0), L0 + L_DUMP, (lane >> 1) * (S * 2) + (lane & 1) * 128, (pi * 128 + 32 * (pj & 3)) * (S * 2) + pi * 256);
        if (WPF & 1) pf4<0>(rsp, L0 + L_DUMP, (lane >> 1) * (S * 2) + (lane & 1) * 128, (pi * 128 + 32 * w) * (S * 2) + (pi ^ pj) * 256);
        if (WPF & 2) pf4<0>(rsp, L0 + L_DUMP, (lane >> 1) * (S * 2) + (lane & 1) * 128, (pi * 128 + 32 * w) * (S * 2) + (pi ^ (pj & 3)) * 256);
#define NPF ((WPF & 1) + (WPF >> 1 & 1) + (WPF >> 2 & 1) + KVE + (HKE == 1))
            unsigned vo[3];
            const int kap = opqs(b) / 3, th = b - kap * 3, kq = kap / 9, ke = kap - kq * 9, pr = ke & 3;
            const unsigned wos = th * 384 + w * 96 + (ke == 8 ? 18 * kq + 4 : 18 * kq + (ke >> 2) * 9 + 2 * pr + (pr >> 1)) * (16 * D * 2);
#pragma unroll
            for (int i = 0; i < 3; ++i) { const unsigned c = i * 64 + lane, rr = c / 6; vo[i] = (ke == 8 ? (rr >> 3 & 1) * 144 + (rr & 4) * 2 + (rr & 3) + (rr >> 4) * 4 : rr) * (D * 2) + (c - rr * 6) * 16 - i * 1024 + wos; }
            asm volatile("" : "+v"(vo[0]), "+v"(vo[1]), "+v"(vo[2]));
        WAIT_V(15 + NPF);
        LDSW(M_FL + w * 4) = 1u;
        XST(50);
        SPIN(M_FL, 1);
        XST(49);
        COLSUM(lds + warea(0), sc + 16 * 12288);
        st16c(rs, w == 3 && pj == 0 ? lane * 16 : 0x7fffff00u, WS_ST1 + pi * 1024, *(LAS u32x4*)(lds + L_ROWST + lane * 16));
        f32x4 az[2][9];
        unsigned rho = walk(lane >> 2);
        asm volatile("" : "+v"(rho));
        u64 fzv[2];
        const unsigned as0 = (pi * 128 + 32 * w) * (S * 2), zs0 = WS_Z2N + pj * 32 * SL_B + w * 4096;
        unsigned vl, fz;
#define KIDX(t) (2 * __builtin_amdgcn_readlane(rho, 4 * (((t) - 18) >> 1)) + ((t) & 1))
        auto stageA = [&](int t) { dma4v(rsp, lw + (t & 3) * SLOT, vA, as0 + KIDX(t) * 128); };
        auto stageB = [&](int t) {
            const unsigned zs = zs0 + KIDX(t) * SL_B;
            dma16c4<SL_A, 0>(wr, lw + (t & 3) * SLOT, vl, zs);
            dmahc(wr, lh + (t & 3) * SLOT + SL_A, vl, zs + 16384 - w * 3584);
        };
        {
            bf16x8 wf[162];
            unsigned fw;
            unsigned wb = L0 + bb;
#define WF_B(j) ((j) < 2 || (j) >= 6 ? 0 : (j) < 5 ? warea(2) : warea(5))
#define WF_P(s) ((LAS char*)(size_t)(wb + (warea((s) / 18) - WF_B((s) / 18) + ((s) % 9) * 32 + (((s) / 9) & 1) * 9216)))
#pragma unroll
            for (int s = 0; s < ZD; ++s) wf[s] = TRF(WF_P(s));
            SCHED0();
            XST(51);
            __builtin_amdgcn_s_setprio(0);
            wcheck(0u, L0 + M_RC + w * 4, 1);
            STAMP(3);
            SFOR(s, 0, 162)
                constexpr int j = s / 18, ks = (s / 9) & 1, n = s % 9;
                if (s == 72 - ZD - 4) fw = LDSW(M_FL + p4 * 4);
                if (s == 72 - ZD) wcheck(fw, L0 + M_FL + p4 * 4, 2);
                if (s == 126 - ZD - 4) fw = LDSW(M_RC + p4 * 4);
                if (s == 126 - ZD) wcheck(fw, L0 + M_RC + p4 * 4, 2);
                if (s == 18) { xf[8][0] = RD(lds + n0); xf[8][1] = RD(lds + n1 + 1024); }
                if (s == 19) { xf[8][2] = RD(lds + n1); xf[8][3] = RD(lds + n0 + 1024); }
                if (s == 25) asm volatile("" : "+v"(xf[8][0]), "+v"(xf[8][1]), "+v"(xf[8][2]), "+v"(xf[8][3]));
                if (s == 18 || s == 36 || s == 108 || s == 126) LDSW(M_RL + w * 4) = j;
                if constexpr (s == GCS) GATHER(gc, sc + 16 * 12288);
                if (s == 40) { WAIT_V(2 + NPF); LDSW(M_FL + w * 4) = 2u; }
                if (s == 140) { vl = (opqv(tid) & 63) * 16; fz = WS_FZ + pj * 512 + rho * 32 + ((vl >> 1) & 24); asm volatile("" : "+v"(fz), "+v"(vl)); }
                if (s == 146) { const unsigned l3 = vl >> 7, ch = ((vl >> 4 & 7) ^ (l3 >> 1)) * 16; vA[0] = l3 * (S * 2) + ch; asm volatile("" : "+v"(vA[0])); }
                if (s == 150) { vA[1] = (vA[0] ^ 64) + 8 * S * 2 - 1024; vA[2] = vA[0] + 16 * S * 2 - 2048; vA[3] = vA[1] + 16 * S * 2 - 2048; asm volatile("" : "+v"(vA[1]), "+v"(vA[2]), "+v"(vA[3])); }
                if (s == 18) XST(56); if (s == 42) XST(57); if (s == 54) XST(58); if (s == 107) XST(59); if (s == 109) XST(60); if (s == 121) XST(61); if (s == 133) XST(62); if (s == 135) XST(63);
                SCHED0();
                if (s < 9) asm volatile("v_mfma_f32_16x16x32_bf16 %0, %1, %2, 0" : "=&v"(az[0][n]) : "v"(wf[s]), "v"(xf[j][2 * ks]));
                else asm volatile("v_mfma_f32_16x16x32_bf16 %0, %1, %2, %0" : "+v"(az[0][n]) : "v"(wf[s]), "v"(xf[j][2 * ks]));
                SCHED0();
                bf16x4 h0, h1;
                if constexpr ((s + ZD) % 18 == 0 && s + ZD < 162 && WF_B((s + ZD) / 18) != WF_B((s + ZD) / 18 - 1)) { unsigned& wb_ = wb; asm volatile("v_add_u32 %0, %1, %0" : "+v"(wb_) : "n"(WF_B((s + ZD) / 18) - WF_B((s + ZD) / 18 - 1))); }
                if constexpr (s + ZD < 162) h0 = trread(WF_P(s + ZD));
                SCHED0();
                if (s < 9) asm volatile("v_mfma_f32_16x16x32_bf16 %0, %1, %2, 0" : "=&v"(az[1][n]) : "v"(wf[s]), "v"(xf[j][2 * ks + 1]));
                else asm volatile("v_mfma_f32_16x16x32_bf16 %0, %1, %2, %0" : "+v"(az[1][n]) : "v"(wf[s]), "v"(xf[j][2 * ks + 1]));
                SCHED0();
                if constexpr (s + ZD < 162) { h1 = trread(WF_P(s + ZD) + 2304); wf[s + ZD] = cat8(h0, h1); }
                SCHED0();
            SEND
        }
        __builtin_amdgcn_s_setprio(GPRIO);
        LDSW(M_RL + w * 4) = 9u;
        STAMP(4);
        {
        const int lane = vl >> 4, fr = lane & 15, g = lane >> 4, q4 = (lane >> 2) & 3, p4 = lane & 3, l3 = lane >> 3;
        u64 miss;
#define NEEDZ(mask) do { if ((miss & (mask)) != 0ull) { unsigned sp_ = 0; \
            do { miss = __builtin_amdgcn_ballot_w64(ldflag(rs, fz) != tag); } while ((miss & (mask)) != 0ull && ++sp_ < SPIN_CAP); } FENCE(); } while (0)
        u32x4 zp[9];
        hasconv = w < 2 || (w == 2 && b < 64);
        const unsigned fk = WS_FK + w * 512 + lane * 8;
        {
            float nmb[2], k1[2], k2[2];
#pragma unroll
            for (int m = 0; m < 2; ++m) {
                LAS float* p = (LAS float*)(lds + L_ROWST + (16 * w + 8 * m + (fr >> 1)) * 16 + (fr & 1) * 4);
                const float rd = p[2];
                nmb[m] = -p[0]; k1[m] = muls(rd, GC1); k2[m] = muls(muls(rd, rd), muls(rd, GC2));
            }
            u32x4 kl[3];
            WAIT_V(0);
            GCHECK(gc, sc + 16 * 12288);
            *(LAS float*)(lds + L_CS + ((2 * w + (lane >> 5)) * 16 + fr) * 4) = half_sum(CSUM(gc, 0), CSUM(gc, 1));
            cs8 = CSUM(gc, 2);
            *(LAS float*)(lds + L_CS + ((8 + w) * 16 + fr) * 4) = half_sum(cs8, cs8);
            asm volatile("" : "+v"(k1[0]), "+v"(k1[1]), "+v"(k2[0]), "+v"(k2[1]), "+v"(nmb[0]), "+v"(nmb[1]));
#define WTS (WOS > 4 ? WOS + 4 : 8)
#define WOLD() do { _Pragma("unroll") for (int i = 0; i < 3; ++i) dma1n(i * 1024, mk_rsrc(w_out, b < 216 ? FH * D * 2 : 0), L0 + L_WSCR + w * 3072, vo[i], 0); } while (0)
            SPIN(M_RL, 9);
            SPIN(M_RS, 1);
            STAMP(5);
            if (A21) { stageA(20); stageA(21); }
            if (WOS == -2) WOLD();
            miss = ~0ull;
            NEEDZ(0xf0ull);
            stageB(20);
            if (!A21) { stageA(20); stageA(21); }
            if (WOS == -1) WOLD();
            BAR();
            f32x4 cc = *(LAS f32x4*)(lds + L_CS + g * 16);
            kl[0] = *(LAS u32x4*)(lds + KVS + w * 1024 + vl); kl[1] = *(LAS u32x4*)(lds + KVS + 4096 + w * 1024 + vl); kl[2] = *(LAS u32x4*)(lds + KVS + 8192 + vl);
            asm volatile("" : "+v"(kl[0]), "+v"(kl[1]), "+v"(kl[2]));
#define XE(u) (((u) == 6 ? 2 : (u) == 7) + ((u) == WOS ? 3 : (u) >= WTS && (u) < WTS + 3) + ((u) == HKE))
#define NW(u) ((u) == 0 ? 63 : (u) < 5 ? 9 + 3 * ((u) == WOS + 1) : (u) == 5 ? 14 : 18 + XE((u) - 1) + XE((u) - 2) + XE((u) - 3))
            SFOR(u, 0, 19)
                constexpr int n = 19 + u, t = n + 3, tb = u < 4 ? t - 1 : t;
                const unsigned la = lw + (t & 3) * SLOT, lb = lw + (tb & 3) * SLOT + SL_A, as = as0 + KIDX(t) * 128, zs = zs0 + KIDX(tb) * SL_B;
                SCHED0();
                asm volatile("s_waitcnt vmcnt(%0)\n s_barrier" :: "n"(NW(u)), "s"(la), "s"(as), "s"(zs), "v"(cc) : "memory");
                if (u >= 1 && u < 5) { u64 nm_; asm volatile("v_cmp_ne_u64 %0, %1, %2" : "=s"(nm_) : "s"(tag), "v"(fzv[u & 1]) : "memory"); miss &= nm_; }
                if (u < 4) FLAG_ISSUE(fzv[(u + 1) & 1], fz);
                if (u == 4) { NEEDZ(~0ull); stageB(25); }
                if (u == WTS + 6 && w == 3 && lane == 0) stflag(ws + WS_FK + b * 8, tag);
                if (u == 16 && b == 0 && w == 3 && lane == 0) stflag(ws + WS_E, tag);
                if constexpr (u == 0) {
                    stageA(22);
                    stageB(21);
                    if (WOS == 0) WOLD();
                } else {
                constexpr int q = u - 1, m = q & 1, nn = q >> 1;
                bf16x4 wt0, wt1;
                if (u >= WTS && u < WTS + 3) { LAS char* p = lds + L_WSCR + w * 3072 + (4 * g + q4) * 96 + (u - WTS) * 32 + p4 * 8; wt0 = trread(p); wt1 = trread(p + 1536); }
                f32x4 a = az[m][nn];
                float u0, u1, u2, u3;
                asm volatile("s_mov_b32 m0, %15\n v_fma_f32 %4, %8, %12, %4\n buffer_load_dwordx4 %16, %20, %21 offen lds\n"
                             "v_fma_f32 %5, %9, %12, %5\n v_fma_f32 %6, %10, %12, %6\n v_fma_f32 %7, %11, %12, %7\n"
                             "buffer_load_dwordx4 %17, %20, %21 offen offset:1024 lds\n"
                             "v_mul_f32 %0, %4, %4\n v_mul_f32 %1, %5, %5\n v_mul_f32 %2, %6, %6\n v_mul_f32 %3, %7, %7\n"
                             "buffer_load_dwordx4 %18, %20, %21 offen offset:2048 lds\n"
                             "v_fma_f32 %0, %0, %14, %13\n v_fma_f32 %1, %1, %14, %13\n v_fma_f32 %2, %2, %14, %13\n v_fma_f32 %3, %3, %14, %13\n"
                             "buffer_load_dwordx4 %19, %20, %21 offen offset:3072 lds\n"
                             "v_mul_f32 %0, %0, %4\n v_mul_f32 %1, %1, %5\n v_mul_f32 %2, %2, %6\n v_mul_f32 %3, %3, %7"
                             : "=&v"(u0), "=&v"(u1), "=&v"(u2), "=&v"(u3), "+v"(a[0]), "+v"(a[1]), "+v"(a[2]), "+v"(a[3])
                             : "v"(cc[0]), "v"(cc[1]), "v"(cc[2]), "v"(cc[3]), "v"(nmb[m]), "v"(k1[m]), "v"(k2[m]),
                               "s"(la), "v"(vA[0]), "v"(vA[1]), "v"(vA[2]), "v"(vA[3]), "s"(rsp), "s"(as) : "memory");
                f32x4 c9;
                if (m == 1 && nn < 8) cc = *(LAS f32x4*)(lds + L_CS + (nn + 1) * 64 + g * 16);
                if (q == 15) c9 = *(LAS f32x4*)(lds + L_CS + 9 * 64 + g * 16);
                if (u < 4) NEEDZ(3ull << (2 * (tb - 18)));
                asm volatile("s_mov_b32 m0, %8\n v_exp_f32 %0, %0\n buffer_load_dwordx4 %9, %10, %11 offen sc1 lds\n"
                             "v_exp_f32 %1, %1\n v_exp_f32 %2, %2\n v_exp_f32 %3, %3\n"
                             "buffer_load_dwordx4 %9, %10, %11 offen offset:1024 sc1 lds\n"
                             "v_add_f32 %0, 1.0, %0\n v_add_f32 %1, 1.0, %1\n v_add_f32 %2, 1.0, %2\n v_add_f32 %3, 1.0, %3\n"
                             "buffer_load_dwordx4 %9, %10, %11 offen offset:2048 sc1 lds\n"
                             "v_rcp_f32 %0, %0\n v_rcp_f32 %1, %1\n v_rcp_f32 %2, %2\n v_rcp_f32 %3, %3\n"
                             "buffer_load_dwordx4 %9, %10, %11 offen offset:3072 sc1 lds\n"
                             "v_mul_f32 %0, %0, %4\n v_mul_f32 %1, %1, %5\n v_mul_f32 %2, %2, %6\n v_mul_f32 %3, %3, %7\n"
                             "s_mov_b32 m0, %12\n s_mov_b32 exec_hi, 0\n buffer_load_dwordx4 %9, %10, %13 offen sc1 lds\n s_mov_b32 exec_hi, -1\n"
                             "v_cvt_pk_bf16_f32 %0, %0, %1\n v_cvt_pk_bf16_f32 %1, %2, %3"
                             : "+v"(u0), "+v"(u1), "+v"(u2), "+v"(u3)
                             : "v"(a[0]), "v"(a[1]), "v"(a[2]), "v"(a[3]), "s"(lb), "v"(vl), "s"(wr), "s"(zs), "s"(lh + (tb & 3) * SLOT + SL_A), "s"(zs + 16384 - w * 3584) : "memory");
                if (u == WOS) WOLD();
                if (u >= WTS && u < WTS + 3) st16c(rs, b < 216 ? vl : 0x7fffff00u, WS_WT + (b * 12 + w * 3 + u - WTS) * 1024, __builtin_bit_cast(u32x4, cat8(wt0, wt1)));
                if constexpr (u == HKE && u >= 6) PF_HK();
                if (u == 6 || u == 7) {
                    const unsigned so = WS_KVT + (KC * 72 * S + KR * 64) * 2, ko_ = l3 * (S * 2) + ((lane - (l3 >> 2) - 2 * w) & 7) * 16;
                    if (u == 6) st16c(rs, ko_, so + w * (8 * S * 2), kl[0]);
                    if (u == 6) st16c(rs, ko_, so + (w + 4) * (8 * S * 2), kl[1]);
                    if (u == 7) st16c(rs, w == 0 ? ko_ : 0x7fffff00u, so + 64 * S * 2, kl[2]);
                }
                if (q == 15) { cc[0] = adds(cc[0], c9[0]); cc[1] = adds(cc[1], c9[1]); cc[2] = adds(cc[2], c9[2]); cc[3] = adds(cc[3], c9[3]); }
                const int n4 = nn - (nn > 4), zq = nn == 4 ? 8 : m * 4 + (n4 >> 1), h = nn == 4 ? m : n4 & 1;
                zp[zq][h * 2] = __float_as_uint(u0); zp[zq][h * 2 + 1] = __float_as_uint(u1);
                asm volatile("" : "+v"(zp[zq]));
                }
            SEND
        }
        SCHED0();
        STAMP(6);
#define TST(i) do { } while (0)
#pragma unroll 1
        for (int n = 38; n < 42; ++n) {
            WAIT_V(18);
            BAR();
            stageA(n + 3); stageB(n + 3);
        }
        FLAG_ISSUE(fzv[0], fk);
#pragma unroll 1
        for (int n = 42; n < 44; ++n) {
            WAIT_V(19);
            BAR();
            stageA(n + 3); stageB(n + 3);
        }
        WAIT_V(18);
        asm volatile("" : "+v"(fzv[0]));
        if (!allset(fzv[0], tag)) spin_flags(rs, fk, tag);
        FENCE();
        TAILS(); TAILS2();
        ConvIn cin;
        BAR();
        TST(44);
        stageA(47); stageB(47);
        WAIT_V(18);
        BAR();
        TST(45);
        stageA(48); stageB(48);
        WAIT_V(18);
        BAR();
        TST(46);
        stageA(49); stageB(49);
        WAIT_V(18);
        BAR();
        TST(47);
        if (hasconv) conv_load(cin, hk, dv, wr, cd, lane); else G3_LDB(0);
        SCHED0();
        TST(49);
#pragma unroll
        for (int q = 0; q < 8; ++q) *(LAS u32x4*)(lds + ZS + w * 8192 + q * 1024 + lane * 16) = zp[q];
        *(LAS u32x4*)(lds + Z8OFF(w) + lane * 16) = zp[8];
        SCHED0();
        TST(50);
        if (hasconv) WAIT_V(12); else WAIT_V(5);
        WAIT_L0();
        TST(51);
        BAR();
        TST(48);
        if (hasconv) {
            __builtin_amdgcn_s_setprio(VPRIO);
            G3_LDB(0);
            SCHED0();
            WAIT_V(5);
            CONV_PIN(cin);
            if (w == 2) SPIN(M_RC, 3);
            STAMP(7);
            conv_stage(cin, lds + redo, lane, [&](auto i_) { G3_LDB(decltype(i_)::v + 1); });
            __builtin_amdgcn_s_setprio(0);
        } else {
            __builtin_amdgcn_s_setprio(0);
            G3_LDB(1); G3_LDB(2); G3_LDB(3); G3_LDB(4);
        }
        SCHED0();
        STAMP(8);
        if (hasconv) {
            if (CVD) wcheck(0u, L0 + M_RC + w * 4, 5);
            conv_run(cin.dd, rs, lds + redo, cd, lane);
            STAMP(9);
            FP_ISSUE(); G3_DMA1(redo, sB, sB4);
            WAIT_V(9);
            if (lane == 0) stflag(ws + WS_FY + cd * 8, tag);
            XST(29);
            G3_DMA2(redo, sB, sB4);
            STAMP(20);
            FP_CHECK(19);
        } else {
            SPIN(M_RC, RGW);
#pragma unroll 1
            for (int v = 5 - w; v < (b < 64 ? 4 : 6 - w); ++v) G3_DMA(redoff(0) + v * RED_W, sB + (v - 4 - w + 2 * kx) * (9 * KST), sB4 + (v - 4 - w + 2 * kx) * (9 * KST));
            G3_DMA(redo, sB, sB4);
            WAIT_V(18);
            LDSW(M_FL + w * 4) = 4u;
            for (int i_ = 0; i_ < PSL; ++i_) __builtin_amdgcn_s_sleep(16);
            STAMP(20);
            FP_ISSUE(); FP_CHECK(0);
        }
        }
    } else {
        if (tid < 16) *(LAS unsigned*)(lds + L_MISC + tid * 4) = 0u;
        {
            const char* pc; unsigned len, len2, back;
            asm volatile("s_getpc_b64 %0\n.Lpf_here%=:\n s_mov_b32 %1, .Lmega_tail+1024-.Lmega_head\n s_mov_b32 %2, .Lpf_here%=-.Lmega_head\n s_mov_b32 %3, .Lmega_cend-.Lmega_head" : "=s"(pc), "=s"(len), "=s"(back), "=s"(len2));
            const u32x4 rc = mk_rsrc(pc - back, len > len2 ? len : len2);
            if (wid < CPFW) pf4<0>(rc, L0 + L_DUMP, ((r * CPFW + wid) & 7) * 8192 + lane * 128, 0);
        }
        u32x4 zp[9];
        unsigned bsw[4];
        const int wm = w & 1, wn = w >> 1;
#pragma unroll
        for (int m = 0; m < 4; ++m) asm volatile("buffer_load_ushort %0, %1, %2, %3 offen" : "=v"(bsw[m]) : "v"(fr * 2), "s"(mk_rsrc(b_sp, S * 2)), "s"((pi * 128 + 64 * wm + 16 * ((m + 2 * wn) & 3)) * 2) : "memory");
#pragma unroll
        for (int i = 0; i < 5; ++i) SET_VB(i);
        SET_VSP();
        const u32x4 rsp = mk_rsrc(w_sp, S * S * 2);
        const unsigned as0 = (pi * 128 + 32 * w) * (S * 2);
        asm volatile("" : "+v"(vB[0]), "+v"(vB[1]), "+v"(vB[2]), "+v"(vB[3]), "+v"(vB[4]), "+v"(vA[0]), "+v"(vA[1]), "+v"(vA[2]), "+v"(vA[3]));
        unsigned ok[3], ov[3], kd8, kvo, krd;
        {
#pragma unroll
            for (int i = 0; i < 3; ++i) {
                const unsigned c = i * 64 + lane, rr = c / 9, o = (KR * 64 + 16 * w + rr) * (D * 2) + KC * 144 + (c - rr * 9) * 16 - i * 1024;
                ok[i] = i == 2 && rr >= 19 ? 0x7fffff00u : o - 3 * (D * 2);
                ov[i] = i == 2 && rr >= 16 ? 0x7fffff00u : o;
            }
            const int ln = lane < 36 ? lane : 35, sg = ln >= 18, kd = ln - sg * 18;
            kvo = L0 + KVS + kd * 512 + ((2 * w + sg + kd) & 7) * 16; kd8 = kd * 32 + KC * 576; krd = sg * 1152 + kd * 8;
            asm volatile("" : "+v"(ok[0]), "+v"(ok[1]), "+v"(ok[2]), "+v"(ov[0]), "+v"(ov[1]), "+v"(ov[2]), "+v"(kd8), "+v"(kvo), "+v"(krd));
        }
        u32x4 sk[2];
        {
            f32x4 az[2][9];
#pragma unroll
            for (int n = 0; n < 9; ++n) az[0][n] = az[1][n] = zero4;
            unsigned e0 = L0 + (32 * w + fr) * 128 + ((g ^ (fr >> 1)) << 4), e1 = e0 ^ 64, pw = L0 + SL_A + bb;
            bf16x8 ax[2], bx[9], ay[2], by[9];
#define ZRD_X() do { ax[0] = RDP(e0, 0); ax[1] = RDP(e1, 2048); _Pragma("unroll") for (int n = 0; n < 9; ++n) bx[n] = TRP(pw, n * 32); } while (0)
#define ZRD_Y() do { ay[0] = RDP(e1, 0); ay[1] = RDP(e0, 2048); _Pragma("unroll") for (int n = 0; n < 9; ++n) by[n] = TRP(pw, n * 32 + 9216); } while (0)
#define ZMM(A, B, n0_, n1_) do { _Pragma("unroll") for (int n = n0_; n < n1_; ++n) { az[0][n] = mfma16(A[0], B[n], az[0][n]); az[1][n] = mfma16(A[1], B[n], az[1][n]); } } while (0)
#define ZSCHED(rest) do { _Pragma("unroll") for (int i_ = 0; i_ < 6; ++i_) { SGB(0x008, 1); SGB(0x100, 2); } _Pragma("unroll") for (int i_ = 0; i_ < 8; ++i_) { SGB(0x008, 1); SGB(0x100, 1); } if (rest) SGB(0x008, rest); SCHED0(); } while (0)
            XST(26);
            TAG_READY();
            XST(27);
            __builtin_amdgcn_s_setprio(CPRIO);
            BAR();
            STAMP(1);
            ZRD_X();
            WAIT_LB0();
#pragma unroll 1
            for (int t = 0; t < 8; ++t) {
                const int dl = (t & 3) == 3 ? -3 * SLOT : SLOT;
                ZRD_Y();
                ZMM(ax, bx, 0, 7);
                ZSCHED(0);
                ADV(e0, dl); ADV(e1, dl); ADV(pw, dl);
                ZMM(ax, bx, 7, 9); ZMM(ay, by, 0, 2);
                SCHED0();
                WAIT_LB0();
                BAR();
                ZRD_X();
                ZMM(ay, by, 2, 9);
                ZSCHED(0);
            }
            ZRD_Y();
            ZMM(ax, bx, 0, 7);
            ZSCHED(0);
            ZMM(ax, bx, 7, 9); ZMM(ay, by, 0, 2);
            SCHED0();
            WAIT_LB0();
            BAR();
            f32x4 st[4], cq[3];
#pragma unroll
            for (int j = 0; j < 4; ++j) st[j] = *(LAS f32x4*)(lds + L_ROWST + (16 * w + 4 * g + j) * 16);
#pragma unroll
            for (int j = 0; j < 3; ++j) cq[j] = *(LAS f32x4*)(lds + L_CS + fr * 48 + j * 16);
            ZMM(ay, by, 2, 9);
            SCHED_IL(7, 14);
            STAMP(2);
            f32x2 sum[4], sq[4], k1[4], k2[4];
            cq[2][0] += cq[2][1];
#pragma unroll
            for (int j = 0; j < 4; ++j) { const f32x2 rd = hi2(st[j]); k1[j] = rd * GC1; k2[j] = rd * rd * rd * GC2; }
#pragma unroll
            for (int n = 0; n < 9; ++n) {
                const float cc = -((n & 1) ? __uint_as_float(movs(__float_as_uint(cq[n >> 2][n & 3]))) : cq[n >> 2][n & 3]);
#pragma unroll
                for (int j = 0; j < 4; ++j) {
                    f32x4& a = az[j >> 1][n];
                    const f32x2 v = fma2(lo2(st[j]), (f32x2){cc, cc}, (j & 1) ? hi2(a) : lo2(a));
                    const f32x2 e = v * fma2(v * v, k2[j], k1[j]);
                    const f32x2 d = (f32x2){__builtin_amdgcn_exp2f(e.x), __builtin_amdgcn_exp2f(e.y)} + (f32x2){1.0f, 1.0f};
                    const f32x2 z = v * (f32x2){__builtin_amdgcn_rcpf(d.x), __builtin_amdgcn_rcpf(d.y)};
                    a[(j & 1) * 2] = z.x; a[(j & 1) * 2 + 1] = z.y;
                    if (n == 0) { sum[j] = z; sq[j] = z * z; } else { sum[j] += z; sq[j] = fma2(z, z, sq[j]); }
                }
            }
#pragma unroll
            for (int j = 0; j < 4; ++j) { const f32x2 rd = hi2(st[j]); sum[j] *= rd; sq[j] *= rd * rd; asm volatile("" : "+v"(sum[j]), "+v"(sq[j])); }
            SCHED0();
            LDSW(M_RC + w * 4) = 1u;
            SCHED0();
            float r0 = sum[0].x, r1 = sq[0].x, r2 = sum[0].y, r3 = sq[0].y, r4 = sum[1].x, r5 = sq[1].x, r6 = sum[1].y, r7 = sq[1].y;
            asm volatile(
                "s_nop 1\n"
                "v_add_f32_dpp %0, %0, %0 row_mirror row_mask:0xf bank_mask:0x3\n v_add_f32_dpp %1, %1, %1 row_mirror row_mask:0xf bank_mask:0x3\n"
                "v_add_f32_dpp %2, %2, %2 row_mirror row_mask:0xf bank_mask:0x3\n v_add_f32_dpp %3, %3, %3 row_mirror row_mask:0xf bank_mask:0x3\n"
                "v_add_f32_dpp %4, %4, %4 row_mirror row_mask:0xf bank_mask:0x3\n v_add_f32_dpp %5, %5, %5 row_mirror row_mask:0xf bank_mask:0x3\n"
                "v_add_f32_dpp %6, %6, %6 row_mirror row_mask:0xf bank_mask:0x3\n v_add_f32_dpp %7, %7, %7 row_mirror row_mask:0xf bank_mask:0x3\n"
                "v_add_f32_dpp %0, %8, %8 row_mirror row_mask:0xf bank_mask:0xc\n v_add_f32_dpp %1, %9, %9 row_mirror row_mask:0xf bank_mask:0xc\n"
                "v_add_f32_dpp %2, %10, %10 row_mirror row_mask:0xf bank_mask:0xc\n v_add_f32_dpp %3, %11, %11 row_mirror row_mask:0xf bank_mask:0xc\n"
                "v_add_f32_dpp %4, %12, %12 row_mirror row_mask:0xf bank_mask:0xc\n v_add_f32_dpp %5, %13, %13 row_mirror row_mask:0xf bank_mask:0xc\n"
                "v_add_f32_dpp %6, %14, %14 row_mirror row_mask:0xf bank_mask:0xc\n v_add_f32_dpp %7, %15, %15 row_mirror row_mask:0xf bank_mask:0xc\n"
                "v_add_f32_dpp %0, %0, %0 row_half_mirror row_mask:0xf bank_mask:0x5\n v_add_f32_dpp %1, %1, %1 row_half_mirror row_mask:0xf bank_mask:0x5\n"
                "v_add_f32_dpp %2, %2, %2 row_half_mirror row_mask:0xf bank_mask:0x5\n v_add_f32_dpp %3, %3, %3 row_half_mirror row_mask:0xf bank_mask:0x5\n"
                "v_add_f32_dpp %0, %4, %4 row_half_mirror row_mask:0xf bank_mask:0xa\n v_add_f32_dpp %1, %5, %5 row_half_mirror row_mask:0xf bank_mask:0xa\n"
                "v_add_f32_dpp %2, %6, %6 row_half_mirror row_mask:0xf bank_mask:0xa\n v_add_f32_dpp %3, %7, %7 row_half_mirror row_mask:0xf bank_mask:0xa\n"
                "v_add_f32_dpp %0, %0, %0 quad_perm:[1,0,3,2] row_mask:0xf bank_mask:0xf\n v_add_f32_dpp %1, %1, %1 quad_perm:[1,0,3,2] row_mask:0xf bank_mask:0xf\n"
                "v_add_f32_dpp %2, %2, %2 quad_perm:[1,0,3,2] row_mask:0xf bank_mask:0xf\n v_add_f32_dpp %3, %3, %3 quad_perm:[1,0,3,2] row_mask:0xf bank_mask:0xf\n"
                "v_add_f32_dpp %0, %0, %0 quad_perm:[2,3,0,1] row_mask:0xf bank_mask:0xf\n v_add_f32_dpp %1, %1, %1 quad_perm:[2,3,0,1] row_mask:0xf bank_mask:0xf\n"
                "v_add_f32_dpp %2, %2, %2 quad_perm:[2,3,0,1] row_mask:0xf bank_mask:0xf\n v_add_f32_dpp %3, %3, %3 quad_perm:[2,3,0,1] row_mask:0xf bank_mask:0xf\n"
                : "+v"(r0), "+v"(r1), "+v"(r2), "+v"(r3), "+v"(r4), "+v"(r5), "+v"(r6), "+v"(r7)
                : "v"(sum[2].x), "v"(sq[2].x), "v"(sum[2].y), "v"(sq[2].y), "v"(sum[3].x), "v"(sq[3].x), "v"(sum[3].y), "v"(sq[3].y));
            const unsigned tag32 = (unsigned)tag, so2 = WS_ST2 + pi * 16384 + w * 256;
            st16c(rs, p4 == 0 ? lane * 4 : 0x7fffff00u, so2 + pj * 1024, rec4(r0, r1, r2, r3, tag32));
            SCHED0();
            STAMP(3);
            {
                u32x4 pv[4];
                const u32x4 rwi = mk_rsrc(w_in, D * F * 2);
                SPIN(M_RL, 1);
                DMA_W1(7);
#define KVLD() do { _Pragma("unroll") for (int i = 0; i < 3; ++i) dma1(i * 1024, mk_rsrc(vin, SZ_X), L0 + L_WSCR + w * 3072, ov[i], 0); \
                    asm volatile("buffer_load_dwordx4 %0, %2, %3, 0 offen\n buffer_load_dwordx4 %1, %2, %3, 0 offen offset:16" : "=&v"(sk[0]), "=&v"(sk[1]) : "v"(kd8), "s"(mk_rsrc(shk, D * 8)) : "memory"); } while (0)
#define KKLD() do { _Pragma("unroll") for (int i = 0; i < 3; ++i) dma1(i * 1024, mk_rsrc(kin, SZ_X), lw, ok[i], 0); } while (0)
                if (KS1 == 0) KVLD();
                SPIN(M_RL, 2);
                DMA_W1(8);
                if (KS1 == 0) KKLD();
                const unsigned po = g * 1024 + fr * 16;
#pragma unroll
                for (int i = 0; i < 4; ++i) ALD("x4", pv[i], po, wr, so2 + i * 4096, " sc1");
                if (KS1 == 1) { KVLD(); KKLD(); }
                WAIT_V(KS1 == 1 ? 8 : 0);
                LDSW(M_RC + w * 4) = 2u;
                unsigned spins = 0;
                for (;;) {
                    asm volatile("" : "+v"(pv[0]), "+v"(pv[1]), "+v"(pv[2]), "+v"(pv[3]));
                    const unsigned bad = ((pv[0][3] ^ tag32) | (pv[1][3] ^ tag32)) | ((pv[2][3] ^ tag32) | (pv[3][3] ^ tag32));
                    if (__builtin_amdgcn_ballot_w64(bad != 0u) == 0ull || ++spins > SPIN_CAP) break;
#pragma unroll
                    for (int i = 0; i < 4; ++i) ALD("x4", pv[i], po, wr, so2 + i * 4096, " sc1");
                    WAIT_V(0);
                }
                STAMP(5);
                const f32x2 c = rec_consts<4>(pv, 1.0f / FH);
                LAS float* p = (LAS float*)(lds + L_ZST + w * 256 + fr * 16 + (lane >> 5) * 4);
                p[0] = muls(c.y, *(LAS float*)(lds + L_ROWST + (16 * w + fr) * 16 + 8 + (lane >> 5) * 4)); p[2] = muls(-c.x, c.y);
            }
            FENCE();
            const unsigned zdst = WS_Z2N + ((pj * 32 + pi * 2 + (w >> 1)) * 18 + (w & 1)) * 1024;
#pragma unroll
            for (int j = 0; j < 4; ++j) st[j] = *(LAS f32x4*)(lds + L_ZST + w * 256 + (4 * g + j) * 16);
#pragma unroll
            for (int n = 0; n < 9; ++n) {
#pragma unroll
                for (int j = 0; j < 4; ++j) { const f32x4 a = az[j >> 1][n]; zp[n][j] = norm2s((j & 1) ? hi2(a) : lo2(a), st[j]); }
                st16c(rs, lane * 16, zdst + n * 2048, zp[n]);
                SCHED0();
            }
        }
        STAMP(6);
        {
            u32x2 kw[11], vw[8];
            WAIT_V(9);
            asm volatile("" : "+v"(sk[0]), "+v"(sk[1]));
#pragma unroll
            for (int i = 0; i < 11; ++i) kw[i] = *(LAS u32x2*)(lds + w * 4096 + krd + i * 144);
#pragma unroll
            for (int i = 0; i < 8; ++i) vw[i] = *(LAS u32x2*)(lds + L_WSCR + w * 3072 + krd + i * 144);
            SCHED0();
            SPIN(M_RL, 6);
#pragma unroll
            for (int n = 0; n < 9; ++n) *(LAS u32x4*)(lds + (w < 2 ? T18 : T19) + SL_A + n * 2048 + (w & 1) * 1024 + lane * 16) = zp[n];
            SCHED0();
#define HALF(w_, c) (((c) & 1) ? bfhi(w_) : bflo(w_))
#define KVC(c) do { SCHED0(); asm volatile("" : "+v"(sk[c >> 1])); \
                const float h[4] = {bflo(sk[c >> 1][(c & 1) * 2]), bfhi(sk[c >> 1][(c & 1) * 2]), bflo(sk[c >> 1][(c & 1) * 2 + 1]), bfhi(sk[c >> 1][(c & 1) * 2 + 1])}; \
                float kk[11], o[8]; \
                _Pragma("unroll") for (int i = 0; i < 11; ++i) kk[i] = HALF(kw[i][c >> 1], c); \
                _Pragma("unroll") for (int i = 0; i < 8; i += 2) tap4d(o[i], o[i + 1], kk + i, h, HALF(vw[i][c >> 1], c), HALF(vw[i + 1][c >> 1], c)); \
                *(LAS u32x4*)(size_t)(kvo + c * 128) = (u32x4){pk2(o[7], o[6]), pk2(o[5], o[4]), pk2(o[3], o[2]), pk2(o[1], o[0])}; SCHED0(); } while (0)
            asm volatile("" : "+v"(kw[0]), "+v"(kw[1]), "+v"(kw[2]), "+v"(kw[3]), "+v"(kw[4]), "+v"(kw[5]), "+v"(kw[6]), "+v"(kw[7]), "+v"(kw[8]), "+v"(kw[9]), "+v"(kw[10]));
            asm volatile("" : "+v"(vw[0]), "+v"(vw[1]), "+v"(vw[2]), "+v"(vw[3]), "+v"(vw[4]), "+v"(vw[5]), "+v"(vw[6]), "+v"(vw[7]));
            if (FE == 2) { dma4v(rsp, lw + T18, vA, as0 + pi * 256); dma4v(rsp, lw + T19, vA, as0 + pi * 256 + 128); }
            WAIT_V(FE == 2 ? 8 : 0);
            if (lane == 0) stflag(ws + WS_FZ + ((pj * 16 + pi) * 4 + w) * 8, tag);
            LDSW(M_RS + w * 4) = 1u;
            STAMP(7);
            if (FE == 1) { dma4v(rsp, lw + T18, vA, as0 + pi * 256); dma4v(rsp, lw + T19, vA, as0 + pi * 256 + 128); }
            __builtin_amdgcn_s_setprio(KPRIO);
            SPIN(M_RL, 7);
            KVC(0); KVC(1); KVC(2); KVC(3);
            __builtin_amdgcn_s_setprio(CPRIO);
        }
        STAMP(4);
        asm volatile("" : "+v"(bsw[0]), "+v"(bsw[1]), "+v"(bsw[2]), "+v"(bsw[3]));
        f32x4 ac[4][4], a4[2];
#pragma unroll
        for (int m = 0; m < 4; ++m) {
            const float bs = bflo(bsw[m]);
#pragma unroll
            for (int l = 0; l < 4; ++l) ac[m][l] = (f32x4){bs, bs, bs, bs};
            if (m < 2) a4[m] = (f32x4){bs, bs, bs, bs};
            asm volatile("" : "+v"(ac[m][0]), "+v"(ac[m][1]), "+v"(ac[m][2]), "+v"(ac[m][3]));
        }
        asm volatile("" : "+v"(a4[0]), "+v"(a4[1]));
        const int swz = (g ^ (fr >> 1)) << 4;
        unsigned x01 = L0 + T18 + (64 * wm + 32 * wn + fr) * 128 + swz, x23 = L0 + T18 + (64 * wm + 32 * (1 - wn) + fr) * 128 + swz, y01 = x01 ^ 64, y23 = x23 ^ 64;
        unsigned pb = L0 + T18 + SL_A + 5 * wn * 2048 + lane * 16, p4_ = L0 + T18 + SL_A + 4 * 2048 + lane * 16;
        bf16x8 ax[4], bx[5], ay[4], by[5];
#define MMA_L(B, A, l) do { _Pragma("unroll") for (int m = 0; m < 4; ++m) ac[m][l] = mfma16(B[l], A[m], ac[m][l]); } while (0)
#define MMA_4(B, A) do { a4[0] = mfma16(B[4], A[0], a4[0]); a4[1] = mfma16(B[4], A[1], a4[1]); } while (0)
#define RDX_B() do { bx[0] = RDP(pb, 0); bx[1] = RDP(pb, 2048); bx[2] = RDP(pb, 4096); bx[3] = RDP(pb, 6144); } while (0)
#define RDX_A() do { ax[0] = RDP(x01, 0); ax[1] = RDP(x01, 2048); ax[2] = RDP(x23, 0); ax[3] = RDP(x23, 2048); bx[4] = RDP(p4_, 0); } while (0)
#define RDY() do { by[0] = RDP(pb, 1024); ay[0] = RDP(y01, 0); ay[1] = RDP(y01, 2048); ay[2] = RDP(y23, 0); ay[3] = RDP(y23, 2048); \
        by[1] = RDP(pb, 3072); by[2] = RDP(pb, 5120); by[3] = RDP(pb, 7168); by[4] = RDP(p4_, 1024); } while (0)
        asm volatile("" : "+v"(x01), "+v"(x23), "+v"(y01), "+v"(y23), "+v"(pb), "+v"(p4_));
        SCHED0();
        STAMP(8);
        WAIT_V(FE == 1 ? 4 : 5);
        WAIT_L0();
        BAR();
        STAMP(9);
        RDX_B();
        RDX_A();
        int e0 = T19 - T18 - SLOT, e1 = 3 * SLOT - T19;
#define CSTEP(sync) do { \
            RDY(); \
            MMA_L(bx, ax, 0); MMA_L(bx, ax, 1); MMA_L(bx, ax, 2); \
            SCHED_IL(9, 12); \
            ADV(x01, dl); ADV(x23, dl); ADV(y01, dl); ADV(y23, dl); ADV(pb, dl); ADV(p4_, dl); \
            MMA_L(bx, ax, 3); \
            SGB(0x002, 2); SGB(0x008, 1); SGB(0x002, 2); SGB(0x008, 1); SGB(0x002, 2); SGB(0x008, 2); \
            SCHED0(); \
            sync; \
            RDX_B(); \
            MMA_4(bx, ax); MMA_L(by, ay, 0); \
            SCHED_IL(4, 6); \
            asm volatile("" : "+v"(a4[0]), "+v"(a4[1])); \
            RDX_A(); \
            MMA_L(by, ay, 1); MMA_L(by, ay, 2); MMA_L(by, ay, 3); MMA_4(by, ay); \
            SCHED_IL(5, 14); } while (0)
#pragma unroll 1
        for (int t = 18; t < 48; ++t) {
            const int dl = ((t & 3) == 3 ? -3 * SLOT : SLOT) + e0;
            e0 = opqs(e1); e1 = 0;
            CSTEP(__builtin_amdgcn_s_waitcnt(FE ? 0x0070 : 0x0071); BAR());
        }
        {
            const int dl = SLOT;
            CSTEP(LDSW(M_RC + w * 4) = 3u; SCHED0());
        }
        STAMP(10);
        RDY();
#pragma unroll
        for (int q = 0; q < 8; ++q) zp[q] = *(LAS u32x4*)(lds + ZS + ((4 * wm + (((q >> 1) + 2 * wn) & 3)) * 4 + 2 * wn + (q & 1)) * 1024 + lane * 16);
        zp[8] = *(LAS u32x4*)(lds + Z8OFF(2 * wm + wn) + lane * 16);
        MMA_L(bx, ax, 0); MMA_L(bx, ax, 1); MMA_L(bx, ax, 2); MMA_L(bx, ax, 3); MMA_4(bx, ax);
        SCHED_IL(18, 18);
        LDSW(M_RC + w * 4) = 4u;
        SCHED0();
        MMA_L(by, ay, 0); MMA_L(by, ay, 1); MMA_L(by, ay, 2); MMA_L(by, ay, 3); MMA_4(by, ay);
        SCHED0();
        STAMP(11);
        {
            const int kap0 = 9 * (pj >> 1) + 4 * (pj & 1) + 2 * wn, kap4 = 9 * (pj >> 1) + 8;
#pragma unroll
            for (int lp = 0; lp < 2; ++lp)
#pragma unroll
                for (int m = 0; m < 4; ++m) {
                    const unsigned dst = WS_P + (pi * 8 + 4 * wm + ((m + 2 * wn) & 3)) * (72 * 1024);
                    const u32x4 z = zp[m * 2 + lp];
                    const f32x4 a = ac[m][2 * lp], c = ac[m][2 * lp + 1];
                    st16c(rs, PSTO(lane * 16), dst + (kap0 + lp) * 1024, (u32x4){mulz(z[0], lo2(a)), mulz(z[1], hi2(a)), mulz(z[2], lo2(c)), mulz(z[3], hi2(c))});
                    SCHED0();
                }
#pragma unroll
            for (int m = 0; m < 2; ++m)
                st8c(rs, PSTO((lane >> 5) * 256 + fr * 16 + (g & 1) * 8), WS_P + (pi * 8 + 4 * wm + 2 * wn + m) * (72 * 1024) + kap4 * 1024 + (pj & 1) * 512, (u32x2){mulz(zp[8][2 * m], lo2(a4[m])), mulz(zp[8][2 * m + 1], hi2(a4[m]))});
        }
        SCHED0();
        if (CVD || RGW == 5) LDSW(M_RC + w * 4) = 5u;
        STAMP(12);
        TAILS(); TAILS2();
#pragma unroll
        for (int i = 0; i < CPRE; ++i) G3_LDB(i);
        SCHED0();
        WAIT_V(5 * CPRE);
        if (lane == 0) stflag(ws + WS_FP + ((pi * 16 + pj) * 4 + w) * 8, tag);
        __builtin_amdgcn_s_setprio(0);
        STAMP(13);
        SCHED0();
#pragma unroll
        for (int i = CPRE; i < RB; ++i) G3_LDB(i);
        SCHED0();
        if (w < 2) { SPIN(M_RC, 4); G3_DMA(redo, sB, sB4); }
        FP_ISSUE();
        STAMP(20);
        FP_CHECK(0);
        WAIT_VB(0);
        asm volatile(".Lmega_cend:");
    }
    {
        const int lane = opqv(tid) & 63, fr = lane & 15, g = lane >> 4;
        TAILS(); TAILS2();
        f32x4 acc[4][5];
        if (LPRIO) { if (wid >= 4) __builtin_amdgcn_s_setprio(LPRIO); }
        STAMP(21);
        const int l0 = 2 * (wid & 1);
#define OUTPOS() const int lane = opqv(tid) & 63, fr = lane & 15, g = lane >> 4, tA = 64 * rg + 16 * (wid >> 1) + fr, dA = 16 * (n30 + l0) + 4 * g, \
        tH = 64 * rg + 32 * (wid & 1) + 16 * (g >> 1) + fr, dH = 16 * n34 + 8 * (ct & 1) + 4 * (g & 1)
        u32x2 xv[3]; unsigned yv[3][4], fy; float mb[2], rd[2];
        bool ylate;
#define LOADY() do { OUTPOS(); _Pragma("unroll") for (int rr = 0; rr < 4; ++rr) { yv[0][rr] = ld2c(rs, (dA * S + tA) * 2, WS_YT + rr * (S * 2)); \
        yv[1][rr] = ld2c(rs, (dA * S + tA) * 2, WS_YT + (16 + rr) * (S * 2)); yv[2][rr] = ld2c(rs, (dH * S + tH) * 2, WS_YT + rr * (S * 2)); } } while (0)
        {
            bf16x8 aq[9][4];
            unsigned fw;
            const int fwo = wid_ == 2 || (wid_ == 3 && b_ < 64) ? M_FL + 12 : wid_ == 3 ? M_FL + 8 : M_RC;
            u64 fyv;
            G3_LDA(0); G3_LDA(1); G3_LDA(2); G3_LDA(3);
            SCHED0();
#pragma unroll
            for (int i = 0; i < 9; ++i) {
                if (i == YCK) {
                    asm volatile("" : "+v"(fyv) :: "memory");
                    ylate = !allset(fyv, tag);
                    LOADY();
                    SCHED0();
                }
#pragma unroll
                for (int m = 0; m < 4; ++m)
#pragma unroll
                    for (int l = 0; l < 5; ++l) acc[m][l] = mfma16(bq[i][l], aq[i][m], i == 0 ? zero4 : acc[m][l]);
                SCHED0();
                XST(30 + i);
                if (2 * i + 4 < 9) G3_LDA(2 * i + 4);
                if (2 * i + 5 < 9) G3_LDA(2 * i + 5);
                if (i == YIS) { const int lane = opqv(tid) & 63; fy = WS_FY + (lane < 32 ? 16 * (n30 + l0) + lane : 16 * n34 + 8 * (ct & 1) + (lane & 7)) * 8; fyv = ldflag(rs, fy); }
                if (i == XIS) {
                    OUTPOS();
                    xv[0] = *(const u32x2*)((const char*)x + (unsigned)((tA * D + dA) * 2));
                    xv[1] = *(const u32x2*)((const char*)x + (unsigned)((tA * D + dA + 16) * 2));
                    xv[2] = *(const u32x2*)((const char*)x + (unsigned)((tH * D + dH) * 2));
                    mb[0] = __uint_as_float(ld4c(rs, WS_ST1 + (tA >> 1) * 16 + (tA & 1) * 4)); rd[0] = __uint_as_float(ld4c(rs, WS_ST1 + (tA >> 1) * 16 + (tA & 1) * 4 + 8));
                    mb[1] = __uint_as_float(ld4c(rs, WS_ST1 + (tH >> 1) * 16 + (tH & 1) * 4)); rd[1] = __uint_as_float(ld4c(rs, WS_ST1 + (tH >> 1) * 16 + (tH & 1) * 4 + 8));
                }
                if (i == 2) fw = LDSW(fwo);
                if (i == 3) wcheck(fw, L0 + fwo, 4);
                if (i >= 3 && i + 2 < 9) G3_RDB(i + 2);
                SCHED0();
            }
        }
        STAMP(22);
        LAS char* red = lds + redo;
#pragma unroll
        for (int mm = 0; mm < 2; ++mm)
#pragma unroll
            for (int l = 0; l < 4; ++l) *(LAS f32x4*)(red + (mm * 4 + l) * 1024 + lane * 16) = acc[mm][l];
#pragma unroll
        for (int mm = 2; mm < 4; ++mm)
#pragma unroll
            for (int l = 0; l < 4; ++l) *(LAS f32x4*)(red + (mm * 4 + l) * 1024 + lane * 16) = acc[mm][l];
        if ((g >> 1) == (ct & 1)) {
#pragma unroll
            for (int mm = 0; mm < 4; ++mm) *(LAS f32x4*)(red + 16384 + mm * 512 + (lane & 31) * 16) = acc[mm][4];
        }
        STAMP(23);
        asm volatile("s_waitcnt lgkmcnt(0)\n s_barrier\n.Lmega_tail:" ::: "memory");
        STAMP(24);
        if (ylate) { spin_flags(rs, fy, tag); LOADY(); XST(39); }
        {
        OUTPOS();
#pragma unroll
        for (int u = 0; u < 3; ++u) {
            if (u == 2 && wid >= 2) break;
            const int off = (u < 2 ? (2 * wid + u) * 1024 : 16384 + wid * 1024) + lane * 16;
            f32x4 o = zero4;
#pragma unroll
            for (int v = 0; v < 4; ++v) o += *(LAS f32x4*)(lds + redoff(v) + off) + *(LAS f32x4*)(lds + redoff(v + 4) + off);
            float res[4];
#pragma unroll
            for (int rr = 0; rr < 4; ++rr) {
                const float ob = rbf(o[rr] * rd[u >> 1]);
                const float sg = rbf(ob * __builtin_amdgcn_rcpf(1.0f + __builtin_amdgcn_exp2f(-1.4426950408889634f * ob)));
                const float xx = (rr & 1) ? bfhi(xv[u][rr >> 1]) : bflo(xv[u][rr >> 1]);
                res[rr] = xx + rbf(rbf(rbf(rbf(xx - mb[u >> 1]) * rd[u >> 1]) * bflo(yv[u][rr])) * sg);
            }
            const unsigned oo = u < 2 ? (tA * D + dA + 16 * u) * 2 : (tH * D + dH) * 2;
            __builtin_amdgcn_raw_buffer_store_b64((u32x2){pk2(res[0], res[1]), pk2(res[2], res[3])}, in_rsrc(out, SZ_X), oo, 0, OAUX);
        }
        }
    }
}

extern "C" void kernel_launch(void* const* d_in, const int* in_sizes, int n_in,
                              void* d_out, int out_size, void* d_ws, size_t ws_size,
                              hipStream_t stream) {
    (void)hipFuncSetAttribute((const void*)mega, hipFuncAttributeMaxDynamicSharedMemorySize, LDS_SZ);
    mega<<<256, 512, LDS_SZ, stream>>>((const u16*)d_in[0], (const u16*)d_in[1], (const u16*)d_in[2], (const u16*)d_in[3], (const u16*)d_in[4], (const u16*)d_in[5],
                                       (const u16*)d_in[7], (const u16*)d_in[8], (const u16*)d_in[9], (const u16*)d_in[10], (char*)d_ws, (u16*)d_out);
}
```

```cpp
#include <hip/hip_runtime.h>
#include <stdint.h>
#define CODE_PF

#define LAS __attribute__((address_space(3)))
#define GAS __attribute__((address_space(1)))
typedef unsigned short u16;
typedef unsigned long long u64;
typedef short bf16x8 __attribute__((ext_vector_type(8)));
typedef short bf16x4 __attribute__((ext_vector_type(4)));
typedef float f32x4 __attribute__((ext_vector_type(4)));
typedef float f32x2 __attribute__((ext_vector_type(2)));
typedef unsigned u32x4 __attribute__((ext_vector_type(4)));
typedef unsigned u32x2 __attribute__((ext_vector_type(2)));
typedef __bf16 bf16x2_t __attribute__((ext_vector_type(2)));

constexpr int S = 2048, D = 576, F = 4608, FH = 2304;

__device__ __forceinline__ float bflo(unsigned w) { return __uint_as_float(w << 16); }
__device__ __forceinline__ float bfhi(unsigned w) { return __uint_as_float(w & 0xffff0000u); }
__device__ __forceinline__ unsigned pk2v(f32x2 p) { return __builtin_bit_cast(unsigned, __builtin_convertvector(p, bf16x2_t)); }
__device__ __forceinline__ unsigned pk2(float lo, float hi) { return pk2v((f32x2){lo, hi}); }
__device__ __forceinline__ f32x2 up2(unsigned w) { return (f32x2){bflo(w), bfhi(w)}; }
__device__ __forceinline__ float rbf(float x) { return bflo(pk2(x, x)); }
__device__ __forceinline__ f32x2 rbf2(f32x2 x) { return up2(pk2v(x)); }
__device__ __forceinline__ f32x2 fma2(f32x2 a, f32x2 b, f32x2 c) { return __builtin_elementwise_fma(a, b, c); }
__device__ __forceinline__ f32x2 lo2(f32x4 a) { return (f32x2){a[0], a[1]}; }
__device__ __forceinline__ f32x2 hi2(f32x4 a) { return (f32x2){a[2], a[3]}; }
__device__ __forceinline__ f32x4 mfma16(bf16x8 a, bf16x8 b, f32x4 c) { return __builtin_amdgcn_mfma_f32_16x16x32_bf16(a, b, c, 0, 0, 0); }
__device__ __forceinline__ bf16x4 trread(LAS char* p) { return __builtin_bit_cast(bf16x4, __builtin_amdgcn_ds_read_tr16_b64_v4i16((LAS bf16x4*)p)); }
__device__ __forceinline__ bf16x8 cat8(bf16x4 a, bf16x4 b) { return __builtin_shufflevector(a, b, 0, 1, 2, 3, 4, 5, 6, 7); }
#define WAIT_V(n) asm volatile("s_waitcnt vmcnt(%0)" ::"n"(n) : "memory")
#define WAIT_VB(n) __builtin_amdgcn_s_waitcnt(((n) & 15) | (7 << 4) | (15 << 8) | (((n) >> 4) << 14))
#define WAIT_L0() asm volatile("s_waitcnt lgkmcnt(0)" ::: "memory")
#define BAR() do { asm volatile("" ::: "memory"); __builtin_amdgcn_s_barrier(); asm volatile("" ::: "memory"); } while (0)
#define SCHED0() __builtin_amdgcn_sched_barrier(0)
#define RLX_AGENT __ATOMIC_RELAXED, __HIP_MEMORY_SCOPE_AGENT
#define PIN9(z) asm volatile("" : "+v"(z[0]), "+v"(z[1]), "+v"(z[2]), "+v"(z[3]), "+v"(z[4]), "+v"(z[5]), "+v"(z[6]), "+v"(z[7]), "+v"(z[8]))
__device__ __forceinline__ int opqs(int x) { asm volatile("" : "+s"(x)); return x; }
__device__ __forceinline__ int opqv(int x) { asm volatile("" : "+v"(x)); return x; }
template <int I> struct IC { static constexpr int v = I; };
template <int A, int B, class Fn> __device__ __forceinline__ void sfor(Fn&& f) { if constexpr (A < B) { f(IC<A>{}); sfor<A + 1, B>(f); } }
#define SFOR(i, a, b) sfor<a, b>([&](auto i##_) __attribute__((always_inline)) { constexpr int i = decltype(i##_)::v;
#define SEND });
#define RFL(x) __builtin_amdgcn_readfirstlane(x)

#define TIMER "s_memrealtime"
#define STAMP(i) do { } while (0)

__device__ __forceinline__ f32x2 gelu2(f32x2 v) {
    const float c1 = -2.0f * 0.7978845608028654f * 1.4426950408889634f;
    const float c2 = c1 * 0.044715f;
    f32x2 a = v * fma2(v * v, (f32x2){c2, c2}, (f32x2){c1, c1});
    f32x2 d = (f32x2){__builtin_amdgcn_exp2f(a.x), __builtin_amdgcn_exp2f(a.y)} + (f32x2){1.0f, 1.0f};
    return v * (f32x2){__builtin_amdgcn_rcpf(d.x), __builtin_amdgcn_rcpf(d.y)};
}
__device__ __forceinline__ float adds(float a, float b) { float o; asm("v_add_f32 %0, %1, %2" : "=v"(o) : "v"(a), "v"(b)); return o; }
__device__ __forceinline__ float muls(float a, float b) { float o; asm("v_mul_f32 %0, %1, %2" : "=v"(o) : "v"(a), "v"(b)); return o; }
__device__ __forceinline__ f32x2 ln_consts(float sm, float sq, float inv_n) {
    float mean, m2;
    asm("v_mul_f32 %0, %2, %3\n v_mul_f32 %1, %2, %4" : "=&v"(mean), "=v"(m2) : "s"(inv_n), "v"(sm), "v"(sq));
    const float var = fmaxf(fmaf(-mean, mean, m2), 0.f);
    return (f32x2){rbf(mean), __builtin_amdgcn_rcpf(rbf(__builtin_amdgcn_sqrtf(rbf(rbf(var) + 1e-5f))))};
}

constexpr unsigned SZ_Z = S * FH * 2, SZ_X = S * D * 2;
constexpr unsigned WS_Z2N = 0;
constexpr unsigned WS_P = WS_Z2N + SZ_Z;
constexpr unsigned WS_KVT = WS_P + SZ_Z;
constexpr unsigned WS_YT = WS_KVT + SZ_X;
constexpr unsigned WS_WT = WS_YT + SZ_X;
constexpr unsigned WS_ST2 = WS_WT + FH * D * 2;
constexpr unsigned WS_ST1 = WS_ST2 + 256 * 1024;
constexpr unsigned WS_SR = WS_ST1 + 16384;
constexpr unsigned WS_SC = WS_SR + 16 * 12288;
constexpr unsigned WS_FZ = WS_SC + 32 * 12288;
constexpr unsigned WS_FP = WS_FZ + 8192;
constexpr unsigned WS_FK = WS_FP + 8192;
constexpr unsigned WS_FY = WS_FK + 2048;
constexpr unsigned WS_E = WS_FY + 4608 + 128;
constexpr unsigned WS_END = WS_E + 128;
constexpr u64 TAG_INC = 0x9E3779B97F4A7C15ull;

__device__ __forceinline__ __amdgpu_buffer_rsrc_t ws_rsrc(char* ws) { return __builtin_amdgcn_make_buffer_rsrc(ws, 0, (int)WS_END, 0x00020000); }
__device__ __forceinline__ void st16c(__amdgpu_buffer_rsrc_t r, unsigned off, unsigned soff, u32x4 v) { __builtin_amdgcn_raw_buffer_store_b128(v, r, off, soff, 16); asm volatile("s_nop 1" :: "v"(v)); }
__device__ __forceinline__ void st8c(__amdgpu_buffer_rsrc_t r, unsigned off, unsigned soff, u32x2 v) { __builtin_amdgcn_raw_buffer_store_b64(v, r, off, soff, 16); }
__device__ __forceinline__ u32x4 ld16c(__amdgpu_buffer_rsrc_t r, unsigned off, unsigned soff) { return __builtin_amdgcn_raw_buffer_load_b128(r, off, soff, 16); }
__device__ __forceinline__ unsigned ld4c(__amdgpu_buffer_rsrc_t r, unsigned off) { return __builtin_amdgcn_raw_buffer_load_b32(r, off, 0, 16); }
__device__ __forceinline__ unsigned ld2c(__amdgpu_buffer_rsrc_t r, unsigned off, unsigned soff) { return __builtin_amdgcn_raw_buffer_load_b16(r, off, soff, 16); }
__device__ __forceinline__ __amdgpu_buffer_rsrc_t in_rsrc(const void* p, unsigned bytes) { return __builtin_amdgcn_make_buffer_rsrc((void*)p, 0, (int)bytes, 0x00020000); }
__device__ __forceinline__ u32x4 mk_rsrc(const void* p, unsigned bytes) { const u64 a = (u64)p; return (u32x4){(unsigned)a, (unsigned)(a >> 32) & 0xffffu, bytes, 0x00020000u}; }
#define DMA_HEAD "s_add_i32 m0, %1, %5\n s_add_i32 %0, %4, %6\n buffer_load_dwordx4 %2, %3, %0 offen"
#define DMA_NEXT(o) "\n buffer_load_dwordx4 %2, %3, %0 offen offset:" #o " sc1 lds"
#define DMA_OPS : "=&s"(t) : "s"(lb), "v"(voff), "s"(r), "s"(sb), "n"(LI), "n"(SI) : "scc", "memory"
template <int LI, int SI> __device__ __forceinline__ void dma16(u32x4 r, unsigned lb, unsigned voff, unsigned sb) { unsigned t; asm volatile(DMA_HEAD " lds" DMA_OPS); }
template <int LI, int SI> __device__ __forceinline__ void dma16c(u32x4 r, unsigned lb, unsigned voff, unsigned sb) { unsigned t; asm volatile(DMA_HEAD " sc1 lds" DMA_OPS); }
template <int LI, int SI> __device__ __forceinline__ void dma16c3(u32x4 r, unsigned lb, unsigned voff, unsigned sb) { unsigned t; asm volatile(DMA_HEAD " sc1 lds" DMA_NEXT(1024) DMA_NEXT(2048) DMA_OPS); }
template <int LI, int SI> __device__ __forceinline__ void dma16c4(u32x4 r, unsigned lb, unsigned voff, unsigned sb) { unsigned t; asm volatile(DMA_HEAD " sc1 lds" DMA_NEXT(1024) DMA_NEXT(2048) DMA_NEXT(3072) DMA_OPS); }
template <int SI> __device__ __forceinline__ void pf4(u32x4 r, unsigned lb, unsigned voff, unsigned sb) {
    unsigned t;
    asm volatile("s_mov_b32 m0, %1\n s_add_i32 %0, %4, %5\n buffer_load_dword %2, %3, %0 offen lds" : "=&s"(t) : "s"(lb), "v"(voff), "s"(r), "s"(sb), "n"(SI) : "scc", "memory");
}
template <int SI> __device__ __forceinline__ void pf4c(u32x4 r, unsigned lb, unsigned voff, unsigned sb) {
    unsigned t;
    asm volatile("s_mov_b32 m0, %1\n s_add_i32 %0, %4, %5\n buffer_load_dword %2, %3, %0 offen sc1 lds" : "=&s"(t) : "s"(lb), "v"(voff), "s"(r), "s"(sb), "n"(SI) : "scc", "memory");
}
__device__ __forceinline__ void dma4v(u32x4 r, unsigned lb, const unsigned* v, unsigned sb) {
    asm volatile("s_mov_b32 m0, %0\n s_nop 0\n buffer_load_dwordx4 %1, %5, %6 offen lds\n buffer_load_dwordx4 %2, %5, %6 offen offset:1024 lds\n"
                 "buffer_load_dwordx4 %3, %5, %6 offen offset:2048 lds\n buffer_load_dwordx4 %4, %5, %6 offen offset:3072 lds"
                 :: "s"(lb), "v"(v[0]), "v"(v[1]), "v"(v[2]), "v"(v[3]), "s"(r), "s"(sb) : "memory");
}
__device__ __forceinline__ void dmah(u32x4 r, unsigned lb, unsigned v, unsigned sb) {
    asm volatile("s_mov_b32 m0, %0\n s_mov_b32 exec_hi, 0\n buffer_load_dwordx4 %1, %2, %3 offen lds\n s_mov_b32 exec_hi, -1" :: "s"(lb), "v"(v), "s"(r), "s"(sb) : "memory");
}
__device__ __forceinline__ void dmahc(u32x4 r, unsigned lb, unsigned v, unsigned sb) {
    asm volatile("s_mov_b32 m0, %0\n s_mov_b32 exec_hi, 0\n buffer_load_dwordx4 %1, %2, %3 offen sc1 lds\n s_mov_b32 exec_hi, -1" :: "s"(lb), "v"(v), "s"(r), "s"(sb) : "memory");
}
__device__ __forceinline__ u64 ldflag(__amdgpu_buffer_rsrc_t r, unsigned off) {
    asm volatile("" ::: "memory");
    return __builtin_bit_cast(u64, __builtin_amdgcn_raw_buffer_load_b64(r, off, 0, 16));
}
__device__ __forceinline__ void stflag(char* p, u64 v) { __hip_atomic_store((GAS u64*)p, v, RLX_AGENT); }
__device__ __forceinline__ bool allset(u64 v, u64 tag) { return __builtin_amdgcn_ballot_w64(v != tag) == 0ull; }

constexpr int SL_A = 16384, SL_B = 18432, SLOT = SL_A + SL_B;
constexpr unsigned SPIN_CAP = 1u << 20;
constexpr int L_ROWST = 145408;
constexpr int L_CS = L_ROWST + 1024;
constexpr int L_ZST = L_CS + 1024;
constexpr int L_WSCR = L_ZST + 1024;
constexpr int LDS_SZ = 163840;
constexpr int L_DUMP = LDS_SZ - 256;
constexpr int L_MISC = L_DUMP - 256;
constexpr int M_RL = L_MISC, M_FL = L_MISC + 16, M_RC = L_MISC + 32, M_RS = L_MISC + 48;
constexpr int CV_U = 512 + 4096, CV_HSTRIDE = 4160, CV_WAVE = CV_U + 4 * CV_HSTRIDE;
constexpr int RED_W = 18432, ZS = 2 * SLOT;
__host__ __device__ constexpr int redoff(int v) { return v < 4 ? 256 + CV_WAVE + v * RED_W : v == 7 ? 256 + CV_WAVE + 4 * RED_W : v == 6 ? 256 : (v == 4 ? L_MISC : 4 * SLOT) - CV_WAVE; }
#define Z8OFF(w_) (ZS + 32768 + (w_) * 1024 + ((w_) >> 1) * SLOT)
constexpr u64 redtab(int v) { return (u64)(redoff(v) >> 8) | (u64)(redoff(v + 1) >> 8) << 16 | (u64)(redoff(v + 2) >> 8) << 32 | (u64)(redoff(v + 3) >> 8) << 48; }
__host__ __device__ constexpr int warea(int j) { return 16384 + 18432 * (j == 0 || j == 7 ? 1 : j == 1 || j == 8 ? 0 : j == 2 ? 3 : j == 3 ? 5 : j == 4 ? 4 : j == 5 ? 6 : 2); }
constexpr int T18 = warea(2), T19 = warea(3), KVS = warea(6);
typedef unsigned u32x3 __attribute__((ext_vector_type(3)));

#define SCHED_IL(nr, nm) do { _Pragma("unroll") for (int i_ = 0; i_ < (nr); ++i_) { __builtin_amdgcn_sched_group_barrier(0x008, 1, 0); __builtin_amdgcn_sched_group_barrier(0x100, 1, 0); } \
        __builtin_amdgcn_sched_group_barrier(0x008, (nm) - (nr), 0); SCHED0(); } while (0)

__device__ __forceinline__ void spin_flags(__amdgpu_buffer_rsrc_t r, unsigned p, u64 tag) {
    unsigned spins = 0;
    while (!allset(ldflag(r, p), tag)) { if (++spins > SPIN_CAP) break; __builtin_amdgcn_s_sleep(1); }
    asm volatile("" ::: "memory");
}
__device__ __forceinline__ void dma1(int OFF, u32x4 r, unsigned lb, unsigned v, unsigned sb) {
    asm volatile("s_mov_b32 m0, %0\n s_nop 0\n buffer_load_dwordx4 %1, %2, %3 offen offset:%4 lds" :: "s"(lb), "v"(v), "s"(r), "s"(sb), "n"(OFF) : "memory");
}
#define WNT " nt"
#define KNT ""
__device__ __forceinline__ void dma1n(int OFF, u32x4 r, unsigned lb, unsigned v, unsigned sb) {
    asm volatile("s_mov_b32 m0, %0\n s_nop 0\n buffer_load_dwordx4 %1, %2, %3 offen offset:%4" WNT " lds" :: "s"(lb), "v"(v), "s"(r), "s"(sb), "n"(OFF) : "memory");
}
__device__ __forceinline__ void dma1c(int OFF, u32x4 r, unsigned lb, unsigned v, unsigned sb) {
    asm volatile("s_mov_b32 m0, %0\n s_nop 0\n buffer_load_dwordx4 %1, %2, %3 offen offset:%4 sc1 lds" :: "s"(lb), "v"(v), "s"(r), "s"(sb), "n"(OFF) : "memory");
}
__device__ __forceinline__ u32x4 rec4(float a, float b, float c, float d, unsigned tag) {
    const unsigned q = __float_as_uint(d);
    return (u32x4){__builtin_amdgcn_perm(__float_as_uint(a), q, 0x07060501u), __builtin_amdgcn_perm(__float_as_uint(b), q, 0x07060502u), __builtin_amdgcn_perm(__float_as_uint(c), q, 0x07060503u), tag};
}
__device__ __forceinline__ float half_sum(float a, float b) {
    asm("s_nop 1\n v_permlane32_swap_b32 %0, %1\n v_add_f32 %0, %0, %1\n v_mov_b32 %1, %0\n s_nop 1\n v_permlane16_swap_b32 %0, %1\n v_add_f32 %0, %0, %1" : "+v"(a), "+v"(b));
    return a;
}
template <int N> __device__ __forceinline__ f32x2 rec_consts(const u32x4* pv, float inv_n) {
    float a0, a1, a2, a3;
#pragma unroll
    for (int i = 0; i < N; ++i) {
        const float e = __uint_as_float(__builtin_amdgcn_perm(pv[i][2], __builtin_amdgcn_perm(pv[i][1], pv[i][0], 0x0c04000cu), 0x04020100u));
        if (i == 0) { a0 = __uint_as_float(pv[i][0]); a1 = __uint_as_float(pv[i][1]); a2 = __uint_as_float(pv[i][2]); a3 = e; }
        else { a0 = adds(a0, __uint_as_float(pv[i][0])); a1 = adds(a1, __uint_as_float(pv[i][1])); a2 = adds(a2, __uint_as_float(pv[i][2])); a3 = adds(a3, e); }
    }
    return ln_consts(half_sum(a0, a2), half_sum(a1, a3), inv_n);
}
__device__ __forceinline__ void wcheck(unsigned f, unsigned addr, unsigned need) {
    unsigned cnt;
    asm volatile("v_cmp_gt_u32 vcc, %3, %0\n s_cbranch_vccz 2f\n s_mov_b32 %1, 0x400000\n"
                 "1:\n s_sleep 1\n ds_read_b32 %0, %2\n s_sub_u32 %1, %1, 1\n s_waitcnt lgkmcnt(0)\n s_cbranch_scc1 2f\n v_cmp_gt_u32 vcc, %3, %0\n s_cbranch_vccnz 1b\n2:"
                 : "+v"(f), "=&s"(cnt) : "v"(addr), "n"(need) : "vcc", "scc", "memory");
}
__device__ __forceinline__ unsigned norm2s(f32x2 z, f32x4 st) {
    unsigned o, t;
    asm("v_fma_f32 %0, %2, %4, %6\n v_fma_f32 %1, %3, %5, %7\n v_cvt_pk_bf16_f32 %0, %0, %1" : "=&v"(o), "=&v"(t) : "v"(z.x), "v"(z.y), "v"(st[0]), "v"(st[1]), "v"(st[2]), "v"(st[3]));
    return o;
}
__device__ __forceinline__ void tap4d(float& o0, float& o1, const float* k, const float* h, float v0, float v1) {
    asm("v_mul_f32 %0, %2, %10\n v_mul_f32 %1, %3, %10\n v_fma_f32 %0, %3, %9, %0\n v_fma_f32 %1, %4, %9, %1\n v_fma_f32 %0, %4, %8, %0\n v_fma_f32 %1, %5, %8, %1\n"
        "v_fma_f32 %0, %5, %7, %0\n v_fma_f32 %1, %6, %7, %1\n v_mul_f32 %0, %0, %11\n v_mul_f32 %1, %1, %12"
        : "=&v"(o0), "=&v"(o1) : "v"(k[0]), "v"(k[1]), "v"(k[2]), "v"(k[3]), "v"(k[4]), "v"(h[0]), "v"(h[1]), "v"(h[2]), "v"(h[3]), "v"(v0), "v"(v1));
}

__device__ __forceinline__ float fmas(float a, float b, float c) { float o; asm("v_fma_f32 %0, %1, %2, %3" : "=v"(o) : "v"(a), "v"(b), "v"(c)); return o; }
__device__ __forceinline__ unsigned mulz(unsigned z, f32x2 a) {
    unsigned o, t;
    asm("v_lshlrev_b32 %0, 16, %2\n v_and_b32 %1, 0xffff0000, %2\n v_mul_f32 %0, %0, %3\n v_mul_f32 %1, %1, %4\n v_cvt_pk_bf16_f32 %0, %0, %1" : "=&v"(o), "=&v"(t) : "v"(z), "v"(a.x), "v"(a.y));
    return o;
}
__device__ __forceinline__ unsigned movs(unsigned a) { unsigned o; asm("v_mov_b32 %0, %1" : "=v"(o) : "v"(a)); return o; }
__device__ __forceinline__ unsigned algn(unsigned hi, unsigned lo) { unsigned o; asm("v_alignbit_b32 %0, %1, %2, 16" : "=v"(o) : "v"(hi), "v"(lo)); return o; }
__device__ __forceinline__ unsigned algn2(unsigned hi, unsigned lo) { unsigned o; asm("v_alignbit_b32 %0, %1, %2, 16 " : "=v"(o) : "v"(hi), "v"(lo)); return o; }

#define YPRIO 0
struct ConvIn { u32x4 uu[4], hh[4]; u32x2 hn[4]; unsigned dd; };
#define ALD(w_, dst, vo, r_, so, x_) asm volatile("buffer_load_dword" w_ " %0, %1, %2, %3 offen" x_ : "=v"(dst) : "v"(vo), "s"(r_), "s"(so) : "memory")
__device__ __forceinline__ void conv_load(ConvIn& c, const u16* __restrict__ hk, const u16* __restrict__ dv, u32x4 wr, int d, int lane) {
    const u32x4 rh = mk_rsrc(hk, SZ_X);
    const unsigned v = lane * 16, su = WS_KVT + d * (S * 2), sh = d * (S * 2);
    ALD("x4", c.uu[0], v, wr, su, " sc1"); ALD("x4", c.uu[1], v, wr, su, " offset:1024 sc1"); ALD("x4", c.uu[2], v, wr, su, " offset:2048 sc1"); ALD("x4", c.uu[3], v, wr, su, " offset:3072 sc1");
    ALD("x4", c.hh[0], v, rh, sh, ""); ALD("x4", c.hh[1], v, rh, sh, " offset:1024"); ALD("x4", c.hh[2], v, rh, sh, " offset:2048"); ALD("x4", c.hh[3], v, rh, sh, " offset:3072");
    ALD("x2", c.hn[0], v, rh, sh, " offset:16"); ALD("x2", c.hn[1], v, rh, sh, " offset:1040"); ALD("x2", c.hn[2], v, rh, sh, " offset:2064"); ALD("x2", c.hn[3], v, rh, sh, " offset:3088");
    c.dd = ((const unsigned*)dv)[d >> 1];
}
#define CONV_PIN(c) asm volatile("" : "+v"(c.uu[0]), "+v"(c.uu[1]), "+v"(c.uu[2]), "+v"(c.uu[3]), "+v"(c.hh[0]), "+v"(c.hh[1]), "+v"(c.hh[2]), "+v"(c.hh[3]), "+v"(c.hn[0]), "+v"(c.hn[1]), "+v"(c.hn[2]), "+v"(c.hn[3]))
template <class MID> __device__ __forceinline__ void conv_stage(ConvIn& c, LAS char* U, int lane, MID mid) {
    LAS char* H = U + CV_U;
    if (lane == 63) c.hn[3] = (u32x2){0u, 0u};
    if (lane < 32) *(LAS u32x4*)(U + lane * 16) = (u32x4){0u, 0u, 0u, 0u};
    const unsigned h0 = RFL(c.hh[0][0]), h1 = RFL(c.hh[0][1]);
    if (lane < 8) {
        const int cc = lane >> 1, half = lane & 1;
        u32x4 z = {0u, 0u, 0u, 0u};
        if (half) {
            if (cc == 1) z[3] = h0 << 16;
            if (cc == 2) z[3] = h0;
            if (cc == 3) { z[2] = h0 << 16; z[3] = __builtin_amdgcn_alignbit(h1, h0, 16); }
        }
        *(LAS u32x4*)(H + cc * CV_HSTRIDE + half * 16) = z;
    }
    SFOR(i, 0, 4)
        SCHED0();
        mid(IC<i>{});
        SCHED0();
        *(LAS u32x4*)(U + 512 + i * 1024 + lane * 16) = c.uu[i];
        const unsigned w0 = c.hh[i][0], w1 = c.hh[i][1], w2 = c.hh[i][2], w3 = c.hh[i][3], w4 = c.hn[i][0], w5 = c.hn[i][1];
        LAS char* p = H + 32 + i * 1024 + lane * 16;
        *(LAS u32x4*)(p) = c.hh[i];
        *(LAS u32x4*)(p + CV_HSTRIDE) = (u32x4){algn(w1, w0), algn(w2, w1), algn(w3, w2), algn(w4, w3)};
        *(LAS u32x4*)(p + 2 * CV_HSTRIDE) = (u32x4){movs(w1), movs(w2), movs(w3), movs(w4)};
        *(LAS u32x4*)(p + 3 * CV_HSTRIDE) = (u32x4){algn2(w2, w1), algn2(w3, w2), algn2(w4, w3), algn2(w5, w4)};
    SEND
}
__device__ __forceinline__ void conv_run(unsigned ddw, __amdgpu_buffer_rsrc_t rs, LAS char* U, int d, int lane) {
    const int fr = lane & 15, g = lane >> 4;
    LAS char* H = U + CV_U;
    const int e0 = 16 * (g >> 1) - 8 * (g & 1) - 7 + fr + 16;
    const int ac = e0 & 3;
    LAS char* ap = H + ac * CV_HSTRIDE + (e0 - ac) * 2;
    LAS char* bp = U + 512 + 32 * (fr - (g >> 1)) + 16 * (g & 1) - 448;
    f32x4 acc[8];
#pragma unroll
    for (int n = 0; n < 8; ++n) acc[n] = (f32x4){0.f, 0.f, 0.f, 0.f};
    bf16x8 LA[2][4], LB[2][4], HA[4], HB[4];
#define CV_RD(A, B, a0, pa, pb) do { _Pragma("unroll") for (int a = 0; a < 4; ++a) { B[a] = *(LAS bf16x8*)((pb) + (a0 + a) * 512); \
        A[a] = cat8(*(LAS bf16x4*)((pa) + (a0 + a) * 512), *(LAS bf16x4*)((pa) + (a0 + a) * 512 + 8)); } } while (0)
#define CV_STEP(cur, nxt, pa, pb) do { \
        CV_RD(HA, HB, 4, pa, pb); \
        _Pragma("unroll") for (int s = 0; s < 4; ++s) { \
            _Pragma("unroll") for (int a = 0; a < s; ++a) { acc[a + s] = mfma16(LA[cur][a], LB[cur][s], acc[a + s]); acc[a + s] = mfma16(LA[cur][s], LB[cur][a], acc[a + s]); } \
            acc[2 * s] = mfma16(LA[cur][s], LB[cur][s], acc[2 * s]); } \
        SCHED_IL(12, 16); \
        CV_RD(LA[nxt], LB[nxt], 0, (pa) + 64, (pb) - 64); \
        _Pragma("unroll") for (int s = 0; s < 4; ++s) \
            _Pragma("unroll") for (int a = 0; a + s < 4; ++a) { acc[a + s + 4] = mfma16(LA[cur][a], HB[s], acc[a + s + 4]); acc[a + s + 4] = mfma16(HA[s], LB[cur][a], acc[a + s + 4]); } \
        SCHED_IL(12, 20); \
    } while (0)
    CV_RD(LA[0], LB[0], 0, ap, bp + 448);
#pragma unroll 1
    for (int bb = 0; bb < 8; bb += 2) {
        LAS char* pa = ap + bb * 64;
        LAS char* pb = bp + (7 - bb) * 64;
        CV_STEP(0, 1, pa, pb);
        CV_STEP(1, 0, pa + 64, pb - 64);
    }
    if (YPRIO) __builtin_amdgcn_s_setprio(YPRIO);
    const float dd = (d & 1) ? bfhi(ddw) : bflo(ddw);
    unsigned yw[8][2];
    u32x2 uw[8];
#pragma unroll
    for (int nu = 0; nu < 8; ++nu) uw[nu] = *(LAS u32x2*)(U + 512 + (256 * nu + 16 * fr + 8 * (g >> 1)) * 2 + ((g & 1) ? 0 : 8));
#pragma unroll
    for (int nu = 0; nu < 8; ++nu) {
        yw[nu][0] = pk2(fmas(bfhi(uw[nu][1]), dd, acc[nu][0]), fmas(bflo(uw[nu][1]), dd, acc[nu][1]));
        yw[nu][1] = pk2(fmas(bfhi(uw[nu][0]), dd, acc[nu][2]), fmas(bflo(uw[nu][0]), dd, acc[nu][3]));
    }
#pragma unroll
    for (int p = 0; p < 4; ++p) {
        const u32x2 s0 = __builtin_amdgcn_permlane16_swap(yw[2 * p][0], yw[2 * p + 1][0], false, false);
        const u32x2 s1 = __builtin_amdgcn_permlane16_swap(yw[2 * p][1], yw[2 * p + 1][1], false, false);
        st16c(rs, (256 * (g & 1) + 16 * fr + 8 * (g >> 1)) * 2 + p * 1024, WS_YT + d * (S * 2), (u32x4){s0[0], s1[0], s0[1], s1[1]});
    }
    SCHED0();
}

constexpr int RB = 5, KST = 36 * 1024;
#define KROT 0
#define SPOLL 0
#define KAP(i) ((i) + (KROT) >= 9 ? (i) + (KROT) - 9 : (i) + (KROT))
#define G3_LDB(i) do { _Pragma("unroll") for (int l = 0; l < 4; ++l) bq[i][l] = __builtin_bit_cast(bf16x8, ld16c(rs, lane * 16 + l * 1024, sB + KAP(i) * KST)); \
        bq[i][4] = __builtin_bit_cast(bf16x8, ld16c(rs, lane * 16, sB4 + KAP(i) * KST)); } while (0)
#define MG(m) (((m) + 1 + wid_) & 3)
#define G3_LDA(i) do { _Pragma("unroll") for (int m = 0; m < 4; ++m) aq[i][m] = __builtin_bit_cast(bf16x8, ld16c(rs, lane * 16, sA + MG(m) * (72 * 1024) + KAP(i) * 1024)); } while (0)
#define G3_RDB(i) do { _Pragma("unroll") for (int l = 0; l < 4; ++l) bq[i][l] = *(LAS bf16x8*)(lds + redo + ((i) - RB) * 4096 + l * 1024 + lane * 16); \
        bq[i][4] = *(LAS bf16x8*)(lds + redo + 16384 + ((i) - RB) * 512 + (g * 8 + (fr & 7)) * 16); } while (0)
#define G3_PRE() do { _Pragma("unroll") for (int i = 0; i < RB; ++i) G3_LDB(i); SCHED0(); } while (0)
#define G3_HO ((((lane >> 3) & 3) * 16 + 8 * (ct & 1) + (lane & 7)) * 16)
#define G3_DMA1(dst, sB, sB4) do { dma16c4<0, 0>(wr, L0 + (dst), lane * 16, sB + KAP(5) * KST); dma16c4<4096, 0>(wr, L0 + (dst), lane * 16, sB + KAP(6) * KST); \
        dma16c<16384, 0>(wr, L0 + (dst), G3_HO + (lane < 32 ? KAP(5) : KAP(6)) * KST, sB4); } while (0)
#define G3_DMA2(dst, sB, sB4) do { dma16c4<8192, 0>(wr, L0 + (dst), lane * 16, sB + KAP(7) * KST); dma16c4<12288, 0>(wr, L0 + (dst), lane * 16, sB + KAP(8) * KST); \
        dma16c<17408, 0>(wr, L0 + (dst), G3_HO + (lane < 32 ? KAP(7) : KAP(8)) * KST, sB4); } while (0)
#define G3_DMA(dst, sB, sB4) do { G3_DMA1(dst, sB, sB4); G3_DMA2(dst, sB, sB4); } while (0)
#define FLAG_ISSUE(v_, o_) asm volatile("buffer_load_dwordx2 %0, %1, %2, 0 offen sc1" : "=v"(v_) : "v"(o_), "s"(wr) : "memory")
#define FP_SPIN() spin_flags(rs, sF + (lane & 1) * 16 + (lane & 2) * 16, tag)
#define FP_ISSUE() FLAG_ISSUE(fpv, sF + (lane & 1) * 16 + (lane & 2) * 16)
#define FP_CHECK(NV) do { u64 ne; \
        asm volatile("s_waitcnt vmcnt(%3)\n v_cmp_ne_u64 %0, %1, %2" : "=s"(ne) : "s"(tag), "v"(fpv), "n"(NV) : "memory"); \
        if (ne != 0ull) FP_SPIN(); \
        asm volatile("" ::: "memory"); } while (0)
#define XST(i) do { } while (0)
#define NOSTAT 0
#define ROT (pi + pj) % 9
#define CPRE 2
constexpr float GC1 = -2.0f * 0.7978845608028654f * 1.4426950408889634f, GC2 = GC1 * 0.044715f;
#define CPRIO 2
#define CPFW 2
#define T1W 1
#define KPF 1
#define GS 72
#define WBASE 0
#define WALK 2
#define WPF 4
#define CVD 0
#define YIS 8
#define YCK 8
#define PSTO(x) (x)
#define PSL 2
#define XIS 8
#define LPRIO 0
#define HPF 1
#define CVL 47
#define VPRIO 1
#define RGW 4
#define OAUX 0
#define TE 0
#define KXC 1
#define KC ((b >> 3) & 7)
#define KR ((b & 7) * 4 + (b >> 6))
#define KVE 1
#define HKE 17
#define FE 2
#define GCS 84
#define GPRIO 3
#define A21 1
#define KPRIO 0
#define KS1 1
#define KIL 0
#define WOS 0
#define ZD 6
#define PB1 99
#define PB2 99
#define SPF 2
#define WIL 1
#define XPF 0
#define FRND 0
#define SP2 3
#define SZW 8
#define SZ0 2
#define SZD 8
#define SPR 1
#define SP1 3
#define PAM 0
#define PA0 2

__global__ __launch_bounds__(512, 2) void mega(const u16* __restrict__ x, const u16* __restrict__ kin, const u16* __restrict__ vin, const u16* __restrict__ shk,
                                               const u16* __restrict__ hk, const u16* __restrict__ dv, const u16* __restrict__ w_in, const u16* __restrict__ w_sp,
                                               const u16* __restrict__ b_sp, const u16* __restrict__ w_out, char* __restrict__ ws, u16* __restrict__ out) {
    extern __shared__ __attribute__((aligned(16))) char smem_[];
    LAS char* lds = (LAS char*)smem_;
    asm volatile(".Lmega_head:");
    const unsigned L0 = (unsigned)(size_t)lds;
    const int b = blockIdx.x, xcd = b & 7, r = b >> 3;
    const int pi = (xcd & 1) * 8 + (r & 7), pj = (xcd >> 1) * 4 + (r >> 3);
    const int tid = threadIdx.x, wid = RFL(tid >> 6), lane = tid & 63;
    const int fr = lane & 15, g = lane >> 4, q4 = (lane >> 2) & 3, p4 = lane & 3;
    const __amdgpu_buffer_rsrc_t rs = ws_rsrc(ws);
    const u32x4 wr = mk_rsrc(ws, WS_END);
    const f32x4 zero4 = {0.f, 0.f, 0.f, 0.f};
    STAMP(0);
#define TAILS() const int b_ = opqs(b), wid_ = opqs(wid), rg = (b_ & 1) * 16 + ((b_ & 7) >> 1) * 4 + ((b_ >> 3) & 3), ct = b_ >> 5; \
    const int nb3 = (9 * ct) >> 1, n30 = nb3 + (ct & 1), n34 = (ct & 1) ? nb3 : nb3 + 4; \
    const int redo = (int)(((wid_ < 4 ? redtab(0) : redtab(4)) >> (16 * (wid_ & 3))) & 0xffff) << 8
#define KX 0
#define TAILS2() const int kx = (KX) * 4, kw_ = wid_ ^ kx; const unsigned sA = WS_P + (4 * rg * 72 + 9 * kw_) * 1024, sB = WS_WT + (9 * kw_ * 36 + n30) * 1024, sB4 = WS_WT + (9 * kw_ * 36 + n34) * 1024, \
    sF = WS_FP + (rg >> 1) * 512 + kw_ * 64 + (rg & 1) * 8
    u64 tag;
#define TAG_READY() do { asm volatile("s_load_dwordx2 %0, %1, 0x0\n s_waitcnt lgkmcnt(0)" : "=s"(tag) : "s"(ws + WS_E) : "memory"); tag += TAG_INC; } while (0)
#define WAIT_LB0() __builtin_amdgcn_s_waitcnt(0xC07F)
#define RD(p) (*(LAS bf16x8*)(p))
#define RDP(p, o) (*(LAS bf16x8*)(size_t)((p) + (o)))
#define TRF(p) cat8(trread(p), trread((p) + 2304))
#define TRP(p, o) TRF((LAS char*)(size_t)((p) + (o)))
#define ADV(p, d) asm("v_add_u32 %0, %1, %0" : "+v"(p) : "s"(d))
#define LDSW(o) (*(volatile LAS unsigned*)(lds + (o)))
#define FENCE() asm volatile("" ::: "memory")
#define SPIN(word, need) wcheck(0u, L0 + (word) + p4 * 4, need)
#define SGB(m, n) __builtin_amdgcn_sched_group_barrier(m, n, 0)
    const int w = wid & 3, l3 = lane >> 3;
    const unsigned ch = ((lane & 7) ^ (l3 >> 1)) * 16;
    const int bb = (q4 + 4 * (g & 1) + 16 * (g >> 1)) * 288 + p4 * 8;
    unsigned vA[4];
#define SET_VSP() do { const unsigned base = l3 * (S * 2) + ch; vA[0] = base; vA[1] = (base ^ 64) + 8 * S * 2 - 1024; vA[2] = base + 16 * S * 2 - 2048; vA[3] = (base ^ 64) + 24 * S * 2 - 3072; } while (0)
    unsigned vB[5];
#define SET_VB(i) do { const unsigned c = i < 4 ? (4 * w + i) * 64 + lane : 1024 + 32 * w + (lane & 31), rho = c / 18, cc = c - rho * 18; \
        const unsigned grow = (rho & ~12u) | ((rho & 4) << 1) | ((rho & 8) >> 1); vB[i] = grow * (F * 2) + cc * 16 - (i & 3) * 1024; } while (0)
    const unsigned lw = L0 + w * 4096, lh = L0 + 16384 + w * 512, w1 = pj * 288;
    const int rot = ROT;
#define KT(t) ((t) + rot >= 9 ? (t) + rot - 9 : (t) + rot)
#define DMA_W(dst, src) do { dma4v(rwi, lw + (dst), vB, w1 + (src)); dmah(rwi, lh + (dst), vB[4], w1 + (src)); } while (0)
#define DMA_W1(j) DMA_W(warea(j), KT(j) * (64 * F * 2))
    auto walk = [&](int i) {
        const int j = pj & 3, m = i ^ j, x = i - 1;
        const int q = (pi & ~3) | ((j + x + (x >= ((pi - j) & 3))) & 3);
        int e = (WALK & 2) && (m == 7 || m == 8) ? m ^ 15 : m;
        if (WALK & 4) { e += pj & 12; e = e >= 28 ? e - 24 : e >= 16 ? e - 12 : e; }
        return i == 0 ? pi : i < 4 ? q : pi ^ e;
    };
    u64 fpv;
    bf16x8 bq[9][5];
    bool hasconv = false;
    const int cd = w < 2 ? 2 * b + w : 512 + b;

    if (__builtin_expect(wid >= 4, 1)) {
        const u32x4 rx = mk_rsrc(x, SZ_X), rwi = mk_rsrc(w_in, D * F * 2);
        {
            const unsigned base = (8 * (l3 >> 2) + (l3 & 3)) * (D * 2) + ch;
            vA[0] = base; vA[1] = (base ^ 64) + 16 * D * 2 - 1024; vA[2] = (base ^ 64) + 4 * D * 2 - 2048; vA[3] = base + 20 * D * 2 - 3072;
        }
        const unsigned xs = (pi * 128 + 32 * w) * (D * 2);
        XST(26);
        dma4v(rx, lw, vA, xs + rot * 128);
        SCHED0();
#pragma unroll
        for (int i = 0; i < 5; ++i) {
            SET_VB(i);
            if (i < 4) dma1(i * 1024, rwi, lw + SL_A, vB[i], w1 + FH * 2 + rot * (64 * F * 2)); else dmah(rwi, lh + SL_A, vB[4], w1 + FH * 2 + rot * (64 * F * 2));
            SCHED0();
        }
        XST(27);
        TAG_READY();
        const int xi = (fr >> 2) & 1;
        const int n0 = (32 * w + 16 * xi + 4 * (fr >> 3) + (fr & 3)) * 128 + ((g ^ (2 * (fr >> 3) + ((fr >> 1) & 1)) ^ (4 * xi)) << 4), n1 = n0 ^ 64;
        const int c0 = bb + w * 64, c8 = bb + 256 + (w & 1) * 9216;
        bf16x8 ones = {0x3f80, 0x3f80, 0x3f80, 0x3f80, 0x3f80, 0x3f80, 0x3f80, 0x3f80};
        asm volatile("" : "+v"(ones));
        bf16x8 xf[9][4];
        const unsigned tag32 = (unsigned)tag;
        const unsigned go = g * 1024 + fr * 16, go2 = g == 0 ? 8192 + fr * 16 : 0x7fffff00u, tagx = g == 0 ? tag32 : 0u;
        const unsigned sr = WS_SR + pi * 12288 + w * 256, sc = WS_SC + pj * 12288 + w * 256;
#define COLSUM(base, dst) do { bf16x8 cf[5]; cf[0] = TRF((base) + c0); cf[1] = TRF((base) + c0 + 32); cf[2] = TRF((base) + c0 + 9216); cf[3] = TRF((base) + c0 + 9216 + 32); cf[4] = TRF((base) + c8); \
            const f32x4 s0_ = mfma16(ones, cf[2], mfma16(ones, cf[0], zero4)), s1_ = mfma16(ones, cf[3], mfma16(ones, cf[1], zero4)), s2_ = mfma16(ones, cf[4], zero4); \
            st16c(rs, g == 0 && pi < 9 ? fr * 16 : 0x7fffff00u, (dst) + pi * 1024, (u32x4){__float_as_uint(s0_[0]), __float_as_uint(s1_[0]), __float_as_uint(s2_[0]), tag32}); } while (0)
#define GATHER(pv, so) do { ALD("x4", pv[0], go, wr, so, " sc1"); ALD("x4", pv[1], go, wr, (so) + 4096, " sc1"); ALD("x4", pv[2], go2, wr, so, " sc1"); } while (0)
#define GBAD(pv) (__builtin_amdgcn_ballot_w64(((pv[0][3] ^ tag32) | (pv[1][3] ^ tag32) | (pv[2][3] ^ tagx)) != 0u) != 0ull)
#define GCHECK(pv, so) do { asm volatile("" : "+v"(pv[0]), "+v"(pv[1]), "+v"(pv[2])); \
            if (__builtin_expect(GBAD(pv), 0)) { unsigned sp_ = 0; \
                do { GATHER(pv, so); WAIT_V(0); asm volatile("" : "+v"(pv[0]), "+v"(pv[1]), "+v"(pv[2])); } while (GBAD(pv) && ++sp_ < SPIN_CAP); } } while (0)
#define CSUM(pv, k) adds(adds(__uint_as_float(pv[0][k]), __uint_as_float(pv[1][k])), __uint_as_float(pv[2][k]))
        u32x4 gr[3], gc[3];
        f32x2 lnc; float cs01, cs8;
        const u32x4 rsp = mk_rsrc(w_sp, S * S * 2);
        __builtin_amdgcn_s_setprio(3);
#pragma unroll
        for (int t = 0; t < 9; ++t) {
            SCHED0();
            if (t == 0) {
                asm volatile("" :: "s"(KT(1)), "s"(KT(2)), "s"(KT(3)), "s"(KT(4)), "s"(KT(5)), "s"(KT(6)), "s"(KT(7)), "s"(KT(8)), "v"(n0), "v"(c0), "v"(c8), "v"(go), "v"(go2));
                XST(28);
                dma4v(rx, lw + SLOT, vA, xs + KT(1) * 128);
                if (T1W) DMA_W(SLOT + SL_A, FH * 2 + KT(1) * (64 * F * 2));
            }
#define PA(t) ((PAM) == 1 ? (t) >= PA0 && (t) < PA0 + 4 : (PAM) == 2 ? (t) >= PA0 && (t) < PA0 + 2 : (PAM) == 3 ? (t) >= PA0 && (t) < PA0 + 3 : (PAM) == 4 ? (t) >= PA0 && (t) < PA0 + 5 : 0)
            WAIT_V((t == 0 ? (T1W ? 9 : 4) : t == 2 || t == 4 ? 10 : t == 3 ? 11 : t == 7 ? 15 : t == 8 ? 10 : 9) + PA(t - 1) + (t < 8 ? PA(t - 2) : 0));
            BAR();
            XST(40 + t);
            LAS char* sb = lds + (t & 3) * SLOT;
            if (t < 8) { xf[t][0] = RD(sb + n0); xf[t][1] = RD(sb + n1 + 1024); xf[t][2] = RD(sb + n1); xf[t][3] = RD(sb + n0 + 1024); }
            SCHED0();
            if (t == 0 && !T1W) DMA_W(SLOT + SL_A, FH * 2 + KT(1) * (64 * F * 2));
            if (t < 7) {
                dma4v(rx, lw + ((t + 2) & 3) * SLOT, vA, xs + KT(t + 2) * 128);
                DMA_W(((t + 2) & 3) * SLOT + SL_A, FH * 2 + KT(t + 2) * (64 * F * 2));
            }
            SCHED0();
            if (PA(t)) {
                const unsigned vp = (lane >> 1 & 7) * (S * 2) + (lane & 1) * 128, sp = (pi * 128 + 32 * w) * (S * 2);
                const int k = t - PA0, c = pj >> 2, j = pj & 3;
                if (PAM == 1 || (PAM == 4 && k < 4)) pf4<0>(rsp, L0 + L_DUMP, vp, sp + ((k ^ c) << 15) + walk(4) * 256);
                if (PAM == 4 && k == 4) pf4<0>(rsp, L0 + L_DUMP, vp, sp + (j << 15) + pi * 256);
                if ((PAM == 2 || PAM == 3) && k < 2) pf4<0>(rsp, L0 + L_DUMP, vp, sp + (c << 15) + (pi ^ j ^ (4 * k)) * 256);
                if (PAM == 3 && k == 2) pf4<0>(rsp, L0 + L_DUMP, vp, sp + (j << 15) + pi * 256);
                SCHED0();
            }
            if (t == 1) COLSUM(lds + SL_A, sc);
            if (t == 2) {
                f32x4 rsm[2], rq[2];
#pragma unroll
                for (int m = 0; m < 2; ++m) {
                    rsm[m] = mfma16(ones, xf[0][2 + m], mfma16(ones, xf[0][m], zero4));
                    rq[m] = mfma16(xf[0][2 + m], xf[0][2 + m], mfma16(xf[0][m], xf[0][m], zero4));
                }
#define DIAG(q_) ((fr & 2) ? ((fr & 1) ? q_[3] : q_[2]) : ((fr & 1) ? q_[1] : q_[0]))
                st16c(rs, (fr >> 2) == g && pj < 9 ? fr * 16 : 0x7fffff00u, sr + pj * 1024, rec4(rsm[0][0], DIAG(rq[0]), rsm[1][0], DIAG(rq[1]), tag32));
            }
            SCHED0();
            if (t == 6) { GATHER(gr, sr); GATHER(gc, sc); }
            if (t == 7) { DMA_W1(0); DMA_W1(2); }
            if (t == 8) {
                GCHECK(gr, sr); GCHECK(gc, sc);
                lnc = rec_consts<3>(gr, 1.0f / D);
                cs01 = half_sum(CSUM(gc, 0), CSUM(gc, 1)); cs8 = CSUM(gc, 2); cs8 = half_sum(cs8, cs8);
                asm volatile("" : "+v"(lnc), "+v"(cs01), "+v"(cs8));
            }
            if (t < 8) asm volatile("" : "+v"(xf[t][0]), "+v"(xf[t][1]), "+v"(xf[t][2]), "+v"(xf[t][3]));
        }
        {
            LAS float* p = (LAS float*)(lds + L_ROWST + (16 * w + 8 * (lane >> 5) + (fr >> 1)) * 16 + (fr & 1) * 4);
            p[0] = lnc.x; p[2] = lnc.y;
            LAS float* c = (LAS float*)(lds + L_CS) + fr * 12;
            c[2 * w + (lane >> 5)] = cs01;
            c[8 + w] = cs8;
        }
        WAIT_L0();
        BAR();
        STAMP(2);
        DMA_W1(1); DMA_W1(3); DMA_W1(4); DMA_W1(5); DMA_W1(6);
#define PF_KV() pf4<0>(mk_rsrc(w < 2 ? kin : vin, KPF ? SZ_X : 0), L0 + L_DUMP, lane * (D * 2), KR * 64 * (D * 2) + KC * 144 + (w & 1) * 140)
#define PF_HK() pf4<0>(mk_rsrc(hk + cd * S, HPF && (w < 2 || (w == 2 && b < 64)) ? S * 2 : 0), L0 + L_DUMP, (opqv(tid) & 63) * 128, 0)
        if (KVE) PF_KV();
        if (HKE == 1) PF_HK();
        if (WPF & 4) pf4<0>(w == 0 ? rsp : mk_rsrc(w_sp, 0), L0 + L_DUMP, (lane >> 1) * (S * 2) + (lane & 1) * 128, (pi * 128 + 32 * (pj & 3)) * (S * 2) + pi * 256);
        if (WPF & 1) pf4<0>(rsp, L0 + L_DUMP, (lane >> 1) * (S * 2) + (lane & 1) * 128, (pi * 128 + 32 * w) * (S * 2) + (pi ^ pj) * 256);
        if (WPF & 2) pf4<0>(rsp, L0 + L_DUMP, (lane >> 1) * (S * 2) + (lane & 1) * 128, (pi * 128 + 32 * w) * (S * 2) + (pi ^ (pj & 3)) * 256);
#define NPF ((WPF & 1) + (WPF >> 1 & 1) + (WPF >> 2 & 1) + KVE + (HKE == 1))
            unsigned vo[3];
            const int kap = opqs(b) / 3, th = b - kap * 3, kq = kap / 9, ke = kap - kq * 9, pr = ke & 3;
            const unsigned wos = th * 384 + w * 96 + (ke == 8 ? 18 * kq + 4 : 18 * kq + (ke >> 2) * 9 + 2 * pr + (pr >> 1)) * (16 * D * 2);
#pragma unroll
            for (int i = 0; i < 3; ++i) { const unsigned c = i * 64 + lane, rr = c / 6; vo[i] = (ke == 8 ? (rr >> 3 & 1) * 144 + (rr & 4) * 2 + (rr & 3) + (rr >> 4) * 4 : rr) * (D * 2) + (c - rr * 6) * 16 - i * 1024 + wos; }
            asm volatile("" : "+v"(vo[0]), "+v"(vo[1]), "+v"(vo[2]));
        WAIT_V(15 + NPF);
        LDSW(M_FL + w * 4) = 1u;
        XST(50);
        SPIN(M_FL, 1);
        XST(49);
        COLSUM(lds + warea(0), sc + 16 * 12288);
        st16c(rs, w == 3 && pj == 0 ? lane * 16 : 0x7fffff00u, WS_ST1 + pi * 1024, *(LAS u32x4*)(lds + L_ROWST + lane * 16));
        f32x4 az[2][9];
        unsigned rho = walk(lane >> 2);
        asm volatile("" : "+v"(rho));
        u64 fzv[2];
        const unsigned as0 = (pi * 128 + 32 * w) * (S * 2), zs0 = WS_Z2N + pj * 32 * SL_B + w * 4096;
        unsigned vl, fz;
        u64 sd = 0;
#define SPL(so_, k_) do { u64& sd_ = sd; asm volatile("s_buffer_load_dwordx2 %0, %1, %2 offset:%3" : "+s"(sd_) : "s"(rsp), "s"(so_), "n"(((k_) & 7) * 4096 + ((k_) >> 3) * 128 + 60) : "memory"); } while (0)
#define SPO(i_) (as0 + ((pj >> 2) << 15) + __builtin_amdgcn_readlane(rho, 4 * (i_)) * 256)
        const unsigned soz = SPO(SZW);
#define KIDX(t) (2 * __builtin_amdgcn_readlane(rho, 4 * (((t) - 18) >> 1)) + ((t) & 1))
        auto stageA = [&](int t) { dma4v(rsp, lw + (t & 3) * SLOT, vA, as0 + KIDX(t) * 128); };
        auto stageB = [&](int t) {
            const unsigned zs = zs0 + KIDX(t) * SL_B;
            dma16c4<SL_A, 0>(wr, lw + (t & 3) * SLOT, vl, zs);
            dmahc(wr, lh + (t & 3) * SLOT + SL_A, vl, zs + 16384 - w * 3584);
        };
        {
            bf16x8 wf[162];
            unsigned fw;
            unsigned wb = L0 + bb;
#define WF_B(j) ((j) < 2 || (j) >= 6 ? 0 : (j) < 5 ? warea(2) : warea(5))
#define WF_P(s) ((LAS char*)(size_t)(wb + (warea((s) / 18) - WF_B((s) / 18) + ((s) % 9) * 32 + (((s) / 9) & 1) * 9216)))
#pragma unroll
            for (int s = 0; s < ZD; ++s) wf[s] = TRF(WF_P(s));
            SCHED0();
            XST(51);
            __builtin_amdgcn_s_setprio(0);
            wcheck(0u, L0 + M_RC + w * 4, 1);
            STAMP(3);
            SFOR(s, 0, 162)
                constexpr int j = s / 18, ks = (s / 9) & 1, n = s % 9;
                if (s == 72 - ZD - 4) fw = LDSW(M_FL + p4 * 4);
                if (s == 72 - ZD) wcheck(fw, L0 + M_FL + p4 * 4, 2);
                if (s == 126 - ZD - 4) fw = LDSW(M_RC + p4 * 4);
                if (s == 126 - ZD) wcheck(fw, L0 + M_RC + p4 * 4, 2);
                if (s == 18) { xf[8][0] = RD(lds + n0); xf[8][1] = RD(lds + n1 + 1024); }
                if (s == 19) { xf[8][2] = RD(lds + n1); xf[8][3] = RD(lds + n0 + 1024); }
                if (s == 25) asm volatile("" : "+v"(xf[8][0]), "+v"(xf[8][1]), "+v"(xf[8][2]), "+v"(xf[8][3]));
                if (s == 18 || s == 36 || s == 108 || s == 126) LDSW(M_RL + w * 4) = j;
                if constexpr (s == GCS) GATHER(gc, sc + 16 * 12288);
                if constexpr ((SPF & 4) && s >= SZ0 && (s - SZ0) % SZD == 0 && (s - SZ0) / SZD < 16) SPL(soz, (s - SZ0) / SZD);
                if (s == 40) { WAIT_V(2 + NPF); LDSW(M_FL + w * 4) = 2u; }
                if (s == 140) { vl = (opqv(tid) & 63) * 16; fz = WS_FZ + pj * 512 + rho * 32 + ((vl >> 1) & 24); asm volatile("" : "+v"(fz), "+v"(vl)); }
                if (s == 146) { const unsigned l3 = vl >> 7, ch = ((vl >> 4 & 7) ^ (l3 >> 1)) * 16; vA[0] = l3 * (S * 2) + ch; asm volatile("" : "+v"(vA[0])); }
                if (s == 150) { vA[1] = (vA[0] ^ 64) + 8 * S * 2 - 1024; vA[2] = vA[0] + 16 * S * 2 - 2048; vA[3] = vA[1] + 16 * S * 2 - 2048; asm volatile("" : "+v"(vA[1]), "+v"(vA[2]), "+v"(vA[3])); }
                if (s == 18) XST(56); if (s == 42) XST(57); if (s == 54) XST(58); if (s == 107) XST(59); if (s == 109) XST(60); if (s == 121) XST(61); if (s == 133) XST(62); if (s == 135) XST(63);
                SCHED0();
                if (s < 9) asm volatile("v_mfma_f32_16x16x32_bf16 %0, %1, %2, 0" : "=&v"(az[0][n]) : "v"(wf[s]), "v"(xf[j][2 * ks]));
                else asm volatile("v_mfma_f32_16x16x32_bf16 %0, %1, %2, %0" : "+v"(az[0][n]) : "v"(wf[s]), "v"(xf[j][2 * ks]));
                SCHED0();
                bf16x4 h0, h1;
                if constexpr ((s + ZD) % 18 == 0 && s + ZD < 162 && WF_B((s + ZD) / 18) != WF_B((s + ZD) / 18 - 1)) { unsigned& wb_ = wb; asm volatile("v_add_u32 %0, %1, %0" : "+v"(wb_) : "n"(WF_B((s + ZD) / 18) - WF_B((s + ZD) / 18 - 1))); }
                if constexpr (s + ZD < 162) h0 = trread(WF_P(s + ZD));
                SCHED0();
                if (s < 9) asm volatile("v_mfma_f32_16x16x32_bf16 %0, %1, %2, 0" : "=&v"(az[1][n]) : "v"(wf[s]), "v"(xf[j][2 * ks + 1]));
                else asm volatile("v_mfma_f32_16x16x32_bf16 %0, %1, %2, %0" : "+v"(az[1][n]) : "v"(wf[s]), "v"(xf[j][2 * ks + 1]));
                SCHED0();
                if constexpr (s + ZD < 162) { h1 = trread(WF_P(s + ZD) + 2304); wf[s + ZD] = cat8(h0, h1); }
                SCHED0();
            SEND
        }
        __builtin_amdgcn_s_setprio(GPRIO);
        LDSW(M_RL + w * 4) = 9u;
        STAMP(4);
        {
        const int lane = vl >> 4, fr = lane & 15, g = lane >> 4, q4 = (lane >> 2) & 3, p4 = lane & 3, l3 = lane >> 3;
        u64 miss;
#define NEEDZ(mask) do { if ((miss & (mask)) != 0ull) { unsigned sp_ = 0; \
            do { miss = __builtin_amdgcn_ballot_w64(ldflag(rs, fz) != tag); } while ((miss & (mask)) != 0ull && ++sp_ < SPIN_CAP); } FENCE(); } while (0)
        u32x4 zp[9];
        hasconv = w < 2 || (w == 2 && b < 64);
        const unsigned fk = WS_FK + w * 512 + lane * 8;
        {
            float nmb[2], k1[2], k2[2];
#pragma unroll
            for (int m = 0; m < 2; ++m) {
                LAS float* p = (LAS float*)(lds + L_ROWST + (16 * w + 8 * m + (fr >> 1)) * 16 + (fr & 1) * 4);
                const float rd = p[2];
                nmb[m] = -p[0]; k1[m] = muls(rd, GC1); k2[m] = muls(muls(rd, rd), muls(rd, GC2));
            }
            u32x4 kl[3];
            WAIT_V(0);
            GCHECK(gc, sc + 16 * 12288);
            *(LAS float*)(lds + L_CS + ((2 * w + (lane >> 5)) * 16 + fr) * 4) = half_sum(CSUM(gc, 0), CSUM(gc, 1));
            cs8 = CSUM(gc, 2);
            *(LAS float*)(lds + L_CS + ((8 + w) * 16 + fr) * 4) = half_sum(cs8, cs8);
            asm volatile("" : "+v"(k1[0]), "+v"(k1[1]), "+v"(k2[0]), "+v"(k2[1]), "+v"(nmb[0]), "+v"(nmb[1]));
#define WTS (WOS > 4 ? WOS + 4 : 8)
#define WOLD() do { _Pragma("unroll") for (int i = 0; i < 3; ++i) dma1n(i * 1024, mk_rsrc(w_out, b < 216 ? FH * D * 2 : 0), L0 + L_WSCR + w * 3072, vo[i], 0); } while (0)
            SPIN(M_RL, 9);
            SPIN(M_RS, 1);
            STAMP(5);
            if (A21) { stageA(20); stageA(21); }
            if (WOS == -2) WOLD();
            miss = ~0ull;
            NEEDZ(0xf0ull);
            stageB(20);
            if (!A21) { stageA(20); stageA(21); }
            if (WOS == -1) WOLD();
            BAR();
            f32x4 cs_[10];
#pragma unroll
            for (int i = 0; i < 10; ++i) cs_[i] = *(LAS f32x4*)(lds + L_CS + i * 64 + g * 16);
            bf16x4 wt[6];
            kl[0] = *(LAS u32x4*)(lds + KVS + w * 1024 + vl); kl[1] = *(LAS u32x4*)(lds + KVS + 4096 + w * 1024 + vl); kl[2] = *(LAS u32x4*)(lds + KVS + 8192 + vl);
            asm volatile("" : "+v"(kl[0]), "+v"(kl[1]), "+v"(kl[2]));
            asm volatile("" : "+v"(cs_[0]), "+v"(cs_[1]), "+v"(cs_[2]), "+v"(cs_[3]), "+v"(cs_[4]), "+v"(cs_[5]), "+v"(cs_[6]), "+v"(cs_[7]), "+v"(cs_[8]), "+v"(cs_[9]));
            const unsigned so12 = SPO(12);
#define PBV 0
#define PBN (PBV == 2 || PBV == 4 ? 6 : PBV == 5 ? 1 : 4)
#define PB(u) (((u) >= PB1 && (u) < PB1 + PBN) || ((u) >= PB2 && (u) < PB2 + PBN))
#define XE(u) (((u) == 6 ? 2 : (u) == 7) + ((u) == WOS ? 3 : (u) >= WTS && (u) < WTS + 3) + ((u) == HKE) + PB(u))
            unsigned vpf = PBV == 0 ? (lane >> 1 & 7) * (S * 2) + (lane & 1) * 128 + ((pj >> 2) << 15) : PBV == 1 ? (lane >> 1 & 7) * (S * 2) + (lane & 1) * 128
                : PBV == 2 ? ((lane >> 1) % 6) * (S * 2) + (lane & 1) * 128 : PBV == 3 ? (lane >> 1 & 1) * (S * 2) + (lane & 1) * 128 + ((pj >> 2) << 15)
                : PBV == 4 ? (lane >> 1 & 1) * (S * 2) + (lane & 1) * 128 + ((pj >> 2) << 15) : (lane >> 1 & 7) * (S * 2) + (lane & 1) * 128 + ((pj >> 2) << 15);
            asm volatile("" : "+v"(vpf));
#define NW(u) ((u) == 0 ? 63 : (u) < 5 ? 9 + 3 * ((u) == WOS + 1) : (u) == 5 ? 14 : 18 + XE((u) - 1) + XE((u) - 2) + XE((u) - 3))
            SFOR(u, 0, 19)
                constexpr int n = 19 + u, t = n + 3, tb = u < 4 ? t - 1 : t;
                const unsigned la = lw + (t & 3) * SLOT, lb = lw + (tb & 3) * SLOT + SL_A, as = as0 + KIDX(t) * 128, zs = zs0 + KIDX(tb) * SL_B;
                SCHED0();
                asm volatile("s_waitcnt vmcnt(%0)\n s_barrier" :: "n"(NW(u)), "s"(la), "s"(as), "s"(zs) : "memory");
                if (u >= 1 && u < 5) { u64 nm_; asm volatile("v_cmp_ne_u64 %0, %1, %2" : "=s"(nm_) : "s"(tag), "v"(fzv[u & 1]) : "memory"); miss &= nm_; }
                if (u < 4) FLAG_ISSUE(fzv[(u + 1) & 1], fz);
                if (u == 4) { NEEDZ(~0ull); stageB(25); }
                if (u == WTS + 6 && w == 3 && lane == 0) stflag(ws + WS_FK + b * 8, tag);
                if (u == 16 && b == 0 && w == 3 && lane == 0) stflag(ws + WS_E, tag);
                if constexpr (u == 0) {
                    stageA(22);
                    stageB(21);
                    if (WOS == 0) WOLD();
                } else {
                constexpr int q = u - 1, m = q & 1, nn = q >> 1;
                if (u == WOS + 2) {
#pragma unroll
                    for (int i = 0; i < 3; ++i) { LAS char* p = lds + L_WSCR + w * 3072 + (4 * g + q4) * 96 + i * 32 + p4 * 8; wt[2 * i] = trread(p); wt[2 * i + 1] = trread(p + 1536); }
                }
                const f32x4 cc = cs_[nn];
                f32x4 a = az[m][nn];
                float u0, u1, u2, u3;
                asm volatile("s_mov_b32 m0, %15\n v_fma_f32 %4, %8, %12, %4\n buffer_load_dwordx4 %16, %20, %21 offen lds\n"
                             "v_fma_f32 %5, %9, %12, %5\n v_fma_f32 %6, %10, %12, %6\n v_fma_f32 %7, %11, %12, %7\n"
                             "buffer_load_dwordx4 %17, %20, %21 offen offset:1024 lds\n"
                             "v_mul_f32 %0, %4, %4\n v_mul_f32 %1, %5, %5\n v_mul_f32 %2, %6, %6\n v_mul_f32 %3, %7, %7\n"
                             "buffer_load_dwordx4 %18, %20, %21 offen offset:2048 lds\n"
                             "v_fma_f32 %0, %0, %14, %13\n v_fma_f32 %1, %1, %14, %13\n v_fma_f32 %2, %2, %14, %13\n v_fma_f32 %3, %3, %14, %13\n"
                             "buffer_load_dwordx4 %19, %20, %21 offen offset:3072 lds\n"
                             "v_mul_f32 %0, %0, %4\n v_mul_f32 %1, %1, %5\n v_mul_f32 %2, %2, %6\n v_mul_f32 %3, %3, %7"
                             : "=&v"(u0), "=&v"(u1), "=&v"(u2), "=&v"(u3), "+v"(a[0]), "+v"(a[1]), "+v"(a[2]), "+v"(a[3])
                             : "v"(cc[0]), "v"(cc[1]), "v"(cc[2]), "v"(cc[3]), "v"(nmb[m]), "v"(k1[m]), "v"(k2[m]),
                               "s"(la), "v"(vA[0]), "v"(vA[1]), "v"(vA[2]), "v"(vA[3]), "s"(rsp), "s"(as) : "memory");
                if (u < 4) NEEDZ(3ull << (2 * (tb - 18)));
                asm volatile("s_mov_b32 m0, %8\n v_exp_f32 %0, %0\n buffer_load_dwordx4 %9, %10, %11 offen sc1 lds\n"
                             "v_exp_f32 %1, %1\n v_exp_f32 %2, %2\n v_exp_f32 %3, %3\n"
                             "buffer_load_dwordx4 %9, %10, %11 offen offset:1024 sc1 lds\n"
                             "v_add_f32 %0, 1.0, %0\n v_add_f32 %1, 1.0, %1\n v_add_f32 %2, 1.0, %2\n v_add_f32 %3, 1.0, %3\n"
                             "buffer_load_dwordx4 %9, %10, %11 offen offset:2048 sc1 lds\n"
                             "v_rcp_f32 %0, %0\n v_rcp_f32 %1, %1\n v_rcp_f32 %2, %2\n v_rcp_f32 %3, %3\n"
                             "buffer_load_dwordx4 %9, %10, %11 offen offset:3072 sc1 lds\n"
                             "v_mul_f32 %0, %0, %4\n v_mul_f32 %1, %1, %5\n v_mul_f32 %2, %2, %6\n v_mul_f32 %3, %3, %7\n"
                             "s_mov_b32 m0, %12\n s_mov_b32 exec_hi, 0\n buffer_load_dwordx4 %9, %10, %13 offen sc1 lds\n s_mov_b32 exec_hi, -1\n"
                             "v_cvt_pk_bf16_f32 %0, %0, %1\n v_cvt_pk_bf16_f32 %1, %2, %3"
                             : "+v"(u0), "+v"(u1), "+v"(u2), "+v"(u3)
                             : "v"(a[0]), "v"(a[1]), "v"(a[2]), "v"(a[3]), "s"(lb), "v"(vl), "s"(wr), "s"(zs), "s"(lh + (tb & 3) * SLOT + SL_A), "s"(zs + 16384 - w * 3584) : "memory");
                if (u == WOS) WOLD();
                if (u >= WTS && u < WTS + 3) st16c(rs, b < 216 ? vl : 0x7fffff00u, WS_WT + (b * 12 + w * 3 + u - WTS) * 1024, __builtin_bit_cast(u32x4, cat8(wt[2 * (u - WTS) % 6], wt[(2 * (u - WTS) + 1) % 6])));
                if (u == WOS + 2) asm volatile("" : "+v"(wt[0]), "+v"(wt[1]), "+v"(wt[2]), "+v"(wt[3]), "+v"(wt[4]), "+v"(wt[5]));
                if constexpr ((SPF & 2) && u >= SP2 && u < SP2 + 16) SPL(so12, u - SP2);
                if constexpr (u == HKE && u >= 6) PF_HK();
                if constexpr (PB(u)) { constexpr int k = u - (u < PB2 ? PB1 : PB2);
                    pf4<0>(rsp, L0 + L_DUMP, PBV < 2 ? vpf ^ (k << 15) : PBV == 2 ? vpf + (k == 5 ? 26 : k * 6) * (S * 2) : PBV == 3 ? vpf + k * 2 * (S * 2) : PBV == 4 ? vpf + (k < 4 ? k * 2 : (k - 4) * 2 + 8) * (S * 2) : vpf, as0 + __builtin_amdgcn_readlane(rho, u < PB2 ? 32 : 48) * 256); }
                if (u == 6 || u == 7) {
                    const unsigned so = WS_KVT + (KC * 72 * S + KR * 64) * 2, ko_ = l3 * (S * 2) + ((lane - (l3 >> 2) - 2 * w) & 7) * 16;
                    if (u == 6) st16c(rs, ko_, so + w * (8 * S * 2), kl[0]);
                    if (u == 6) st16c(rs, ko_, so + (w + 4) * (8 * S * 2), kl[1]);
                    if (u == 7) st16c(rs, w == 0 ? ko_ : 0x7fffff00u, so + 64 * S * 2, kl[2]);
                }
                if (q == 15) { cs_[8][0] = adds(cs_[8][0], cs_[9][0]); cs_[8][1] = adds(cs_[8][1], cs_[9][1]); cs_[8][2] = adds(cs_[8][2], cs_[9][2]); cs_[8][3] = adds(cs_[8][3], cs_[9][3]); }
                const int n4 = nn - (nn > 4), zq = nn == 4 ? 8 : m * 4 + (n4 >> 1), h = nn == 4 ? m : n4 & 1;
                zp[zq][h * 2] = __float_as_uint(u0); zp[zq][h * 2 + 1] = __float_as_uint(u1);
                asm volatile("" : "+v"(zp[zq]));
                }
            SEND
        }
        SCHED0();
        if (!SPF) STAMP(6);
#define TST(i) do { } while (0)
#pragma unroll 1
        for (int n = 38; n < 42; ++n) {
            WAIT_V(18);
            BAR();
            stageA(n + 3); stageB(n + 3);
        }
        asm volatile("s_waitcnt lgkmcnt(0)" : "+s"(sd) :: "memory");
        FLAG_ISSUE(fzv[0], fk);
#pragma unroll 1
        for (int n = 42; n < 44; ++n) {
            WAIT_V(19);
            BAR();
            stageA(n + 3); stageB(n + 3);
        }
        WAIT_V(18);
        asm volatile("" : "+v"(fzv[0]));
        if (!allset(fzv[0], tag)) spin_flags(rs, fk, tag);
        FENCE();
        TAILS(); TAILS2();
        ConvIn cin;
        BAR();
        TST(44);
        stageA(47); stageB(47);
        WAIT_V(18);
        BAR();
        TST(45);
        stageA(48); stageB(48);
        WAIT_V(18);
        BAR();
        TST(46);
        stageA(49); stageB(49);
        WAIT_V(18);
        BAR();
        TST(47);
        if (hasconv) conv_load(cin, hk, dv, wr, cd, lane); else G3_LDB(0);
        SCHED0();
        TST(49);
#pragma unroll
        for (int q = 0; q < 8; ++q) *(LAS u32x4*)(lds + ZS + w * 8192 + q * 1024 + lane * 16) = zp[q];
        *(LAS u32x4*)(lds + Z8OFF(w) + lane * 16) = zp[8];
        SCHED0();
        TST(50);
        if (hasconv) WAIT_V(12); else WAIT_V(5);
        WAIT_L0();
        TST(51);
        BAR();
        TST(48);
        if (hasconv) {
            __builtin_amdgcn_s_setprio(VPRIO);
            G3_LDB(0);
            SCHED0();
            WAIT_V(5);
            CONV_PIN(cin);
            if (w == 2) SPIN(M_RC, 3);
            STAMP(7);
            conv_stage(cin, lds + redo, lane, [&](auto i_) { G3_LDB(decltype(i_)::v + 1); });
            __builtin_amdgcn_s_setprio(0);
        } else {
            __builtin_amdgcn_s_setprio(0);
            G3_LDB(1); G3_LDB(2); G3_LDB(3); G3_LDB(4);
        }
        SCHED0();
        STAMP(8);
        if (hasconv) {
            if (CVD) wcheck(0u, L0 + M_RC + w * 4, 5);
            conv_run(cin.dd, rs, lds + redo, cd, lane);
            STAMP(9);
            FP_ISSUE(); G3_DMA1(redo, sB, sB4);
            WAIT_V(9);
            if (lane == 0) stflag(ws + WS_FY + cd * 8, tag);
            XST(29);
            G3_DMA2(redo, sB, sB4);
            STAMP(20);
            FP_CHECK(19);
        } else {
            SPIN(M_RC, RGW);
#pragma unroll 1
            for (int v = 5 - w; v < (b < 64 ? 4 : 6 - w); ++v) G3_DMA(redoff(0) + v * RED_W, sB + (v - 4 - w + 2 * kx) * (9 * KST), sB4 + (v - 4 - w + 2 * kx) * (9 * KST));
            G3_DMA(redo, sB, sB4);
            WAIT_V(18);
            LDSW(M_FL + w * 4) = 4u;
            if (XPF && w == 3) {
                pf4<0>(rx, L0 + L_DUMP, lane * (D * 2), rg * (64 * D * 2) + ct * 144); pf4<140>(rx, L0 + L_DUMP, lane * (D * 2), rg * (64 * D * 2) + ct * 144);
                pf4c<WS_ST1>(wr, L0 + L_DUMP, lane * 8, rg * 512);
            }
            for (int i_ = 0; i_ < PSL; ++i_) __builtin_amdgcn_s_sleep(16);
            STAMP(20);
            FP_ISSUE(); FP_CHECK(0);
        }
        }
    } else {
        if (tid < 16) *(LAS unsigned*)(lds + L_MISC + tid * 4) = 0u;
        {
            const char* pc; unsigned len, len2, back;
            asm volatile("s_getpc_b64 %0\n.Lpf_here%=:\n s_mov_b32 %1, .Lmega_tail+1024-.Lmega_head\n s_mov_b32 %2, .Lpf_here%=-.Lmega_head\n s_mov_b32 %3, .Lmega_cend-.Lmega_head" : "=s"(pc), "=s"(len), "=s"(back), "=s"(len2));
            const u32x4 rc = mk_rsrc(pc - back, len > len2 ? len : len2);
            if (wid < CPFW) pf4<0>(rc, L0 + L_DUMP, ((r * CPFW + wid) & 7) * 8192 + lane * 128, 0);
        }
        u32x4 zp[9];
        unsigned bsw[4];
        const int wm = w & 1, wn = w >> 1;
#pragma unroll
        for (int m = 0; m < 4; ++m) asm volatile("buffer_load_ushort %0, %1, %2, %3 offen" : "=v"(bsw[m]) : "v"(fr * 2), "s"(mk_rsrc(b_sp, S * 2)), "s"((pi * 128 + 64 * wm + 16 * ((m + 2 * wn) & 3)) * 2) : "memory");
#pragma unroll
        for (int i = 0; i < 5; ++i) SET_VB(i);
        SET_VSP();
        const u32x4 rsp = mk_rsrc(w_sp, S * S * 2);
        const unsigned as0 = (pi * 128 + 32 * w) * (S * 2);
        asm volatile("" : "+v"(vB[0]), "+v"(vB[1]), "+v"(vB[2]), "+v"(vB[3]), "+v"(vB[4]), "+v"(vA[0]), "+v"(vA[1]), "+v"(vA[2]), "+v"(vA[3]));
        unsigned ok[3], ov[3], kd8, kvo, krd;
        {
#pragma unroll
            for (int i = 0; i < 3; ++i) {
                const unsigned c = i * 64 + lane, rr = c / 9, o = (KR * 64 + 16 * w + rr) * (D * 2) + KC * 144 + (c - rr * 9) * 16 - i * 1024;
                ok[i] = i == 2 && rr >= 19 ? 0x7fffff00u : o - 3 * (D * 2);
                ov[i] = i == 2 && rr >= 16 ? 0x7fffff00u : o;
            }
            const int ln = lane < 36 ? lane : 35, sg = ln >= 18, kd = ln - sg * 18;
            kvo = L0 + KVS + kd * 512 + ((2 * w + sg + kd) & 7) * 16; kd8 = kd * 32 + KC * 576; krd = sg * 1152 + kd * 8;
            asm volatile("" : "+v"(ok[0]), "+v"(ok[1]), "+v"(ok[2]), "+v"(ov[0]), "+v"(ov[1]), "+v"(ov[2]), "+v"(kd8), "+v"(kvo), "+v"(krd));
        }
        u32x4 sk[2];
        {
            f32x4 az[2][9];
#pragma unroll
            for (int n = 0; n < 9; ++n) az[0][n] = az[1][n] = zero4;
            unsigned e0 = L0 + (32 * w + fr) * 128 + ((g ^ (fr >> 1)) << 4), e1 = e0 ^ 64, pw = L0 + SL_A + bb;
            bf16x8 ax[2], bx[9], ay[2], by[9];
#define ZRD_X() do { ax[0] = RDP(e0, 0); ax[1] = RDP(e1, 2048); _Pragma("unroll") for (int n = 0; n < 9; ++n) bx[n] = TRP(pw, n * 32); } while (0)
#define ZRD_Y() do { ay[0] = RDP(e1, 0); ay[1] = RDP(e0, 2048); _Pragma("unroll") for (int n = 0; n < 9; ++n) by[n] = TRP(pw, n * 32 + 9216); } while (0)
#define ZMM(A, B, n0_, n1_) do { _Pragma("unroll") for (int n = n0_; n < n1_; ++n) { az[0][n] = mfma16(A[0], B[n], az[0][n]); az[1][n] = mfma16(A[1], B[n], az[1][n]); } } while (0)
#define ZSCHED(rest) do { _Pragma("unroll") for (int i_ = 0; i_ < 6; ++i_) { SGB(0x008, 1); SGB(0x100, 2); } _Pragma("unroll") for (int i_ = 0; i_ < 8; ++i_) { SGB(0x008, 1); SGB(0x100, 1); } if (rest) SGB(0x008, rest); SCHED0(); } while (0)
            XST(26);
            TAG_READY();
            XST(27);
            __builtin_amdgcn_s_setprio(CPRIO);
            BAR();
            STAMP(1);
            ZRD_X();
            WAIT_LB0();
#pragma unroll 1
            for (int t = 0; t < 8; ++t) {
                const int dl = (t & 3) == 3 ? -3 * SLOT : SLOT;
                ZRD_Y();
                ZMM(ax, bx, 0, 7);
                ZSCHED(0);
                ADV(e0, dl); ADV(e1, dl); ADV(pw, dl);
                ZMM(ax, bx, 7, 9); ZMM(ay, by, 0, 2);
                SCHED0();
                WAIT_LB0();
                BAR();
                ZRD_X();
                ZMM(ay, by, 2, 9);
                ZSCHED(0);
            }
            ZRD_Y();
            ZMM(ax, bx, 0, 7);
            ZSCHED(0);
            ZMM(ax, bx, 7, 9); ZMM(ay, by, 0, 2);
            SCHED0();
            WAIT_LB0();
            BAR();
            f32x4 st[4], cq[3];
#pragma unroll
            for (int j = 0; j < 4; ++j) st[j] = *(LAS f32x4*)(lds + L_ROWST + (16 * w + 4 * g + j) * 16);
#pragma unroll
            for (int j = 0; j < 3; ++j) cq[j] = *(LAS f32x4*)(lds + L_CS + fr * 48 + j * 16);
            ZMM(ay, by, 2, 9);
            SCHED_IL(7, 14);
            STAMP(2);
            f32x2 sum[4], sq[4], k1[4], k2[4];
            cq[2][0] += cq[2][1];
#pragma unroll
            for (int j = 0; j < 4; ++j) { const f32x2 rd = hi2(st[j]); k1[j] = rd * GC1; k2[j] = rd * rd * rd * GC2; }
#pragma unroll
            for (int n = 0; n < 9; ++n) {
                const float cc = -((n & 1) ? __uint_as_float(movs(__float_as_uint(cq[n >> 2][n & 3]))) : cq[n >> 2][n & 3]);
#pragma unroll
                for (int j = 0; j < 4; ++j) {
                    f32x4& a = az[j >> 1][n];
                    const f32x2 v = fma2(lo2(st[j]), (f32x2){cc, cc}, (j & 1) ? hi2(a) : lo2(a));
                    const f32x2 e = v * fma2(v * v, k2[j], k1[j]);
                    const f32x2 d = (f32x2){__builtin_amdgcn_exp2f(e.x), __builtin_amdgcn_exp2f(e.y)} + (f32x2){1.0f, 1.0f};
                    const f32x2 z = v * (f32x2){__builtin_amdgcn_rcpf(d.x), __builtin_amdgcn_rcpf(d.y)};
                    a[(j & 1) * 2] = z.x; a[(j & 1) * 2 + 1] = z.y;
                    if (n == 0) { sum[j] = z; sq[j] = z * z; } else { sum[j] += z; sq[j] = fma2(z, z, sq[j]); }
                }
            }
#pragma unroll
            for (int j = 0; j < 4; ++j) { const f32x2 rd = hi2(st[j]); sum[j] *= rd; sq[j] *= rd * rd; asm volatile("" : "+v"(sum[j]), "+v"(sq[j])); }
            SCHED0();
            LDSW(M_RC + w * 4) = 1u;
            SCHED0();
            float r0 = sum[0].x, r1 = sq[0].x, r2 = sum[0].y, r3 = sq[0].y, r4 = sum[1].x, r5 = sq[1].x, r6 = sum[1].y, r7 = sq[1].y;
            asm volatile(
                "s_nop 1\n"
                "v_add_f32_dpp %0, %0, %0 row_mirror row_mask:0xf bank_mask:0x3\n v_add_f32_dpp %1, %1, %1 row_mirror row_mask:0xf bank_mask:0x3\n"
                "v_add_f32_dpp %2, %2, %2 row_mirror row_mask:0xf bank_mask:0x3\n v_add_f32_dpp %3, %3, %3 row_mirror row_mask:0xf bank_mask:0x3\n"
                "v_add_f32_dpp %4, %4, %4 row_mirror row_mask:0xf bank_mask:0x3\n v_add_f32_dpp %5, %5, %5 row_mirror row_mask:0xf bank_mask:0x3\n"
                "v_add_f32_dpp %6, %6, %6 row_mirror row_mask:0xf bank_mask:0x3\n v_add_f32_dpp %7, %7, %7 row_mirror row_mask:0xf bank_mask:0x3\n"
                "v_add_f32_dpp %0, %8, %8 row_mirror row_mask:0xf bank_mask:0xc\n v_add_f32_dpp %1, %9, %9 row_mirror row_mask:0xf bank_mask:0xc\n"
                "v_add_f32_dpp %2, %10, %10 row_mirror row_mask:0xf bank_mask:0xc\n v_add_f32_dpp %3, %11, %11 row_mirror row_mask:0xf bank_mask:0xc\n"
                "v_add_f32_dpp %4, %12, %12 row_mirror row_mask:0xf bank_mask:0xc\n v_add_f32_dpp %5, %13, %13 row_mirror row_mask:0xf bank_mask:0xc\n"
                "v_add_f32_dpp %6, %14, %14 row_mirror row_mask:0xf bank_mask:0xc\n v_add_f32_dpp %7, %15, %15 row_mirror row_mask:0xf bank_mask:0xc\n"
                "v_add_f32_dpp %0, %0, %0 row_half_mirror row_mask:0xf bank_mask:0x5\n v_add_f32_dpp %1, %1, %1 row_half_mirror row_mask:0xf bank_mask:0x5\n"
                "v_add_f32_dpp %2, %2, %2 row_half_mirror row_mask:0xf bank_mask:0x5\n v_add_f32_dpp %3, %3, %3 row_half_mirror row_mask:0xf bank_mask:0x5\n"
                "v_add_f32_dpp %0, %4, %4 row_half_mirror row_mask:0xf bank_mask:0xa\n v_add_f32_dpp %1, %5, %5 row_half_mirror row_mask:0xf bank_mask:0xa\n"
                "v_add_f32_dpp %2, %6, %6 row_half_mirror row_mask:0xf bank_mask:0xa\n v_add_f32_dpp %3, %7, %7 row_half_mirror row_mask:0xf bank_mask:0xa\n"
                "v_add_f32_dpp %0, %0, %0 quad_perm:[1,0,3,2] row_mask:0xf bank_mask:0xf\n v_add_f32_dpp %1, %1, %1 quad_perm:[1,0,3,2] row_mask:0xf bank_mask:0xf\n"
                "v_add_f32_dpp %2, %2, %2 quad_perm:[1,0,3,2] row_mask:0xf bank_mask:0xf\n v_add_f32_dpp %3, %3, %3 quad_perm:[1,0,3,2] row_mask:0xf bank_mask:0xf\n"
                "v_add_f32_dpp %0, %0, %0 quad_perm:[2,3,0,1] row_mask:0xf bank_mask:0xf\n v_add_f32_dpp %1, %1, %1 quad_perm:[2,3,0,1] row_mask:0xf bank_mask:0xf\n"
                "v_add_f32_dpp %2, %2, %2 quad_perm:[2,3,0,1] row_mask:0xf bank_mask:0xf\n v_add_f32_dpp %3, %3, %3 quad_perm:[2,3,0,1] row_mask:0xf bank_mask:0xf\n"
                : "+v"(r0), "+v"(r1), "+v"(r2), "+v"(r3), "+v"(r4), "+v"(r5), "+v"(r6), "+v"(r7)
                : "v"(sum[2].x), "v"(sq[2].x), "v"(sum[2].y), "v"(sq[2].y), "v"(sum[3].x), "v"(sq[3].x), "v"(sum[3].y), "v"(sq[3].y));
            const unsigned tag32 = (unsigned)tag, so2 = WS_ST2 + pi * 16384 + w * 256;
            st16c(rs, p4 == 0 ? lane * 4 : 0x7fffff00u, so2 + pj * 1024, rec4(r0, r1, r2, r3, tag32));
            SCHED0();
            STAMP(3);
            {
                u32x4 pv[4];
                const u32x4 rwi = mk_rsrc(w_in, D * F * 2);
                SPIN(M_RL, 1);
                DMA_W1(7);
#define KVLD() do { _Pragma("unroll") for (int i = 0; i < 3; ++i) dma1(i * 1024, mk_rsrc(vin, SZ_X), L0 + L_WSCR + w * 3072, ov[i], 0); \
                    asm volatile("buffer_load_dwordx4 %0, %2, %3, 0 offen\n buffer_load_dwordx4 %1, %2, %3, 0 offen offset:16" : "=&v"(sk[0]), "=&v"(sk[1]) : "v"(kd8), "s"(mk_rsrc(shk, D * 8)) : "memory"); } while (0)
#define KKLD() do { _Pragma("unroll") for (int i = 0; i < 3; ++i) dma1(i * 1024, mk_rsrc(kin, SZ_X), lw, ok[i], 0); } while (0)
                if (KS1 == 0) KVLD();
                SPIN(M_RL, 2);
                DMA_W1(8);
                if (KS1 == 0) KKLD();
                const unsigned po = g * 1024 + fr * 16;
#pragma unroll
                for (int i = 0; i < 4; ++i) ALD("x4", pv[i], po, wr, so2 + i * 4096, " sc1");
                if (KS1 == 1) { KVLD(); KKLD(); }
                WAIT_V(KS1 == 1 ? 8 : 0);
                LDSW(M_RC + w * 4) = 2u;
                unsigned spins = 0;
                for (;;) {
                    asm volatile("" : "+v"(pv[0]), "+v"(pv[1]), "+v"(pv[2]), "+v"(pv[3]));
                    const unsigned bad = ((pv[0][3] ^ tag32) | (pv[1][3] ^ tag32)) | ((pv[2][3] ^ tag32) | (pv[3][3] ^ tag32));
                    if (__builtin_amdgcn_ballot_w64(bad != 0u) == 0ull || ++spins > SPIN_CAP) break;
#pragma unroll
                    for (int i = 0; i < 4; ++i) ALD("x4", pv[i], po, wr, so2 + i * 4096, " sc1");
                    WAIT_V(0);
                }
                STAMP(5);
                const f32x2 c = rec_consts<4>(pv, 1.0f / FH);
                LAS float* p = (LAS float*)(lds + L_ZST + w * 256 + fr * 16 + (lane >> 5) * 4);
                p[0] = muls(c.y, *(LAS float*)(lds + L_ROWST + (16 * w + fr) * 16 + 8 + (lane >> 5) * 4)); p[2] = muls(-c.x, c.y);
            }
            FENCE();
            const unsigned zdst = WS_Z2N + ((pj * 32 + pi * 2 + (w >> 1)) * 18 + (w & 1)) * 1024;
#pragma unroll
            for (int j = 0; j < 4; ++j) st[j] = *(LAS f32x4*)(lds + L_ZST + w * 256 + (4 * g + j) * 16);
#pragma unroll
            for (int n = 0; n < 9; ++n) {
#pragma unroll
                for (int j = 0; j < 4; ++j) { const f32x4 a = az[j >> 1][n]; zp[n][j] = norm2s((j & 1) ? hi2(a) : lo2(a), st[j]); }
                st16c(rs, lane * 16, zdst + n * 2048, zp[n]);
                SCHED0();
            }
        }
        STAMP(6);
        {
            u32x2 kw[11], vw[8];
            WAIT_V(9);
            asm volatile("" : "+v"(sk[0]), "+v"(sk[1]));
#pragma unroll
            for (int i = 0; i < 11; ++i) kw[i] = *(LAS u32x2*)(lds + w * 4096 + krd + i * 144);
#pragma unroll
            for (int i = 0; i < 8; ++i) vw[i] = *(LAS u32x2*)(lds + L_WSCR + w * 3072 + krd + i * 144);
            SCHED0();
            SPIN(M_RL, 6);
#pragma unroll
            for (int n = 0; n < 9; ++n) *(LAS u32x4*)(lds + (w < 2 ? T18 : T19) + SL_A + n * 2048 + (w & 1) * 1024 + lane * 16) = zp[n];
            SCHED0();
#define HALF(w_, c) (((c) & 1) ? bfhi(w_) : bflo(w_))
#define KVC(c) do { SCHED0(); asm volatile("" : "+v"(sk[c >> 1])); \
                const float h[4] = {bflo(sk[c >> 1][(c & 1) * 2]), bfhi(sk[c >> 1][(c & 1) * 2]), bflo(sk[c >> 1][(c & 1) * 2 + 1]), bfhi(sk[c >> 1][(c & 1) * 2 + 1])}; \
                float kk[11], o[8]; \
                _Pragma("unroll") for (int i = 0; i < 11; ++i) kk[i] = HALF(kw[i][c >> 1], c); \
                _Pragma("unroll") for (int i = 0; i < 8; i += 2) tap4d(o[i], o[i + 1], kk + i, h, HALF(vw[i][c >> 1], c), HALF(vw[i + 1][c >> 1], c)); \
                *(LAS u32x4*)(size_t)(kvo + c * 128) = (u32x4){pk2(o[7], o[6]), pk2(o[5], o[4]), pk2(o[3], o[2]), pk2(o[1], o[0])}; SCHED0(); } while (0)
            asm volatile("" : "+v"(kw[0]), "+v"(kw[1]), "+v"(kw[2]), "+v"(kw[3]), "+v"(kw[4]), "+v"(kw[5]), "+v"(kw[6]), "+v"(kw[7]), "+v"(kw[8]), "+v"(kw[9]), "+v"(kw[10]));
            asm volatile("" : "+v"(vw[0]), "+v"(vw[1]), "+v"(vw[2]), "+v"(vw[3]), "+v"(vw[4]), "+v"(vw[5]), "+v"(vw[6]), "+v"(vw[7]));
            if (FE == 2) { dma4v(rsp, lw + T18, vA, as0 + pi * 256); dma4v(rsp, lw + T19, vA, as0 + pi * 256 + 128); }
            WAIT_V(FE == 2 ? 8 : 0);
            if (lane == 0) stflag(ws + WS_FZ + ((pj * 16 + pi) * 4 + w) * 8, tag);
            LDSW(M_RS + w * 4) = 1u;
            STAMP(7);
            if (FE == 1) { dma4v(rsp, lw + T18, vA, as0 + pi * 256); dma4v(rsp, lw + T19, vA, as0 + pi * 256 + 128); }
            __builtin_amdgcn_s_setprio(KPRIO);
            SPIN(M_RL, 7);
            KVC(0); KVC(1); KVC(2); KVC(3);
            __builtin_amdgcn_s_setprio(CPRIO);
        }
        STAMP(4);
        asm volatile("" : "+v"(bsw[0]), "+v"(bsw[1]), "+v"(bsw[2]), "+v"(bsw[3]));
        f32x4 ac[4][4], a4[2];
#pragma unroll
        for (int m = 0; m < 4; ++m) {
            const float bs = bflo(bsw[m]);
#pragma unroll
            for (int l = 0; l < 4; ++l) ac[m][l] = (f32x4){bs, bs, bs, bs};
            if (m < 2) a4[m] = (f32x4){bs, bs, bs, bs};
            asm volatile("" : "+v"(ac[m][0]), "+v"(ac[m][1]), "+v"(ac[m][2]), "+v"(ac[m][3]));
        }
        asm volatile("" : "+v"(a4[0]), "+v"(a4[1]));
        const int swz = (g ^ (fr >> 1)) << 4;
        unsigned x01 = L0 + T18 + (64 * wm + 32 * wn + fr) * 128 + swz, x23 = L0 + T18 + (64 * wm + 32 * (1 - wn) + fr) * 128 + swz, y01 = x01 ^ 64, y23 = x23 ^ 64;
        unsigned pb = L0 + T18 + SL_A + 5 * wn * 2048 + lane * 16, p4_ = L0 + T18 + SL_A + 4 * 2048 + lane * 16;
        bf16x8 ax[4], bx[5], ay[4], by[5];
#define MMA_L(B, A, l) do { _Pragma("unroll") for (int m = 0; m < 4; ++m) ac[m][l] = mfma16(B[l], A[m], ac[m][l]); } while (0)
#define MMA_4(B, A) do { a4[0] = mfma16(B[4], A[0], a4[0]); a4[1] = mfma16(B[4], A[1], a4[1]); } while (0)
#define RDX_B() do { bx[0] = RDP(pb, 0); bx[1] = RDP(pb, 2048); bx[2] = RDP(pb, 4096); bx[3] = RDP(pb, 6144); } while (0)
#define RDX_A() do { ax[0] = RDP(x01, 0); ax[1] = RDP(x01, 2048); ax[2] = RDP(x23, 0); ax[3] = RDP(x23, 2048); bx[4] = RDP(p4_, 0); } while (0)
#define RDY() do { by[0] = RDP(pb, 1024); ay[0] = RDP(y01, 0); ay[1] = RDP(y01, 2048); ay[2] = RDP(y23, 0); ay[3] = RDP(y23, 2048); \
        by[1] = RDP(pb, 3072); by[2] = RDP(pb, 5120); by[3] = RDP(pb, 7168); by[4] = RDP(p4_, 1024); } while (0)
        asm volatile("" : "+v"(x01), "+v"(x23), "+v"(y01), "+v"(y23), "+v"(pb), "+v"(p4_));
        SCHED0();
        STAMP(8);
        WAIT_V(FE == 1 ? 4 : 5);
        WAIT_L0();
        BAR();
        STAMP(9);
        RDX_B();
        RDX_A();
        int e0 = T19 - T18 - SLOT, e1 = 3 * SLOT - T19;
#define CSTEP(sync) do { \
            RDY(); \
            MMA_L(bx, ax, 0); MMA_L(bx, ax, 1); MMA_L(bx, ax, 2); \
            SCHED_IL(9, 12); \
            ADV(x01, dl); ADV(x23, dl); ADV(y01, dl); ADV(y23, dl); ADV(pb, dl); ADV(p4_, dl); \
            MMA_L(bx, ax, 3); \
            SGB(0x002, 2); SGB(0x008, 1); SGB(0x002, 2); SGB(0x008, 1); SGB(0x002, 2); SGB(0x008, 2); \
            SCHED0(); \
            sync; \
            RDX_B(); \
            MMA_4(bx, ax); MMA_L(by, ay, 0); \
            SCHED_IL(4, 6); \
            asm volatile("" : "+v"(a4[0]), "+v"(a4[1])); \
            RDX_A(); \
            MMA_L(by, ay, 1); MMA_L(by, ay, 2); MMA_L(by, ay, 3); MMA_4(by, ay); \
            SCHED_IL(5, 14); } while (0)
#pragma unroll 1
        for (int t = 18; t < 48; ++t) {
            const int dl = ((t & 3) == 3 ? -3 * SLOT : SLOT) + e0;
            e0 = opqs(e1); e1 = 0;
            CSTEP(__builtin_amdgcn_s_waitcnt(FE ? 0x0070 : 0x0071); BAR());
        }
        {
            const int dl = SLOT;
            CSTEP(LDSW(M_RC + w * 4) = 3u; SCHED0());
        }
        STAMP(10);
        RDY();
#pragma unroll
        for (int q = 0; q < 8; ++q) zp[q] = *(LAS u32x4*)(lds + ZS + ((4 * wm + (((q >> 1) + 2 * wn) & 3)) * 4 + 2 * wn + (q & 1)) * 1024 + lane * 16);
        zp[8] = *(LAS u32x4*)(lds + Z8OFF(2 * wm + wn) + lane * 16);
        MMA_L(bx, ax, 0); MMA_L(bx, ax, 1); MMA_L(bx, ax, 2); MMA_L(bx, ax, 3); MMA_4(bx, ax);
        SCHED_IL(18, 18);
        LDSW(M_RC + w * 4) = 4u;
        SCHED0();
        MMA_L(by, ay, 0); MMA_L(by, ay, 1); MMA_L(by, ay, 2); MMA_L(by, ay, 3); MMA_4(by, ay);
        SCHED0();
        STAMP(11);
        {
            const int kap0 = 9 * (pj >> 1) + 4 * (pj & 1) + 2 * wn, kap4 = 9 * (pj >> 1) + 8;
#pragma unroll
            for (int lp = 0; lp < 2; ++lp)
#pragma unroll
                for (int m = 0; m < 4; ++m) {
                    const unsigned dst = WS_P + (pi * 8 + 4 * wm + ((m + 2 * wn) & 3)) * (72 * 1024);
                    const u32x4 z = zp[m * 2 + lp];
                    const f32x4 a = ac[m][2 * lp], c = ac[m][2 * lp + 1];
                    st16c(rs, PSTO(lane * 16), dst + (kap0 + lp) * 1024, (u32x4){mulz(z[0], lo2(a)), mulz(z[1], hi2(a)), mulz(z[2], lo2(c)), mulz(z[3], hi2(c))});
                    SCHED0();
                }
#pragma unroll
            for (int m = 0; m < 2; ++m)
                st8c(rs, PSTO((lane >> 5) * 256 + fr * 16 + (g & 1) * 8), WS_P + (pi * 8 + 4 * wm + 2 * wn + m) * (72 * 1024) + kap4 * 1024 + (pj & 1) * 512, (u32x2){mulz(zp[8][2 * m], lo2(a4[m])), mulz(zp[8][2 * m + 1], hi2(a4[m]))});
        }
        SCHED0();
        if (CVD || RGW == 5) LDSW(M_RC + w * 4) = 5u;
        STAMP(12);
        TAILS(); TAILS2();
#pragma unroll
        for (int i = 0; i < CPRE; ++i) G3_LDB(i);
        SCHED0();
        WAIT_V(5 * CPRE);
        if (lane == 0) stflag(ws + WS_FP + ((pi * 16 + pj) * 4 + w) * 8, tag);
        __builtin_amdgcn_s_setprio(0);
        STAMP(13);
        SCHED0();
#pragma unroll
        for (int i = CPRE; i < RB; ++i) G3_LDB(i);
        SCHED0();
        if (w < 2) { SPIN(M_RC, 4); G3_DMA(redo, sB, sB4); }
        FP_ISSUE();
        STAMP(20);
        FP_CHECK(0);
        WAIT_VB(0);
        asm volatile(".Lmega_cend:");
    }
    {
        const int lane = opqv(tid) & 63, fr = lane & 15, g = lane >> 4;
        TAILS(); TAILS2();
        f32x4 acc[4][5];
        if (LPRIO) { if (wid >= 4) __builtin_amdgcn_s_setprio(LPRIO); }
        STAMP(21);
#define OUTPOS() const int lane = opqv(tid) & 63, fr = lane & 15, g = lane >> 4, tA = 64 * rg + 16 * wid + fr, dA = 16 * n30 + 4 * g, dH = 16 * n34 + 8 * (ct & 1) + 4 * (g & 1)
        unsigned fy;
        u32x4 fyv, stv4;
#define LOADY() do { const int ln_ = opqv(tid) & 63; unsigned t_; \
        asm volatile("s_mov_b32 m0, %1\n s_add_i32 %0, %4, 0x1fc00\n buffer_load_dwordx4 %2, %3, %4 offen sc1 lds\n buffer_load_dwordx4 %2, %3, %0 offen offset:1024 sc1 lds\n" \
                     "s_add_i32 %0, %4, 0x3f800\n s_mov_b64 exec, 0xffff\n buffer_load_dwordx4 %2, %3, %0 offen offset:2048 sc1 lds\n s_mov_b64 exec, -1" \
                     : "=&s"(t_) : "s"(L0 + redo + wid * 4608), "v"((ln_ >> 1) * (S * 2) + (ln_ & 1) * 16), "s"(wr), "s"(RFL(WS_YT + (72 * ct * S + 64 * rg + 16 * wid) * 2)) : "scc", "memory"); } while (0)
        LAS char* red = lds + redo;
#define PUT(mm) do { _Pragma("unroll") for (int l = 0; l < 4; ++l) *(LAS f32x4*)(red + MG(mm) * 4608 + l * 1024 + lane * 16) = acc[mm][l]; } while (0)
#define PUTH(mm) (*(LAS f32x4*)(red + MG(mm) * 4608 + 4096 + (lane & 31) * 16) = acc[mm][4])
        {
            bf16x8 aq[9][4];
            unsigned fw;
            const int fwo = wid_ == 2 || (wid_ == 3 && b_ < 64) ? M_FL + 12 : wid_ == 3 ? M_FL + 8 : M_RC;
            G3_LDA(0); G3_LDA(1); G3_LDA(2); G3_LDA(3);
            SCHED0();
#pragma unroll
            for (int i = 0; i < 9; ++i) {
                if (i == 8) {
                    asm volatile("" : "+v"(aq[8][0]), "+v"(aq[8][1]), "+v"(aq[8][2]), "+v"(aq[8][3]), "+v"(bq[8][0]), "+v"(bq[8][1]), "+v"(bq[8][2]), "+v"(bq[8][3]), "+v"(bq[8][4]) :: "memory");
                    if (wid < 4) {
                        const unsigned ln = opqv(tid) & 63;
                        fy = WS_FY + 576 * ct + (ln < 35 ? ln : 35) * 16; fyv = ld16c(rs, fy, 0);
                        stv4 = ld16c(rs, (ln & 14) * 8, WS_ST1 + (64 * rg + 16 * wid) * 8);
                        unsigned xo[3];
#pragma unroll
                        for (int j = 0; j < 3; ++j) { const unsigned c = ln + 64 * j; xo[j] = c * 16 + (c * 57 >> 9) * (D * 2 - 144) - j * 1024; }
                        asm volatile("s_mov_b32 m0, %0\n s_nop 0\n buffer_load_dwordx4 %1, %4, %5 offen lds\n buffer_load_dwordx4 %2, %4, %5 offen offset:1024 lds\n"
                                     "s_mov_b64 exec, 0xffff\n buffer_load_dwordx4 %3, %4, %5 offen offset:2048 lds\n s_mov_b64 exec, -1"
                                     :: "s"(L0 + redo + wid * 4608 + 2304), "v"(xo[0]), "v"(xo[1]), "v"(xo[2]), "s"(mk_rsrc(x, SZ_X)), "s"(RFL((64 * rg + 16 * wid) * (D * 2) + 144 * ct)) : "memory");
                    }
                    SCHED0();
                }
#pragma unroll
                for (int m = 0; m < 4; ++m) {
#pragma unroll
                    for (int l = 0; l < 5; ++l) acc[m][l] = mfma16(bq[i][l], aq[i][m], i == 0 ? zero4 : acc[m][l]);
                    if (WIL && i == 8 && m > 0) { PUT(m - 1); _Pragma("unroll") for (int l = 0; l < 4; ++l) { SGB(0x008, 1); SGB(0x200, 1); } SGB(0x008, 1); }
                    if (WIL && i == 8) SCHED0();
                }
                SCHED0();
                XST(30 + i);
                if (2 * i + 4 < 9) G3_LDA(2 * i + 4);
                if (2 * i + 5 < 9) G3_LDA(2 * i + 5);
                if (i == 2) fw = LDSW(fwo);
                if (i == 3) wcheck(fw, L0 + fwo, 4);
                if (i >= 3 && i + 2 < 9) G3_RDB(i + 2);
                SCHED0();
            }
        }
        STAMP(22);
        if (!WIL) { PUT(0); PUT(1); PUT(2); }
        if ((g >> 1) == (ct & 1)) { PUTH(0); PUTH(1); PUTH(2); }
        if (wid < 4) {
            STAMP(23);
            f32x4* o = acc[3];
            FENCE();
            LDSW(M_RC + wid * 4) = 6u;
            asm volatile("s_waitcnt vmcnt(0)" : "+v"(fyv), "+v"(stv4) :: "memory");
            OUTPOS();
            u32x2 xv[5];
#pragma unroll
            for (int u = 0; u < 5; ++u) xv[u] = *(LAS u32x2*)(red + wid * 4608 + 2304 + fr * 144 + (u < 4 ? (ct & 1) * 16 + u * 32 + g * 8 : ((ct & 1) ? 0 : 128) + (g & 1) * 8));
            for (unsigned sp = 0; sp < SPIN_CAP; ++sp) {
                if (__builtin_amdgcn_ballot_w64(((fyv[0] ^ (unsigned)tag) | (fyv[1] ^ (unsigned)(tag >> 32)) | (fyv[2] ^ (unsigned)tag) | (fyv[3] ^ (unsigned)(tag >> 32))) != 0u) == 0ull) break;
                FENCE();
                fyv = ld16c(rs, fy, 0);
            }
            FENCE();
            LOADY();
            XST(39);
            const float mb = __uint_as_float((fr & 1) ? stv4[1] : stv4[0]), rd = __uint_as_float((fr & 1) ? stv4[3] : stv4[2]);
            f32x4 xx[5], sr[5];
#pragma unroll
            for (int u = 0; u < 5; ++u)
#pragma unroll
                for (int h = 0; h < 2; ++h) {
                    const float x0 = bflo(xv[u][h]), x1 = bfhi(xv[u][h]);
                    unsigned q = pk2(adds(x0, -mb), adds(x1, -mb));
                    q = pk2(muls(bflo(q), rd), muls(bfhi(q), rd));
                    xx[u][2 * h] = x0; xx[u][2 * h + 1] = x1;
                    sr[u][2 * h] = muls(bflo(q), rd); sr[u][2 * h + 1] = muls(bfhi(q), rd);
                }
            unsigned r0 = L0 + wid * 4608 + lane * 16, r7 = r0 + redoff(7), h0 = r0 + 4096 - (ct & 1) * 512, h7 = h0 + redoff(7);
            asm volatile("" : "+v"(r0), "+v"(r7), "+v"(h0), "+v"(h7));
#define PART(v, u) (*(LAS f32x4*)(size_t)((v) == 6 ? ((u) < 4 ? r0 + (redoff(6) + (u) * 1024) : h0 + redoff(6)) : (u) < 4 ? r7 + (redoff(v) - redoff(7) + (u) * 1024) : h7 + (redoff(v) - redoff(7))))
            unsigned oo = (tA * D + dA) * 2, oh = (g >> 1) == (ct & 1) ? (tA * D + dH) * 2 : 0x7fffff00u;
            float ke = muls(rd, -1.4426950408889634f);
            SCHED0();
            wcheck(0u, L0 + M_RC + (lane & 3) * 4, 6);
#pragma unroll
            for (int v = 1; v < 4; ++v) {
                const unsigned ro = redoff(0) + ((wid + v) & 3) * RED_W, a0 = r0 + ro, a4 = h0 + ro;
                f32x4 pp[5];
#pragma unroll
                for (int u = 0; u < 5; ++u) pp[u] = *(LAS f32x4*)(size_t)(u < 4 ? a0 + u * 1024 : a4);
#pragma unroll
                for (int u = 0; u < 5; ++u)
#pragma unroll
                    for (int rr = 0; rr < 4; ++rr) o[u][rr] = adds(o[u][rr], pp[u][rr]);
            }
            SCHED0();
            WAIT_V(0);
            {
                LAS char* yb = red + wid * 4608 + lane * 8;
#pragma unroll
                for (int u = 0; u < 5; ++u) {
                    const u32x2 yw = __builtin_bit_cast(u32x2, trread(u < 4 ? yb + (ct & 1) * 256 + u * 512 : yb + ((ct & 1) ? -256 : 2048)));
#pragma unroll
                    for (int h = 0; h < 2; ++h) { sr[u][2 * h] = muls(sr[u][2 * h], bflo(yw[h])); sr[u][2 * h + 1] = muls(sr[u][2 * h + 1], bfhi(yw[h])); }
                }
            }
#pragma unroll
            for (int u = 0; u < 5; ++u) asm volatile("" : "+v"(o[u]), "+v"(xx[u]), "+v"(sr[u]));
            asm volatile("" : "+v"(oo), "+v"(oh), "+v"(ke));
            XST(40);
            asm volatile("s_barrier\n.Lmega_tail:" ::: "memory");
            STAMP(24);
            f32x4 pp[5][4];
#pragma unroll
            for (int u = 0; u < 5; ++u)
#pragma unroll
                for (int v = 0; v < 4; ++v) { pp[u][v] = PART(v + 4, u); SCHED0(); }
#pragma unroll
            for (int u = 0; u < 5; ++u) {
                const f32x4 z = o[u] + ((pp[u][0] + pp[u][1]) + (pp[u][2] + pp[u][3]));
                u32x2 res;
#pragma unroll
                for (int h = 0; h < 2; ++h) {
                    const f32x2 one = {1.0f, 1.0f};
                    f32x2 zz = h ? hi2(z) : lo2(z);
                    const f32x2 s2 = h ? hi2(sr[u]) : lo2(sr[u]), x2 = h ? hi2(xx[u]) : lo2(xx[u]);
                    const f32x2 e = zz * (f32x2){ke, ke};
                    const f32x2 dn = (f32x2){__builtin_amdgcn_exp2f(e.x), __builtin_amdgcn_exp2f(e.y)} + one;
                    res[h] = pk2v(fma2(zz * s2, (f32x2){__builtin_amdgcn_rcpf(dn.x), __builtin_amdgcn_rcpf(dn.y)}, x2));
                }
                __builtin_amdgcn_raw_buffer_store_b64(res, in_rsrc(out, SZ_X), u < 4 ? oo + 32 * u : oh, 0, OAUX);
            }
        } else {
            PUT(3);
            if ((g >> 1) == (ct & 1)) PUTH(3);
            STAMP(23);
            asm volatile("s_waitcnt lgkmcnt(0)\n s_barrier" ::: "memory");
            STAMP(24);
        }
    }
}

extern "C" void kernel_launch(void* const* d_in, const int* in_sizes, int n_in,
                              void* d_out, int out_size, void* d_ws, size_t ws_size,
                              hipStream_t stream) {
    (void)hipFuncSetAttribute((const void*)mega, hipFuncAttributeMaxDynamicSharedMemorySize, LDS_SZ);
    mega<<<256, 512, LDS_SZ, stream>>>((const u16*)d_in[0], (const u16*)d_in[1], (const u16*)d_in[2], (const u16*)d_in[3], (const u16*)d_in[4], (const u16*)d_in[5],
                                       (const u16*)d_in[7], (const u16*)d_in[8], (const u16*)d_in[9], (const u16*)d_in[10], (char*)d_ws, (u16*)d_out);
}
```

```cpp
#include <hip/hip_runtime.h>
#include <stdint.h>
#define CODE_PF

#define LAS __attribute__((address_space(3)))
#define GAS __attribute__((address_space(1)))
typedef unsigned short u16;
typedef unsigned long long u64;
typedef short bf16x8 __attribute__((ext_vector_type(8)));
typedef short bf16x4 __attribute__((ext_vector_type(4)));
typedef float f32x4 __attribute__((ext_vector_type(4)));
typedef float f32x2 __attribute__((ext_vector_type(2)));
typedef unsigned u32x4 __attribute__((ext_vector_type(4)));
typedef unsigned u32x2 __attribute__((ext_vector_type(2)));
typedef __bf16 bf16x2_t __attribute__((ext_vector_type(2)));

constexpr int S = 2048, D = 576, F = 4608, FH = 2304;

__device__ __forceinline__ float bflo(unsigned w) { return __uint_as_float(w << 16); }
__device__ __forceinline__ float bfhi(unsigned w) { return __uint_as_float(w & 0xffff0000u); }
__device__ __forceinline__ unsigned pk2v(f32x2 p) { return __builtin_bit_cast(unsigned, __builtin_convertvector(p, bf16x2_t)); }
__device__ __forceinline__ unsigned pk2(float lo, float hi) { return pk2v((f32x2){lo, hi}); }
__device__ __forceinline__ f32x2 up2(unsigned w) { return (f32x2){bflo(w), bfhi(w)}; }
__device__ __forceinline__ float rbf(float x) { return bflo(pk2(x, x)); }
__device__ __forceinline__ f32x2 rbf2(f32x2 x) { return up2(pk2v(x)); }
__device__ __forceinline__ f32x2 fma2(f32x2 a, f32x2 b, f32x2 c) { return __builtin_elementwise_fma(a, b, c); }
__device__ __forceinline__ f32x2 lo2(f32x4 a) { return (f32x2){a[0], a[1]}; }
__device__ __forceinline__ f32x2 hi2(f32x4 a) { return (f32x2){a[2], a[3]}; }
__device__ __forceinline__ f32x4 mfma16(bf16x8 a, bf16x8 b, f32x4 c) { return __builtin_amdgcn_mfma_f32_16x16x32_bf16(a, b, c, 0, 0, 0); }
__device__ __forceinline__ bf16x4 trread(LAS char* p) { return __builtin_bit_cast(bf16x4, __builtin_amdgcn_ds_read_tr16_b64_v4i16((LAS bf16x4*)p)); }
__device__ __forceinline__ bf16x8 cat8(bf16x4 a, bf16x4 b) { return __builtin_shufflevector(a, b, 0, 1, 2, 3, 4, 5, 6, 7); }
#define WAIT_V(n) asm volatile("s_waitcnt vmcnt(%0)" ::"n"(n) : "memory")
#define WAIT_VB(n) __builtin_amdgcn_s_waitcnt(((n) & 15) | (7 << 4) | (15 << 8) | (((n) >> 4) << 14))
#define WAIT_L0() asm volatile("s_waitcnt lgkmcnt(0)" ::: "memory")
#define BAR() do { asm volatile("" ::: "memory"); __builtin_amdgcn_s_barrier(); asm volatile("" ::: "memory"); } while (0)
#define SCHED0() __builtin_amdgcn_sched_barrier(0)
#define RLX_AGENT __ATOMIC_RELAXED, __HIP_MEMORY_SCOPE_AGENT
#define PIN9(z) asm volatile("" : "+v"(z[0]), "+v"(z[1]), "+v"(z[2]), "+v"(z[3]), "+v"(z[4]), "+v"(z[5]), "+v"(z[6]), "+v"(z[7]), "+v"(z[8]))
__device__ __forceinline__ int opqs(int x) { asm volatile("" : "+s"(x)); return x; }
__device__ __forceinline__ int opqv(int x) { asm volatile("" : "+v"(x)); return x; }
template <int I> struct IC { static constexpr int v = I; };
template <int A, int B, class Fn> __device__ __forceinline__ void sfor(Fn&& f) { if constexpr (A < B) { f(IC<A>{}); sfor<A + 1, B>(f); } }
#define SFOR(i, a, b) sfor<a, b>([&](auto i##_) __attribute__((always_inline)) { constexpr int i = decltype(i##_)::v;
#define SEND });
#define RFL(x) __builtin_amdgcn_readfirstlane(x)

#define TIMER "s_memrealtime"
#define STAMP(i) do { } while (0)

__device__ __forceinline__ f32x2 gelu2(f32x2 v) {
    const float c1 = -2.0f * 0.7978845608028654f * 1.4426950408889634f;
    const float c2 = c1 * 0.044715f;
    f32x2 a = v * fma2(v * v, (f32x2){c2, c2}, (f32x2){c1, c1});
    f32x2 d = (f32x2){__builtin_amdgcn_exp2f(a.x), __builtin_amdgcn_exp2f(a.y)} + (f32x2){1.0f, 1.0f};
    return v * (f32x2){__builtin_amdgcn_rcpf(d.x), __builtin_amdgcn_rcpf(d.y)};
}
__device__ __forceinline__ float adds(float a, float b) { float o; asm("v_add_f32 %0, %1, %2" : "=v"(o) : "v"(a), "v"(b)); return o; }
__device__ __forceinline__ float muls(float a, float b) { float o; asm("v_mul_f32 %0, %1, %2" : "=v"(o) : "v"(a), "v"(b)); return o; }
__device__ __forceinline__ f32x2 ln_consts(float sm, float sq, float inv_n) {
    float mean, m2;
    asm("v_mul_f32 %0, %2, %3\n v_mul_f32 %1, %2, %4" : "=&v"(mean), "=v"(m2) : "s"(inv_n), "v"(sm), "v"(sq));
    const float var = fmaxf(fmaf(-mean, mean, m2), 0.f);
    return (f32x2){rbf(mean), __builtin_amdgcn_rcpf(rbf(__builtin_amdgcn_sqrtf(rbf(rbf(var) + 1e-5f))))};
}

#define FPS 1
constexpr unsigned SZ_Z = S * FH * 2, SZ_X = S * D * 2;
constexpr unsigned WS_Z2N = 0;
constexpr unsigned WS_P = WS_Z2N + SZ_Z;
constexpr unsigned WS_KVT = WS_P + SZ_Z;
constexpr unsigned WS_YT = WS_KVT + SZ_X;
constexpr unsigned WS_WT = WS_YT + SZ_X;
constexpr unsigned WS_ST2 = WS_WT + FH * D * 2;
constexpr unsigned WS_ST1 = WS_ST2 + 256 * 1024;
constexpr unsigned WS_SR = WS_ST1 + 16384;
constexpr unsigned WS_SC = WS_SR + 16 * 12288;
constexpr unsigned WS_FZ = WS_SC + 32 * 12288;
constexpr unsigned WS_FP = WS_FZ + 8192;
constexpr unsigned WS_FK = WS_FP + 8192 * FPS;
constexpr unsigned WS_FY = WS_FK + 2048;
constexpr unsigned WS_E = WS_FY + 4608 + 128;
constexpr unsigned WS_END = WS_E + 128;
constexpr u64 TAG_INC = 0x9E3779B97F4A7C15ull;

__device__ __forceinline__ __amdgpu_buffer_rsrc_t ws_rsrc(char* ws) { return __builtin_amdgcn_make_buffer_rsrc(ws, 0, (int)WS_END, 0x00020000); }
__device__ __forceinline__ void st16c(__amdgpu_buffer_rsrc_t r, unsigned off, unsigned soff, u32x4 v) { __builtin_amdgcn_raw_buffer_store_b128(v, r, off, soff, 16); asm volatile("s_nop 1" :: "v"(v)); }
__device__ __forceinline__ void st8c(__amdgpu_buffer_rsrc_t r, unsigned off, unsigned soff, u32x2 v) { __builtin_amdgcn_raw_buffer_store_b64(v, r, off, soff, 16); }
__device__ __forceinline__ u32x4 ld16c(__amdgpu_buffer_rsrc_t r, unsigned off, unsigned soff) { return __builtin_amdgcn_raw_buffer_load_b128(r, off, soff, 16); }
__device__ __forceinline__ unsigned ld4c(__amdgpu_buffer_rsrc_t r, unsigned off) { return __builtin_amdgcn_raw_buffer_load_b32(r, off, 0, 16); }
__device__ __forceinline__ unsigned ld2c(__amdgpu_buffer_rsrc_t r, unsigned off, unsigned soff) { return __builtin_amdgcn_raw_buffer_load_b16(r, off, soff, 16); }
__device__ __forceinline__ __amdgpu_buffer_rsrc_t in_rsrc(const void* p, unsigned bytes) { return __builtin_amdgcn_make_buffer_rsrc((void*)p, 0, (int)bytes, 0x00020000); }
__device__ __forceinline__ u32x4 mk_rsrc(const void* p, unsigned bytes) { const u64 a = (u64)p; return (u32x4){(unsigned)a, (unsigned)(a >> 32) & 0xffffu, bytes, 0x00020000u}; }
#define DMA_HEAD "s_add_i32 m0, %1, %5\n s_add_i32 %0, %4, %6\n buffer_load_dwordx4 %2, %3, %0 offen"
#define DMA_NEXT(o) "\n buffer_load_dwordx4 %2, %3, %0 offen offset:" #o " sc1 lds"
#define DMA_OPS : "=&s"(t) : "s"(lb), "v"(voff), "s"(r), "s"(sb), "n"(LI), "n"(SI) : "scc", "memory"
template <int LI, int SI> __device__ __forceinline__ void dma16(u32x4 r, unsigned lb, unsigned voff, unsigned sb) { unsigned t; asm volatile(DMA_HEAD " lds" DMA_OPS); }
template <int LI, int SI> __device__ __forceinline__ void dma16c(u32x4 r, unsigned lb, unsigned voff, unsigned sb) { unsigned t; asm volatile(DMA_HEAD " sc1 lds" DMA_OPS); }
template <int LI, int SI> __device__ __forceinline__ void dma16c3(u32x4 r, unsigned lb, unsigned voff, unsigned sb) { unsigned t; asm volatile(DMA_HEAD " sc1 lds" DMA_NEXT(1024) DMA_NEXT(2048) DMA_OPS); }
template <int LI, int SI> __device__ __forceinline__ void dma16c4(u32x4 r, unsigned lb, unsigned voff, unsigned sb) { unsigned t; asm volatile(DMA_HEAD " sc1 lds" DMA_NEXT(1024) DMA_NEXT(2048) DMA_NEXT(3072) DMA_OPS); }
template <int SI> __device__ __forceinline__ void pf4(u32x4 r, unsigned lb, unsigned voff, unsigned sb) {
    unsigned t;
    asm volatile("s_mov_b32 m0, %1\n s_add_i32 %0, %4, %5\n buffer_load_dword %2, %3, %0 offen lds" : "=&s"(t) : "s"(lb), "v"(voff), "s"(r), "s"(sb), "n"(SI) : "scc", "memory");
}
template <int SI> __device__ __forceinline__ void pf4c(u32x4 r, unsigned lb, unsigned voff, unsigned sb) {
    unsigned t;
    asm volatile("s_mov_b32 m0, %1\n s_add_i32 %0, %4, %5\n buffer_load_dword %2, %3, %0 offen sc1 lds" : "=&s"(t) : "s"(lb), "v"(voff), "s"(r), "s"(sb), "n"(SI) : "scc", "memory");
}
__device__ __forceinline__ void dma4v(u32x4 r, unsigned lb, const unsigned* v, unsigned sb) {
    asm volatile("s_mov_b32 m0, %0\n s_nop 0\n buffer_load_dwordx4 %1, %5, %6 offen lds\n buffer_load_dwordx4 %2, %5, %6 offen offset:1024 lds\n"
                 "buffer_load_dwordx4 %3, %5, %6 offen offset:2048 lds\n buffer_load_dwordx4 %4, %5, %6 offen offset:3072 lds"
                 :: "s"(lb), "v"(v[0]), "v"(v[1]), "v"(v[2]), "v"(v[3]), "s"(r), "s"(sb) : "memory");
}
__device__ __forceinline__ void dmah(u32x4 r, unsigned lb, unsigned v, unsigned sb) {
    asm volatile("s_mov_b32 m0, %0\n s_mov_b32 exec_hi, 0\n buffer_load_dwordx4 %1, %2, %3 offen lds\n s_mov_b32 exec_hi, -1" :: "s"(lb), "v"(v), "s"(r), "s"(sb) : "memory");
}
__device__ __forceinline__ void dmahc(u32x4 r, unsigned lb, unsigned v, unsigned sb) {
    asm volatile("s_mov_b32 m0, %0\n s_mov_b32 exec_hi, 0\n buffer_load_dwordx4 %1, %2, %3 offen sc1 lds\n s_mov_b32 exec_hi, -1" :: "s"(lb), "v"(v), "s"(r), "s"(sb) : "memory");
}
__device__ __forceinline__ u64 ldflag(__amdgpu_buffer_rsrc_t r, unsigned off) {
    asm volatile("" ::: "memory");
    return __builtin_bit_cast(u64, __builtin_amdgcn_raw_buffer_load_b64(r, off, 0, 16));
}
__device__ __forceinline__ void stflag(char* p, u64 v) { __hip_atomic_store((GAS u64*)p, v, RLX_AGENT); }
__device__ __forceinline__ bool allset(u64 v, u64 tag) { return __builtin_amdgcn_ballot_w64(v != tag) == 0ull; }

constexpr int SL_A = 16384, SL_B = 18432, SLOT = SL_A + SL_B;
constexpr unsigned SPIN_CAP = 1u << 20;
constexpr int L_ROWST = 145408;
constexpr int L_CS = L_ROWST + 1024;
constexpr int L_ZST = L_CS + 1024;
constexpr int L_WSCR = L_ZST + 1024;
constexpr int LDS_SZ = 163840;
constexpr int L_DUMP = LDS_SZ - 256;
constexpr int L_MISC = L_DUMP - 256;
constexpr int M_RL = L_MISC, M_FL = L_MISC + 16, M_RC = L_MISC + 32, M_RS = L_MISC + 48;
constexpr int CV_U = 512 + 4096, CV_HSTRIDE = 4160, CV_WAVE = CV_U + 4 * CV_HSTRIDE;
constexpr int RED_W = 18432, ZS = 2 * SLOT;
__host__ __device__ constexpr int redoff(int v) { return v < 4 ? 256 + CV_WAVE + v * RED_W : v == 7 ? 256 + CV_WAVE + 4 * RED_W : v == 6 ? 256 : (v == 4 ? L_MISC : 4 * SLOT) - CV_WAVE; }
#define Z8OFF(w_) (ZS + 32768 + (w_) * 1024 + ((w_) >> 1) * SLOT)
constexpr u64 redtab(int v) { return (u64)(redoff(v) >> 8) | (u64)(redoff(v + 1) >> 8) << 16 | (u64)(redoff(v + 2) >> 8) << 32 | (u64)(redoff(v + 3) >> 8) << 48; }
#define NWA 1
__host__ __device__ constexpr int warea(int j) { return 16384 + 18432 * (j == 0 || j == 7 ? 1 : j == 1 || j == 8 ? 2 * NWA : j == 2 ? 3 : j == 3 ? 5 : j == 4 ? 4 : j == 5 ? 6 : 2 - 2 * NWA); }
constexpr int T18 = warea(2), T19 = warea(3), KVS = NWA ? L_WSCR : warea(6);
typedef unsigned u32x3 __attribute__((ext_vector_type(3)));

#define SCHED_IL(nr, nm) do { _Pragma("unroll") for (int i_ = 0; i_ < (nr); ++i_) { __builtin_amdgcn_sched_group_barrier(0x008, 1, 0); __builtin_amdgcn_sched_group_barrier(0x100, 1, 0); } \
        __builtin_amdgcn_sched_group_barrier(0x008, (nm) - (nr), 0); SCHED0(); } while (0)

__device__ __forceinline__ void spin_flags(__amdgpu_buffer_rsrc_t r, unsigned p, u64 tag) {
    unsigned spins = 0;
    while (!allset(ldflag(r, p), tag)) { if (++spins > SPIN_CAP) break; __builtin_amdgcn_s_sleep(1); }
    asm volatile("" ::: "memory");
}
__device__ __forceinline__ void dma1(int OFF, u32x4 r, unsigned lb, unsigned v, unsigned sb) {
    asm volatile("s_mov_b32 m0, %0\n s_nop 0\n buffer_load_dwordx4 %1, %2, %3 offen offset:%4 lds" :: "s"(lb), "v"(v), "s"(r), "s"(sb), "n"(OFF) : "memory");
}
#define WNT " nt"
#define KNT ""
__device__ __forceinline__ void dma1n(int OFF, u32x4 r, unsigned lb, unsigned v, unsigned sb) {
    asm volatile("s_mov_b32 m0, %0\n s_nop 0\n buffer_load_dwordx4 %1, %2, %3 offen offset:%4" WNT " lds" :: "s"(lb), "v"(v), "s"(r), "s"(sb), "n"(OFF) : "memory");
}
__device__ __forceinline__ void dma1c(int OFF, u32x4 r, unsigned lb, unsigned v, unsigned sb) {
    asm volatile("s_mov_b32 m0, %0\n s_nop 0\n buffer_load_dwordx4 %1, %2, %3 offen offset:%4 sc1 lds" :: "s"(lb), "v"(v), "s"(r), "s"(sb), "n"(OFF) : "memory");
}
__device__ __forceinline__ u32x4 rec4(float a, float b, float c, float d, unsigned tag) {
    const unsigned q = __float_as_uint(d);
    return (u32x4){__builtin_amdgcn_perm(__float_as_uint(a), q, 0x07060501u), __builtin_amdgcn_perm(__float_as_uint(b), q, 0x07060502u), __builtin_amdgcn_perm(__float_as_uint(c), q, 0x07060503u), tag};
}
__device__ __forceinline__ float half_sum(float a, float b) {
    asm("s_nop 1\n v_permlane32_swap_b32 %0, %1\n v_add_f32 %0, %0, %1\n v_mov_b32 %1, %0\n s_nop 1\n v_permlane16_swap_b32 %0, %1\n v_add_f32 %0, %0, %1" : "+v"(a), "+v"(b));
    return a;
}
template <int N> __device__ __forceinline__ f32x2 rec_consts(const u32x4* pv, float inv_n) {
    float a0, a1, a2, a3;
#pragma unroll
    for (int i = 0; i < N; ++i) {
        const float e = __uint_as_float(__builtin_amdgcn_perm(pv[i][2], __builtin_amdgcn_perm(pv[i][1], pv[i][0], 0x0c04000cu), 0x04020100u));
        if (i == 0) { a0 = __uint_as_float(pv[i][0]); a1 = __uint_as_float(pv[i][1]); a2 = __uint_as_float(pv[i][2]); a3 = e; }
        else { a0 = adds(a0, __uint_as_float(pv[i][0])); a1 = adds(a1, __uint_as_float(pv[i][1])); a2 = adds(a2, __uint_as_float(pv[i][2])); a3 = adds(a3, e); }
    }
    return ln_consts(half_sum(a0, a2), half_sum(a1, a3), inv_n);
}
__device__ __forceinline__ void wcheck(unsigned f, unsigned addr, unsigned need) {
    unsigned cnt;
    asm volatile("v_cmp_gt_u32 vcc, %3, %0\n s_cbranch_vccz 2f\n s_mov_b32 %1, 0x400000\n"
                 "1:\n s_sleep 1\n ds_read_b32 %0, %2\n s_sub_u32 %1, %1, 1\n s_waitcnt lgkmcnt(0)\n s_cbranch_scc1 2f\n v_cmp_gt_u32 vcc, %3, %0\n s_cbranch_vccnz 1b\n2:"
                 : "+v"(f), "=&s"(cnt) : "v"(addr), "n"(need) : "vcc", "scc", "memory");
}
__device__ __forceinline__ unsigned norm2s(f32x2 z, f32x4 st) {
    unsigned o, t;
    asm("v_fma_f32 %0, %2, %4, %6\n v_fma_f32 %1, %3, %5, %7\n v_cvt_pk_bf16_f32 %0, %0, %1" : "=&v"(o), "=&v"(t) : "v"(z.x), "v"(z.y), "v"(st[0]), "v"(st[1]), "v"(st[2]), "v"(st[3]));
    return o;
}
__device__ __forceinline__ void tap4d(float& o0, float& o1, const float* k, const float* h, float v0, float v1) {
    asm("v_mul_f32 %0, %2, %10\n v_mul_f32 %1, %3, %10\n v_fma_f32 %0, %3, %9, %0\n v_fma_f32 %1, %4, %9, %1\n v_fma_f32 %0, %4, %8, %0\n v_fma_f32 %1, %5, %8, %1\n"
        "v_fma_f32 %0, %5, %7, %0\n v_fma_f32 %1, %6, %7, %1\n v_mul_f32 %0, %0, %11\n v_mul_f32 %1, %1, %12"
        : "=&v"(o0), "=&v"(o1) : "v"(k[0]), "v"(k[1]), "v"(k[2]), "v"(k[3]), "v"(k[4]), "v"(h[0]), "v"(h[1]), "v"(h[2]), "v"(h[3]), "v"(v0), "v"(v1));
}

__device__ __forceinline__ float fmas(float a, float b, float c) { float o; asm("v_fma_f32 %0, %1, %2, %3" : "=v"(o) : "v"(a), "v"(b), "v"(c)); return o; }
__device__ __forceinline__ unsigned mulz(unsigned z, f32x2 a) {
    unsigned o, t;
    asm("v_lshlrev_b32 %0, 16, %2\n v_and_b32 %1, 0xffff0000, %2\n v_mul_f32 %0, %0, %3\n v_mul_f32 %1, %1, %4\n v_cvt_pk_bf16_f32 %0, %0, %1" : "=&v"(o), "=&v"(t) : "v"(z), "v"(a.x), "v"(a.y));
    return o;
}
__device__ __forceinline__ unsigned movs(unsigned a) { unsigned o; asm("v_mov_b32 %0, %1" : "=v"(o) : "v"(a)); return o; }
__device__ __forceinline__ unsigned algn(unsigned hi, unsigned lo) { unsigned o; asm("v_alignbit_b32 %0, %1, %2, 16" : "=v"(o) : "v"(hi), "v"(lo)); return o; }
__device__ __forceinline__ unsigned algn2(unsigned hi, unsigned lo) { unsigned o; asm("v_alignbit_b32 %0, %1, %2, 16 " : "=v"(o) : "v"(hi), "v"(lo)); return o; }

#define YPRIO 0
struct ConvIn { u32x4 uu[4], hh[4]; u32x2 hn[4]; unsigned dd; };
#define ALD(w_, dst, vo, r_, so, x_) asm volatile("buffer_load_dword" w_ " %0, %1, %2, %3 offen" x_ : "=v"(dst) : "v"(vo), "s"(r_), "s"(so) : "memory")
__device__ __forceinline__ void conv_load(ConvIn& c, const u16* __restrict__ hk, const u16* __restrict__ dv, u32x4 wr, int d, int lane) {
    const u32x4 rh = mk_rsrc(hk, SZ_X);
    const unsigned v = lane * 16, su = WS_KVT + d * (S * 2), sh = d * (S * 2);
    ALD("x4", c.uu[0], v, wr, su, " sc1"); ALD("x4", c.uu[1], v, wr, su, " offset:1024 sc1"); ALD("x4", c.uu[2], v, wr, su, " offset:2048 sc1"); ALD("x4", c.uu[3], v, wr, su, " offset:3072 sc1");
    ALD("x4", c.hh[0], v, rh, sh, ""); ALD("x4", c.hh[1], v, rh, sh, " offset:1024"); ALD("x4", c.hh[2], v, rh, sh, " offset:2048"); ALD("x4", c.hh[3], v, rh, sh, " offset:3072");
    ALD("x2", c.hn[0], v, rh, sh, " offset:16"); ALD("x2", c.hn[1], v, rh, sh, " offset:1040"); ALD("x2", c.hn[2], v, rh, sh, " offset:2064"); ALD("x2", c.hn[3], v, rh, sh, " offset:3088");
    c.dd = ((const __attribute__((address_space(4))) unsigned*)(size_t)dv)[d >> 1];
}
#define CONV_PIN(c) asm volatile("" : "+v"(c.uu[0]), "+v"(c.uu[1]), "+v"(c.uu[2]), "+v"(c.uu[3]), "+v"(c.hh[0]), "+v"(c.hh[1]), "+v"(c.hh[2]), "+v"(c.hh[3]), "+v"(c.hn[0]), "+v"(c.hn[1]), "+v"(c.hn[2]), "+v"(c.hn[3]))
template <class MID> __device__ __forceinline__ void conv_stage(ConvIn& c, LAS char* U, int lane, MID mid) {
    LAS char* H = U + CV_U;
    if (lane == 63) c.hn[3] = (u32x2){0u, 0u};
    if (lane < 32) *(LAS u32x4*)(U + lane * 16) = (u32x4){0u, 0u, 0u, 0u};
    const unsigned h0 = RFL(c.hh[0][0]), h1 = RFL(c.hh[0][1]);
    if (lane < 8) {
        const int cc = lane >> 1, half = lane & 1;
        u32x4 z = {0u, 0u, 0u, 0u};
        if (half) {
            if (cc == 1) z[3] = h0 << 16;
            if (cc == 2) z[3] = h0;
            if (cc == 3) { z[2] = h0 << 16; z[3] = __builtin_amdgcn_alignbit(h1, h0, 16); }
        }
        *(LAS u32x4*)(H + cc * CV_HSTRIDE + half * 16) = z;
    }
    SFOR(i, 0, 4)
        SCHED0();
        mid(IC<i>{});
        SCHED0();
        *(LAS u32x4*)(U + 512 + i * 1024 + lane * 16) = c.uu[i];
        const unsigned w0 = c.hh[i][0], w1 = c.hh[i][1], w2 = c.hh[i][2], w3 = c.hh[i][3], w4 = c.hn[i][0], w5 = c.hn[i][1];
        LAS char* p = H + 32 + i * 1024 + lane * 16;
        *(LAS u32x4*)(p) = c.hh[i];
        *(LAS u32x4*)(p + CV_HSTRIDE) = (u32x4){algn(w1, w0), algn(w2, w1), algn(w3, w2), algn(w4, w3)};
        *(LAS u32x4*)(p + 2 * CV_HSTRIDE) = (u32x4){movs(w1), movs(w2), movs(w3), movs(w4)};
        *(LAS u32x4*)(p + 3 * CV_HSTRIDE) = (u32x4){algn2(w2, w1), algn2(w3, w2), algn2(w4, w3), algn2(w5, w4)};
    SEND
}
__device__ __forceinline__ void conv_run(unsigned ddw, __amdgpu_buffer_rsrc_t rs, LAS char* U, int d, int lane) {
    const int fr = lane & 15, g = lane >> 4;
    LAS char* H = U + CV_U;
    const int e0 = 16 * (g >> 1) - 8 * (g & 1) - 7 + fr + 16;
    const int ac = e0 & 3;
    LAS char* ap = H + ac * CV_HSTRIDE + (e0 - ac) * 2;
    LAS char* bp = U + 512 + 32 * (fr - (g >> 1)) + 16 * (g & 1) - 448;
    f32x4 acc[8];
#pragma unroll
    for (int n = 0; n < 8; ++n) acc[n] = (f32x4){0.f, 0.f, 0.f, 0.f};
    bf16x8 LA[2][4], LB[2][4], HA[4], HB[4];
#define CV_RD(A, B, a0, pa, pb) do { _Pragma("unroll") for (int a = 0; a < 4; ++a) { B[a] = *(LAS bf16x8*)((pb) + (a0 + a) * 512); \
        A[a] = cat8(*(LAS bf16x4*)((pa) + (a0 + a) * 512), *(LAS bf16x4*)((pa) + (a0 + a) * 512 + 8)); } } while (0)
#define CV_STEP(cur, nxt, pa, pb) do { \
        CV_RD(HA, HB, 4, pa, pb); \
        _Pragma("unroll") for (int s = 0; s < 4; ++s) { \
            _Pragma("unroll") for (int a = 0; a < s; ++a) { acc[a + s] = mfma16(LA[cur][a], LB[cur][s], acc[a + s]); acc[a + s] = mfma16(LA[cur][s], LB[cur][a], acc[a + s]); } \
            acc[2 * s] = mfma16(LA[cur][s], LB[cur][s], acc[2 * s]); } \
        SCHED_IL(12, 16); \
        CV_RD(LA[nxt], LB[nxt], 0, (pa) + 64, (pb) - 64); \
        _Pragma("unroll") for (int s = 0; s < 4; ++s) \
            _Pragma("unroll") for (int a = 0; a + s < 4; ++a) { acc[a + s + 4] = mfma16(LA[cur][a], HB[s], acc[a + s + 4]); acc[a + s + 4] = mfma16(HA[s], LB[cur][a], acc[a + s + 4]); } \
        SCHED_IL(12, 20); \
    } while (0)
    CV_RD(LA[0], LB[0], 0, ap, bp + 448);
#pragma unroll 1
    for (int bb = 0; bb < 8; bb += 2) {
        LAS char* pa = ap + bb * 64;
        LAS char* pb = bp + (7 - bb) * 64;
        CV_STEP(0, 1, pa, pb);
        CV_STEP(1, 0, pa + 64, pb - 64);
    }
    if (YPRIO) __builtin_amdgcn_s_setprio(YPRIO);
    const float dd = (d & 1) ? bfhi(ddw) : bflo(ddw);
    unsigned yw[8][2];
    u32x2 uw[8];
#pragma unroll
    for (int nu = 0; nu < 8; ++nu) uw[nu] = *(LAS u32x2*)(U + 512 + (256 * nu + 16 * fr + 8 * (g >> 1)) * 2 + ((g & 1) ? 0 : 8));
#pragma unroll
    for (int nu = 0; nu < 8; ++nu) {
        yw[nu][0] = pk2(fmas(bfhi(uw[nu][1]), dd, acc[nu][0]), fmas(bflo(uw[nu][1]), dd, acc[nu][1]));
        yw[nu][1] = pk2(fmas(bfhi(uw[nu][0]), dd, acc[nu][2]), fmas(bflo(uw[nu][0]), dd, acc[nu][3]));
    }
#pragma unroll
    for (int p = 0; p < 4; ++p) {
        const u32x2 s0 = __builtin_amdgcn_permlane16_swap(yw[2 * p][0], yw[2 * p + 1][0], false, false);
        const u32x2 s1 = __builtin_amdgcn_permlane16_swap(yw[2 * p][1], yw[2 * p + 1][1], false, false);
        st16c(rs, (256 * (g & 1) + 16 * fr + 8 * (g >> 1)) * 2 + p * 1024, WS_YT + d * (S * 2), (u32x4){s0[0], s1[0], s0[1], s1[1]});
    }
    SCHED0();
}

constexpr int RB = 5, KST = 36 * 1024;
#define BHIT 0
#define KROT 0
#define HB4 1
#define SPOLL 0
#define KAP(i) ((i) + (KROT) >= 9 ? (i) + (KROT) - 9 : (i) + (KROT))
#define G3_LDB(i) do { _Pragma("unroll") for (int l = 0; l < 4; ++l) bq[i][l] = __builtin_bit_cast(bf16x8, ld16c(rs, lane * 16 + l * 1024, sB + KAP(i) * KST)); \
        bq[i][4] = __builtin_bit_cast(bf16x8, ld16c(rs, HB4 && ((lane >> 3 ^ ct) & 1) ? 0x7fffff00u : lane * 16, sB4 + KAP(i) * KST)); } while (0)
#define G3_LDBA(i) do { _Pragma("unroll") for (int l = 0; l < 4; ++l) asm volatile("buffer_load_dwordx4 %0, %1, %2, %3 offen offset:%4 sc1" : "=v"(bq[i][l]) : "v"(lv), "s"(wr), "s"(sB + (i) * KST), "n"(l * 1024) : "memory"); \
        asm volatile("buffer_load_dwordx4 %0, %1, %2, %3 offen sc1" : "=v"(bq[i][4]) : "v"(lv4), "s"(wr), "s"(sB4 + (i) * KST) : "memory"); } while (0)
#define MG(m) (((m) + 1 + wid_) & 3)
#define G3_LDA(i) do { _Pragma("unroll") for (int m = 0; m < 4; ++m) aq[i][m] = __builtin_bit_cast(bf16x8, ld16c(rs, lane * 16, sA + MG(m) * (72 * 1024) + KAP(i) * 1024)); } while (0)
#define G3_RDB(i) do { _Pragma("unroll") for (int l = 0; l < 4; ++l) bq[i][l] = *(LAS bf16x8*)(lds + redo + ((i) - RB) * 4096 + l * 1024 + lane * 16); \
        bq[i][4] = *(LAS bf16x8*)(lds + redo + 16384 + ((i) - RB) * 512 + (g * 8 + (fr & 7)) * 16); } while (0)
#define G3_PRE() do { _Pragma("unroll") for (int i = 0; i < RB; ++i) G3_LDB(i); SCHED0(); } while (0)
#define G3_HO ((((lane >> 3) & 3) * 16 + 8 * (ct & 1) + (lane & 7)) * 16)
#define G3_DMA1(dst, sB, sB4) do { dma16c4<0, 0>(wr, L0 + (dst), lane * 16, sB + KAP(5 - BHIT) * KST); dma16c4<4096, 0>(wr, L0 + (dst), lane * 16, sB + KAP(6 - BHIT) * KST); \
        dma16c<16384, 0>(wr, L0 + (dst), G3_HO + (lane < 32 ? KAP(5 - BHIT) : KAP(6 - BHIT)) * KST, sB4); } while (0)
#define G3_DMA2(dst, sB, sB4) do { dma16c4<8192, 0>(wr, L0 + (dst), lane * 16, sB + KAP(7 - BHIT) * KST); dma16c4<12288, 0>(wr, L0 + (dst), lane * 16, sB + KAP(8 - BHIT) * KST); \
        dma16c<17408, 0>(wr, L0 + (dst), G3_HO + (lane < 32 ? KAP(7 - BHIT) : KAP(8 - BHIT)) * KST, sB4); } while (0)
#define G3_DMA(dst, sB, sB4) do { G3_DMA1(dst, sB, sB4); G3_DMA2(dst, sB, sB4); } while (0)
#define FLAG_ISSUE(v_, o_) asm volatile("buffer_load_dwordx2 %0, %1, %2, 0 offen sc1" : "=v"(v_) : "v"(o_), "s"(wr) : "memory")
#define FP_SPIN() spin_flags(rs, sF + (lane & 3) * (16 * FPS), tag)
#define FP_ISSUE() FLAG_ISSUE(fpv, sF + (lane & 3) * (16 * FPS))
#define FP_CHECK(NV) do { u64 ne; \
        asm volatile("s_waitcnt vmcnt(%3)\n v_cmp_ne_u64 %0, %1, %2" : "=s"(ne) : "s"(tag), "v"(fpv), "n"(NV) : "memory"); \
        if (ne != 0ull) FP_SPIN(); \
        asm volatile("" ::: "memory"); } while (0)
#define XST(i) do { } while (0)
#define NOSTAT 0
#define ROT (pi + pj) % 9
#define CPRE 2
constexpr float GC1 = -2.0f * 0.7978845608028654f * 1.4426950408889634f, GC2 = GC1 * 0.044715f;
#define CPRIO 2
#define CPFW 2
#define T1W 1
#define KPF 1
#define GS 72
#define WBASE 0
#define WALK 2
#define WPF 4
#define CVD 0
#define YIS 8
#define YCK 8
#define PSTO(x) (x)
#define PSL 0
#define XIS 8
#define LPRIO 0
#define HPF 1
#define CVL 47
#define VPRIO 1
#define RGW 5
#define OAUX 0
#define TE 0
#define KXC 1
#define KC ((b >> 3) & 7)
#define KR ((b & 7) * 4 + (b >> 6))
#define KVE 1
#define HKE 17
#define FE 2
#define GCS 84
#define GPRIO 3
#define A21 1
#define KPRIO 0
#define KS1 1
#define KIL 0
#define WOS 0
#define ZD 6
#define PB1 99
#define PB2 99
#define SPF 8
#define WIL 1
#define XPF 0
#define FRND 0
#define SP2 3
#define SZW 8
#define SZ0 2
#define SZD 8
#define SPR 1
#define SP1 3
#define LW 0
#define LWI 2
#define LDI 1
#define LPE 99
#define NOLB 0
#define EFP 1
#define CRP 1
#define DRP 1
#define LDD 2
#define GMAP 0
#define AD 2
#define GT 6
#define GC 8
#define CST 1
#define RST 2
#define W0T 7
#define W2T 7
#define W6T 9
#define XFL 0
#define SPRIO 3
#define ZPIN 1
#define W16 1
#define PCD 0
#define PCN 12
#define TGL 1
#define NOTA 0
#define PAM 0
#define GSCH 1
#define VOT 9
#define TGC 0
#define K8P 0
#define ILV 1
#define GCD 0
#define G1P 3
#define XMAP 0
#define GSL 0
#define SUL 1
#define RSE 0
#define PNX 0
#define PNE 0
#define CU4 1
#define WPZ 999
#define WPM 2
#define WPA 99
#define WPN 1
#define WPD 1
#define DVE 0
#define SKP 0
#define WOP 0
#define TLT 0
#define EPF 2
#define EVB 2
#define EPW 0
#define EPH 24
#define EPC 24
#define SPG 0
#define SGR 2
#define PFP 1
#define B21E 1
#define S8 3
#define ZLATE 0
#define PPF 0
#define AU4 0
#define PA0 2

__global__ __launch_bounds__(512, 2) void mega(const u16* __restrict__ x, const u16* __restrict__ w_in, char* __restrict__ ws, const u16* __restrict__ w_sp, const u16* __restrict__ b_sp,
                                               const u16* __restrict__ kin_, const u16* __restrict__ vin_, const u16* __restrict__ shk_, const u16* __restrict__ hk_, const u16* __restrict__ dv_,
                                               const u16* __restrict__ w_out_, u16* __restrict__ out_) {
#define KARG(i) ({ const __attribute__((address_space(4))) u64* ka_ = (const __attribute__((address_space(4))) u64*)__builtin_amdgcn_kernarg_segment_ptr(); asm volatile("" : "+s"(ka_)); (const u16*)ka_[i]; })
    extern __shared__ __attribute__((aligned(16))) char smem_[];
    LAS char* lds = (LAS char*)smem_;
    asm volatile(".Lmega_head:");
    if (EPF == 1) {
        unsigned t0, t1;
        asm volatile("v_readfirstlane_b32 s8, %2\n s_lshr_b32 s8, s8, 6\n s_cmp_lg_u32 s8, %4\n s_cbranch_scc1 .Lepf_skip\n s_getpc_b64 s[8:9]\n.Lepf_here:\n s_and_b32 s9, s9, 0xffff\n s_mov_b32 s10, .Lmega_comp+%5-.Lepf_here\n s_mov_b32 s11, 0x20000\n"
                     "v_and_b32 %0, 63, %2\n v_cmp_gt_u32 vcc, %6, %0\n v_lshlrev_b32 %0, 7, %0\n v_add_u32 %1, .Lmega_comp-%7-.Lepf_here, %0\n s_mov_b32 m0, %3\n v_cndmask_b32 %0, %1, %0, vcc\n"
                     "buffer_load_dword %0, s[8:11], 0 offen lds\n.Lepf_skip:"
                     : "=&v"(t0), "=&v"(t1) : "v"(threadIdx.x), "n"(L_DUMP), "n"(EPW), "n"(EPC * 128), "n"(EPH), "n"(EPH * 128) : "s8", "s9", "s10", "s11", "vcc", "scc", "memory");
        SCHED0();
    }
    if (EPF == 3) {
        unsigned t0;
        asm volatile("v_readfirstlane_b32 s7, %1\n s_cmp_ge_u32 s7, %3\n s_cbranch_scc1 .Lepf_skip\n s_getpc_b64 s[8:9]\n.Lepf_here:\n s_and_b32 s9, s9, 0xffff\n"
                     "s_lshl_b32 s10, %4, 11\n s_lshl_b32 s11, s7, 7\n s_and_b32 s10, s10, 0xc000\n s_or_b32 s10, s10, s11\n s_and_b32 s10, s10, 0xe000\n s_bitset0_b32 s10, 14\n s_bitcmp1_b32 %4, 3\n s_cselect_b32 s11, 0x4000, 0\n s_or_b32 s10, s10, s11\n"
                     "s_cmp_eq_u32 s7, 64\n s_cselect_b32 s10, .Lmega_comp-.Lepf_here, s10\n s_cmp_eq_u32 s7, 0\n s_cselect_b32 s7, 0, s10\n"
                     "s_mov_b32 s10, .Lmega_cend-.Lepf_here\n s_mov_b32 s11, 0x20000\n"
                     "v_and_b32 %0, 63, %1\n s_mov_b32 m0, %2\n v_lshl_add_u32 %0, %0, 7, s7\n"
                     "buffer_load_dword %0, s[8:11], 0 offen lds\n.Lepf_skip:"
                     : "=&v"(t0) : "v"(threadIdx.x), "n"(L_DUMP), "n"(EPF == 2 ? 128 : 256), "s"(blockIdx.x) : "s7", "s8", "s9", "s10", "s11", "scc", "memory");
        SCHED0();
    }
    if (EPF == 2) {
        unsigned t0;
        asm volatile("v_readfirstlane_b32 s7, %1\n s_cmpk_ge_u32 s7, 128\n s_cbranch_scc1 .Lepf_skip\n s_getpc_b64 s[8:9]\n.Lepf_here:\n s_and_b32 s9, s9, 0xffff\n"
                     "s_cmp_eq_u32 s7, 0\n s_cselect_b32 s7, 0, .Lmega_comp-.Lepf_here\n s_mov_b32 s10, .Lmega_cend-.Lepf_here\n s_mov_b32 s11, 0x20000\n"
                     "v_and_b32 %0, 63, %1\n s_mov_b32 m0, %2\n v_lshl_add_u32 %0, %0, 7, s7\n"
                     "buffer_load_dword %0, s[8:11], 0 offen lds\n.Lepf_skip:"
                     : "=&v"(t0) : "v"(threadIdx.x), "n"(L_DUMP) : "s7", "s8", "s9", "s10", "s11", "scc", "memory");
        SCHED0();
    }
    const unsigned L0 = (unsigned)(size_t)lds;
    const int b = blockIdx.x, xcd = b & 7, r = b >> 3;
#define XH(b_) ((b_) & 1)
#define XQ(b_) ((b_) >> 1 & 3)
    const int pi = XH(b) * 8 + (r & 7), pj = XQ(b) * 4 + (r >> 3);
    const int tid = threadIdx.x, wid = RFL(tid >> 6), lane = tid & 63;
    const int fr = lane & 15, g = lane >> 4, q4 = (lane >> 2) & 3, p4 = lane & 3;
    const int w = wid & 3;
    unsigned vB[5];
#define SET_VB(i) do { const unsigned c = i < 4 ? (4 * w + i) * 64 + lane : 1024 + 32 * w + (lane & 31), rho = c / 18, cc = c - rho * 18; \
        const unsigned grow = (rho & ~12u) | ((rho & 4) << 1) | ((rho & 8) >> 1); vB[i] = grow * (F * 2) + cc * 16 - (i & 3) * 1024; } while (0)
    if (EVB && EVB < 4) {
#pragma unroll
        for (int i = 0; i < 5; ++i) SET_VB(i);
        asm volatile("" : "+v"(vB[0]), "+v"(vB[1]), "+v"(vB[2]), "+v"(vB[3]), "+v"(vB[4]));
        if (EVB == 1) asm volatile("" : "+s"(x), "+s"(w_in), "+s"(ws), "+s"(w_sp));
        SCHED0();
    }
    __amdgpu_buffer_rsrc_t rs;
    u32x4 wr;
#define ARGS_READY() do { if (EVB >= 2) { SCHED0(); asm volatile("" : "+s"(x), "+s"(w_in), "+s"(ws), "+s"(w_sp), "+s"(b_sp)); } rs = ws_rsrc(ws); wr = mk_rsrc(ws, WS_END); } while (0)
    if (EVB < 2) ARGS_READY();
    const f32x4 zero4 = {0.f, 0.f, 0.f, 0.f};
    STAMP(0);
    if (K8P == 1) { unsigned t_; asm volatile("s_load_dword %0, %1, 0x40" : "=s"(t_) : "s"(__builtin_amdgcn_kernarg_segment_ptr()), "s"(x) : "memory"); asm volatile("s_waitcnt lgkmcnt(0)" : "+s"(t_) :: "memory"); }
#define TAILS() const int b_ = opqs(b), wid_ = opqs(wid), rg = GMAP == 0 ? XH(b_) * 16 + XQ(b_) * 4 + ((b_ >> 3) & 3) : GMAP == 1 ? (b_ & 1) * 16 + (b_ & 4) * 2 + (b_ >> 5) : (b_ & 1) * 16 + (b_ >> 4), \
        ct = GMAP == 0 ? b_ >> 5 : GMAP == 1 ? (b_ & 2) * 2 + ((b_ >> 3) & 3) : (b_ & 6) + ((b_ >> 3) & 1); \
    const int nb3 = (9 * ct) >> 1, n30 = nb3 + (ct & 1), n34 = (ct & 1) ? nb3 : nb3 + 4; \
    const int redo = (int)(((wid_ < 4 ? redtab(0) : redtab(4)) >> (16 * (wid_ & 3))) & 0xffff) << 8
#define KX 0
#define TAILS2() const int kx = (KX) * 4, kw_ = wid_ ^ kx; const unsigned sA = WS_P + (4 * rg * 72 + 9 * kw_) * 1024, sB = WS_WT + (9 * (kw_ | BHIT) * 36 + n30) * 1024, sB4 = WS_WT + (9 * (kw_ | BHIT) * 36 + n34) * 1024, \
    sF = WS_FP + ((rg >> 1) * 512 + kw_ * 64 + (rg & 1) * 8) * FPS
    u64 tag;
#define TAG_READY() do { asm volatile("s_load_dwordx2 %0, %1, 0x0\n s_waitcnt lgkmcnt(0)" : "=s"(tag) : "s"(ws + WS_E) : "memory"); tag += TAG_INC; } while (0)
#define WAIT_LB0() __builtin_amdgcn_s_waitcnt(0xC07F)
#define RD(p) (*(LAS bf16x8*)(p))
#define RDP(p, o) (*(LAS bf16x8*)(size_t)((p) + (o)))
#define TRF(p) cat8(trread(p), trread((p) + 2304))
#define TRP(p, o) TRF((LAS char*)(size_t)((p) + (o)))
#define ADV(p, d) asm("v_add_u32 %0, %1, %0" : "+v"(p) : "s"(d))
#define LDSW(o) (*(volatile LAS unsigned*)(lds + (o)))
#define FENCE() asm volatile("" ::: "memory")
#define SPIN(word, need) wcheck(0u, L0 + (word) + p4 * 4, need)
#define SGB(m, n) __builtin_amdgcn_sched_group_barrier(m, n, 0)
    const int l3 = lane >> 3;
    const unsigned ch = ((lane & 7) ^ (l3 >> 1)) * 16;
    const int bb = (q4 + 4 * (g & 1) + 16 * (g >> 1)) * 288 + p4 * 8;
    unsigned vA[4];
#define SET_VSP() do { const unsigned base = l3 * (S * 2) + ch; vA[0] = base; vA[1] = (base ^ 64) + 8 * S * 2 - 1024; vA[2] = base + 16 * S * 2 - 2048; vA[3] = (base ^ 64) + 24 * S * 2 - 3072; } while (0)
    const unsigned lw = L0 + w * 4096, lh = L0 + 16384 + w * 512, w1 = pj * 288;
    int rot = ROT;
#define KT(t) ((t) + rot >= 9 ? (t) + rot - 9 : (t) + rot)
#define DMA_W(dst, src) do { dma4v(rwi, lw + (dst), vB, w1 + (src)); dmah(rwi, lh + (dst), vB[4], w1 + (src)); } while (0)
#define DMA_W1(j) DMA_W(warea(j), KT(j) * (64 * F * 2))
    auto walk = [&](int i) {
        const int j = pj & 3, m = i ^ j, x = i - 1;
        const int q = (pi & ~3) | ((j + x + (x >= ((pi - j) & 3))) & 3);
        int e = (WALK & 2) && (m == 7 || m == 8) ? m ^ 15 : m;
        if (WALK & 4) { e += pj & 12; e = e >= 28 ? e - 24 : e >= 16 ? e - 12 : e; }
        return i == 0 ? pi : i < 4 ? q : pi ^ e;
    };
#define PF_WIN() PF_WINK(0)
#define PF_ON(k) (WPN == 1 ? ((WPM >> w_) & 1) : WPN == 2 || WPN == 3 ? w_ == 1 + (k) : w_ == 1 + ((k) >> 1) && (ln_ >> 5) == ((k) & 1))
#define ISWP(t) ((t) >= WPA && ((t) - WPA) % WPD == 0 && ((t) - WPA) / WPD < WPN)
#define PF_WINK(k) do { const int b_ = opqs(b), w_ = opqs(w), pi_ = XH(b_) * 8 + (b_ >> 3 & 7); const unsigned ln_ = opqv(tid) & 63; \
        pf4<0>(mk_rsrc(w_sp, S * S * 2), L0 + L_DUMP, PF_ON(k) ? (ln_ >> 3) * (S * 2) + (ln_ & 7) * 128 : 0x7fffff00u, (pi_ * 128 + 32 * XQ(b_) + 8 * (b_ >> 6)) * (S * 2) + ((pi_ >> 2) ^ w_) * 1024); } while (0)
    u64 fpv;
    bf16x8 bq[9][5];
    bool hasconv = false;
    const int cd = w < 2 ? 2 * b + w : 512 + b;

    if (__builtin_expect(wid >= 4, 1)) {
        if (EVB >= 4) __builtin_amdgcn_s_setprio(3);
        if (EVB == 4) {
#pragma unroll
            for (int i = 0; i < 5; ++i) SET_VB(i);
            asm volatile("" : "+v"(vB[0]), "+v"(vB[1]), "+v"(vB[2]), "+v"(vB[3]), "+v"(vB[4]));
        }
        {
            const unsigned base = (8 * (l3 >> 2) + (l3 & 3)) * (D * 2) + ch;
            vA[0] = base; vA[1] = (base ^ 64) + 16 * D * 2 - 1024; vA[2] = (base ^ 64) + 4 * D * 2 - 2048; vA[3] = base + 20 * D * 2 - 3072;
        }
        const unsigned xs = (pi * 128 + 32 * w) * (D * 2);
        if (EVB >= 2) {
            asm volatile("" : "+v"(vA[0]), "+v"(vA[1]), "+v"(vA[2]), "+v"(vA[3]) : "s"(xs + rot * 128), "s"(w1 + FH * 2 + rot * (64 * F * 2)), "s"(lw), "s"(lw + SL_A), "s"(lh + SL_A), "s"(EVB == 2 ? KT(1) : 0));
            ARGS_READY();
        }
        const u32x4 rx = mk_rsrc(x, SZ_X), rwi = mk_rsrc(w_in, D * F * 2);
        XST(26);
        dma4v(rx, lw, vA, xs + rot * 128);
        SCHED0();
#pragma unroll
        for (int i = 0; i < 5; ++i) {
            if (!EVB || EVB == 5) SET_VB(i);
            if (i < 4) dma1(i * 1024, rwi, lw + SL_A, vB[i], w1 + FH * 2 + rot * (64 * F * 2)); else dmah(rwi, lh + SL_A, vB[4], w1 + FH * 2 + rot * (64 * F * 2));
            SCHED0();
        }
        if (EVB >= 3) asm volatile("" : "+s"(rot));
        XST(27);
        if (TGL) asm volatile("s_load_dwordx2 %0, %1, 0x0" : "=s"(tag) : "s"(ws + WS_E) : "memory"); else TAG_READY();
        const int xi = (fr >> 2) & 1;
        const int n0 = (32 * w + 16 * xi + 4 * (fr >> 3) + (fr & 3)) * 128 + ((g ^ (2 * (fr >> 3) + ((fr >> 1) & 1)) ^ (4 * xi)) << 4), n1 = n0 ^ 64;
        const int c0 = bb + w * 64, c8 = bb + 256 + (w & 1) * 9216;
        bf16x8 ones = {0x3f80, 0x3f80, 0x3f80, 0x3f80, 0x3f80, 0x3f80, 0x3f80, 0x3f80};
        asm volatile("" : "+v"(ones));
        bf16x8 xf[9][4];
        unsigned tag32, tagx;
        if (!TGL) { tag32 = (unsigned)tag; tagx = g == 0 ? tag32 : 0u; }
        const unsigned go = g * 1024 + fr * 16, go2 = g == 0 ? 8192 + fr * 16 : 0x7fffff00u;
        const unsigned sr = WS_SR + pi * 12288 + w * 256, sc = WS_SC + pj * 12288 + w * 256;
#define COLSUM(base, dst) do { bf16x8 cf[5]; cf[0] = TRF((base) + c0); cf[1] = TRF((base) + c0 + 32); cf[2] = TRF((base) + c0 + 9216); cf[3] = TRF((base) + c0 + 9216 + 32); cf[4] = TRF((base) + c8); \
            const f32x4 s0_ = mfma16(ones, cf[2], mfma16(ones, cf[0], zero4)), s1_ = mfma16(ones, cf[3], mfma16(ones, cf[1], zero4)), s2_ = mfma16(ones, cf[4], zero4); \
            st16c(rs, g == 0 && pi < 9 ? fr * 16 : 0x7fffff00u, (dst) + pi * 1024, (u32x4){__float_as_uint(s0_[0]), __float_as_uint(s1_[0]), __float_as_uint(s2_[0]), tag32}); } while (0)
#define GATHER(pv, so) do { ALD("x4", pv[0], go, wr, so, " sc1"); ALD("x4", pv[1], go, wr, (so) + 4096, " sc1"); ALD("x4", pv[2], go2, wr, so, " sc1"); } while (0)
#define GBAD(pv) (__builtin_amdgcn_ballot_w64(((pv[0][3] ^ tag32) | (pv[1][3] ^ tag32) | (pv[2][3] ^ tagx)) != 0u) != 0ull)
#define GCHECK(pv, so) do { asm volatile("" : "+v"(pv[0]), "+v"(pv[1]), "+v"(pv[2])); \
            if (__builtin_expect(GBAD(pv), 0)) { unsigned sp_ = 0; \
                do { GATHER(pv, so); WAIT_V(0); asm volatile("" : "+v"(pv[0]), "+v"(pv[1]), "+v"(pv[2])); } while (GBAD(pv) && ++sp_ < SPIN_CAP); } } while (0)
#define CSUM(pv, k) adds(adds(__uint_as_float(pv[0][k]), __uint_as_float(pv[1][k])), __uint_as_float(pv[2][k]))
        u32x4 gr[3], gc[3];
        f32x2 lnc; float cs01, cs8;
        const u32x4 rsp = mk_rsrc(w_sp, S * S * 2);
        __builtin_amdgcn_s_setprio(3);
#define TILE_A(t) do { if (NOTA && (t) > 3) break; dma4v(rx, lw + ((t) & 3) * SLOT, vA, xs + KT(t) * 128); DMA_W(((t) & 3) * SLOT + SL_A, FH * 2 + KT(t) * (64 * F * 2)); } while (0)
        unsigned vo[3], rho;
#define SET_VO() do { const int kap = opqs(b) / 3, th = b - kap * 3, kq = kap / 9, ke = kap - kq * 9, pr = ke & 3, ln = opqv(lane); \
            const unsigned wos = th * 384 + w * 96 + (ke == 8 ? 18 * kq + 4 : 18 * kq + (ke >> 2) * 9 + 2 * pr + (pr >> 1)) * (16 * D * 2); \
            _Pragma("unroll") for (int i = 0; i < 3; ++i) { const unsigned c = i * 64 + ln, rr = c / 6; vo[i] = (ke == 8 ? (rr >> 3 & 1) * 144 + (rr & 4) * 2 + (rr & 3) + (rr >> 4) * 4 : rr) * (D * 2) + (c - rr * 6) * 16 - i * 1024 + wos; } \
            rho = walk(ln >> 2); \
            asm volatile("" : "+v"(vo[0]), "+v"(vo[1]), "+v"(vo[2]), "+v"(rho)); } while (0)
#define NT_(t) ((t) < 0 ? 0 : ((t) + AD < 9 ? 9 : 0) + ((t) == 0 && AD == 3 ? 9 : 0))
#define NX_(t) ((t) < 0 ? 0 : ((t) == CST) + ((t) == RST) + ((t) == GT ? 6 : 0) + ((t) == W0T ? 5 : 0) + ((t) == W2T ? 5 : 0) + ((t) == W6T ? 5 : 0) + ISWP(t))
#define NA_(t) (NT_(t) + NX_(t))
#pragma unroll
        for (int t = 0; t < 9; ++t) {
            SCHED0();
            if (t == 0) {
                if (TGL < 2) XST(28);
                if (WPA == -2) PF_WIN();
                TILE_A(1);
                if (WPA == -1) PF_WIN();
                SCHED0();
                asm volatile("" :: "s"(KT(1)), "s"(KT(2)), "s"(KT(3)), "s"(KT(4)), "s"(KT(5)), "s"(KT(6)), "s"(KT(7)), "s"(KT(8)), "v"(n0), "v"(c0), "v"(c8), "v"(go), "v"(go2));
                if (VOT < 0) { SET_VO(); SCHED0(); }
#define TAG_WAIT() do { asm volatile("s_waitcnt lgkmcnt(0)" : "+s"(tag) :: "memory"); tag += TAG_INC; tag32 = (unsigned)tag; tagx = g == 0 ? tag32 : 0u; } while (0)
                if (TGL == 1) TAG_WAIT();
            }
            constexpr int big = 63;
            const int wt_ = t == 0 ? 9 + (WPA == -1 || WPA == -2) : t == 1 ? NA_(0) + (WPA == -1) : AD == 2 ? NX_(t - 2) + NA_(t - 1) : t == 2 ? NA_(0) - 9 + NA_(1) : NX_(t - 3) + NA_(t - 2) + NA_(t - 1);
            const int wg_ = t != GC ? big : (GT == W0T ? 5 : 0) + (GT == W2T ? 5 : 0) + (GT == W6T ? 5 : 0) + ISWP(GT) + (GC > GT + 1 ? NA_(GT + 1) : 0) + (GC > GT + 2 ? NA_(GT + 2) : 0);
            WAIT_V(wt_ < wg_ ? wt_ : wg_);
            BAR();
            if (TGL < 2 || t) XST(40 + t);
            LAS char* sb = lds + (t & 3) * SLOT;
            bf16x8 cf[5];
            u32x4 rec;
            if (t < 8) { xf[t][0] = RD(sb + n0); xf[t][1] = RD(sb + n1 + 1024); xf[t][2] = RD(sb + n1); xf[t][3] = RD(sb + n0 + 1024); }
            if (t == CST) { LAS char* base = lds + SL_A; cf[0] = TRF(base + c0); cf[1] = TRF(base + c0 + 32); cf[2] = TRF(base + c0 + 9216); cf[3] = TRF(base + c0 + 9216 + 32); cf[4] = TRF(base + c8); }
            SCHED0();
            if (t + 2 < 9) dma4v(rx, lw + ((t + 2) & 3) * SLOT, vA, xs + KT(t + 2) * 128);
            if (t == W0T && t > 6) DMA_W1(0);
            if (t == W6T && t == 8) DMA_W1(NWA ? 2 : 6);
            if (ISWP(t) && t == 8) PF_WINK((t - WPA) / WPD);
            SCHED0();
            if (t == CST) {
                const f32x4 s0_ = mfma16(ones, cf[2], mfma16(ones, cf[0], zero4)), s1_ = mfma16(ones, cf[3], mfma16(ones, cf[1], zero4)), s2_ = mfma16(ones, cf[4], zero4);
                rec = (u32x4){__float_as_uint(s0_[0]), __float_as_uint(s1_[0]), __float_as_uint(s2_[0]), tag32};
                asm volatile("" : "+v"(rec));
            }
            if (t == RST) {
                f32x4 rsm[2], rq[2];
#pragma unroll
                for (int m = 0; m < 2; ++m) {
                    rsm[m] = mfma16(ones, xf[0][2 + m], mfma16(ones, xf[0][m], zero4));
                    rq[m] = mfma16(xf[0][2 + m], xf[0][2 + m], mfma16(xf[0][m], xf[0][m], zero4));
                }
#define DIAG(q_) ((fr & 2) ? ((fr & 1) ? q_[3] : q_[2]) : ((fr & 1) ? q_[1] : q_[0]))
                rec = rec4(rsm[0][0], DIAG(rq[0]), rsm[1][0], DIAG(rq[1]), tag32);
                asm volatile("" : "+v"(rec));
            }
            if (t == GC) {
                GCHECK(gr, sr); GCHECK(gc, sc);
                lnc = rec_consts<3>(gr, 1.0f / D);
                LAS float* p = (LAS float*)(lds + L_ROWST + (16 * w + 8 * (lane >> 5) + (fr >> 1)) * 16 + (fr & 1) * 4);
                p[0] = lnc.x; p[2] = lnc.y;
            }
            SCHED0();
            if (t + 2 < 9) DMA_W(((t + 2) & 3) * SLOT + SL_A, FH * 2 + KT(t + 2) * (64 * F * 2));
            if (t == CST) st16c(rs, g == 0 && pi < 9 ? fr * 16 : 0x7fffff00u, sc + pi * 1024, rec);
            if (t == RST) st16c(rs, (fr >> 2) == g && pj < 9 ? fr * 16 : 0x7fffff00u, sr + pj * 1024, rec);
            if (t == GT) { GATHER(gr, sr); GATHER(gc, sc); }
            if (t == W0T && t <= 6) DMA_W1(0);
            if (t == W2T) DMA_W1(NWA ? 1 : 2);
            SCHED0();
            if (t == GC + GCD) {
                cs01 = half_sum(CSUM(gc, 0), CSUM(gc, 1)); cs8 = CSUM(gc, 2); cs8 = half_sum(cs8, cs8);
                LAS float* c = (LAS float*)(lds + L_CS) + fr * 12;
                c[2 * w + (lane >> 5)] = cs01;
                c[8 + w] = cs8;
            }
            SCHED0();
            if (t == W6T && t < 8) DMA_W1(NWA ? 2 : 6);
            if (ISWP(t) && t < 8) PF_WINK((t - WPA) / WPD);
            if (t == VOT) { SET_VO(); SCHED0(); }
            if (t < 8) asm volatile("" : "+v"(xf[t][0]), "+v"(xf[t][1]), "+v"(xf[t][2]), "+v"(xf[t][3]));
        }
        const u16* kvp = KARG(5 + (w >> 1));
        const u16 *wop = WOP ? KARG(10) : nullptr, *skp = SKP == 2 ? KARG(7) : nullptr;
        WAIT_L0();
        BAR();
        STAMP(2);
        if (!NWA) DMA_W1(1);
        if (NWA && W6T > 8) DMA_W1(2);
        DMA_W1(3); DMA_W1(4); DMA_W1(5); if (NWA || W6T > 8) DMA_W1(6);
#define PF_KV() pf4<0>(mk_rsrc(kvp, KPF ? SZ_X : 0), L0 + L_DUMP, lane * (D * 2), KR * 64 * (D * 2) + KC * 144 + (w & 1) * 140)
#define PF_HK() pf4<0>(mk_rsrc(hk + cd * S, HPF && (w < 2 || (w == 2 && b < 64)) ? S * 2 : 0), L0 + L_DUMP, (opqv(tid) & 63) * 128, 0)
        if (KVE) PF_KV();
        if (WPF & 4) pf4<0>(w == 0 ? rsp : mk_rsrc(w_sp, 0), L0 + L_DUMP, (lane >> 1) * (S * 2) + (lane & 1) * 128, (pi * 128 + 32 * (pj & 3)) * (S * 2) + pi * 256);
        if (WPF & 1) pf4<0>(rsp, L0 + L_DUMP, (lane >> 1) * (S * 2) + (lane & 1) * 128, (pi * 128 + 32 * w) * (S * 2) + (pi ^ pj) * 256);
        if (WPF & 2) pf4<0>(rsp, L0 + L_DUMP, (lane >> 1) * (S * 2) + (lane & 1) * 128, (pi * 128 + 32 * w) * (S * 2) + (pi ^ (pj & 3)) * 256);
        if (WPA == 9) PF_WIN();
#define NPF ((WPF & 1) + (WPF >> 1 & 1) + (WPF >> 2 & 1) + KVE + (HKE == 1) + (WPA == 9) + (WOP == 1 ? 3 : WOP == 2) + (SKP == 2))
        if (SKP == 2) pf4<0>(mk_rsrc(skp, w == 1 ? D * 8 : 0), L0 + L_DUMP, lane < 6 ? ((KC * 576) & ~127) + lane * 128 : 0x7fffff00u, 0);
        if (VOT > 8) SET_VO();
        if (WOP == 1) { const u32x4 r_ = mk_rsrc(wop, b < 216 ? FH * D * 2 : 0); pf4<0>(r_, L0 + L_DUMP, vo[0], 0); pf4<1024>(r_, L0 + L_DUMP, vo[1], 0); pf4<2048>(r_, L0 + L_DUMP, vo[2], 0); }
        if (WOP == 2) pf4<1024>(mk_rsrc(wop, b < 216 && w == 1 ? FH * D * 2 : 0), L0 + L_DUMP, (lane & 31) == 8 ? vo[1] : 0x7fffff00u, 0);
#define FL1 (!NWA ? 4 : W6T > 8 ? 2 : 3)
#define FL2 (!NWA ? 7 : FL1 + 2)
        WAIT_V(NWA ? 5 * (7 - FL1) + NPF + ISWP(8) : (W6T > 8 ? 15 : 10) + NPF);
        LDSW(M_FL + w * 4) = 1u;
        XST(50);
        SPIN(M_FL, 1);
        XST(49);
        COLSUM(lds + warea(0), sc + 16 * 12288);
        st16c(rs, w == 3 && pj == 0 ? lane * 16 : 0x7fffff00u, WS_ST1 + pi * 1024, *(LAS u32x4*)(lds + L_ROWST + lane * 16));
        f32x4 az[2][9];
        u64 fzv[2];
        const unsigned as0 = (pi * 128 + 32 * w) * (S * 2), zs0 = WS_Z2N + pj * 32 * SL_B + w * 4096;
        unsigned vl, fz;
        u64 sd = 0;
#define SPL(so_, k_) do { u64& sd_ = sd; asm volatile("s_buffer_load_dwordx2 %0, %1, %2 offset:%3" : "+s"(sd_) : "s"(rsp), "s"(so_), "n"(((k_) & 7) * 4096 + ((k_) >> 3) * 128 + 60) : "memory"); } while (0)
#define SPO(i_) (as0 + ((pj >> 2) << 15) + __builtin_amdgcn_readlane(rho, 4 * (i_)) * 256)
        const unsigned soz = SPO(SZW);
#define KIDX(t) (2 * __builtin_amdgcn_readlane(rho, 4 * (((t) - 18) >> 1)) + ((t) & 1))
#define NODMAB 99
        auto stageA = [&](int t) { if (t < NODMAB) dma4v(rsp, lw + (t & 3) * SLOT, vA, as0 + KIDX(t) * 128); };
        auto stageB = [&](int t) {
            if (t >= NODMAB) return;
            const unsigned zs = zs0 + KIDX(t) * SL_B;
            dma16c4<SL_A, 0>(wr, lw + (t & 3) * SLOT, vl, zs);
            dmahc(wr, lh + (t & 3) * SLOT + SL_A, vl, zs + 16384 - w * 3584);
        };
        {
            bf16x8 wf[162];
            unsigned fw;
            unsigned wb = L0 + bb;
#define WF_B(j) ((j) < 2 || (j) >= 6 ? 0 : (j) < 5 ? warea(2) : warea(5))
#define WF_P(s) ((LAS char*)(size_t)(wb + (warea((s) / 18) - WF_B((s) / 18) + ((s) % 9) * 32 + (((s) / 9) & 1) * 9216)))
#pragma unroll
            for (int s = 0; s < ZD; ++s) wf[s] = TRF(WF_P(s));
            SCHED0();
            XST(51);
            __builtin_amdgcn_s_setprio(0);
            wcheck(0u, L0 + M_RC + w * 4, 1);
            STAMP(3);
            SFOR(s, 0, 162)
                constexpr int j = s / 18, ks = (s / 9) & 1, n = s % 9;
                if (s == 18 * FL1 - ZD - 4 || (NWA && s == 18 * FL2 - ZD - 4)) fw = LDSW(M_FL + p4 * 4);
                if (s == 18 * FL1 - ZD) wcheck(fw, L0 + M_FL + p4 * 4, 2);
                if (NWA && s == 18 * FL2 - ZD) wcheck(fw, L0 + M_FL + p4 * 4, 3);
                if (s == 126 - ZD - 4) fw = LDSW(M_RC + p4 * 4);
                if (s == 126 - ZD) wcheck(fw, L0 + M_RC + p4 * 4, 2);
                if (s == 18) { xf[8][0] = RD(lds + n0); xf[8][1] = RD(lds + n1 + 1024); }
                if (s == 19) { xf[8][2] = RD(lds + n1); xf[8][3] = RD(lds + n0 + 1024); }
                if (s == 25) asm volatile("" : "+v"(xf[8][0]), "+v"(xf[8][1]), "+v"(xf[8][2]), "+v"(xf[8][3]));
                if (s == 18 || s == 36 || s == 108 || (!NWA && s == 126)) LDSW(M_RL + w * 4) = j;
                if constexpr (s == GCS) GATHER(gc, sc + 16 * 12288);
                if constexpr ((SPF & 4) && s >= SZ0 && (s - SZ0) % SZD == 0 && (s - SZ0) / SZD < 16) SPL(soz, (s - SZ0) / SZD);
                if (s == (NWA ? 18 * FL1 - ZD - 8 : 40)) { WAIT_V(2 + NPF + 5 * (7 - FL2)); LDSW(M_FL + w * 4) = 2u; }
                if (NWA && s == 18 * FL2 - ZD - 16) { WAIT_V(2 + NPF); LDSW(M_FL + w * 4) = 3u; }
                if constexpr (s == WPZ) {
                    const unsigned ln = opqv(tid) & 63;
                    pf4<0>(((WPM >> w) & 1) ? rsp : mk_rsrc(w_sp, 0), L0 + L_DUMP, (ln >> 1) * (S * 2) + (ln & 1) * 128, (pi * 128 + 32 * (pj >> 2)) * (S * 2) + __builtin_amdgcn_readlane(rho, w == 0 ? 4 : 16 * w) * 256);
                }
                if (s == 140) { vl = (opqv(tid) & 63) * 16; fz = WS_FZ + pj * 512 + rho * 32 + ((vl >> 1) & 24); asm volatile("" : "+v"(fz), "+v"(vl)); }
                if (s == 146) { const unsigned l3 = vl >> 7, ch = ((vl >> 4 & 7) ^ (l3 >> 1)) * 16; vA[0] = l3 * (S * 2) + ch; asm volatile("" : "+v"(vA[0])); }
                if (s == 150) { vA[1] = (vA[0] ^ 64) + 8 * S * 2 - 1024; vA[2] = vA[0] + 16 * S * 2 - 2048; vA[3] = vA[1] + 16 * S * 2 - 2048; asm volatile("" : "+v"(vA[1]), "+v"(vA[2]), "+v"(vA[3])); }
                if (s == 18) XST(56); if (s == 42) XST(57); if (s == 54) XST(58); if (s == 107) XST(59); if (s == 109) XST(60); if (s == 121) XST(61); if (s == 133) XST(62); if (s == 135) XST(63);
                SCHED0();
                if (s < 9) asm volatile("v_mfma_f32_16x16x32_bf16 %0, %1, %2, 0" : "=&v"(az[0][n]) : "v"(wf[s]), "v"(xf[j][2 * ks]));
                else asm volatile("v_mfma_f32_16x16x32_bf16 %0, %1, %2, %0" : "+v"(az[0][n]) : "v"(wf[s]), "v"(xf[j][2 * ks]));
                SCHED0();
                bf16x4 h0, h1;
                if constexpr ((s + ZD) % 18 == 0 && s + ZD < 162 && WF_B((s + ZD) / 18) != WF_B((s + ZD) / 18 - 1)) { unsigned& wb_ = wb; asm volatile("v_add_u32 %0, %1, %0" : "+v"(wb_) : "n"(WF_B((s + ZD) / 18) - WF_B((s + ZD) / 18 - 1))); }
                if constexpr (s + ZD < 162) h0 = trread(WF_P(s + ZD));
                SCHED0();
                if (s < 9) asm volatile("v_mfma_f32_16x16x32_bf16 %0, %1, %2, 0" : "=&v"(az[1][n]) : "v"(wf[s]), "v"(xf[j][2 * ks + 1]));
                else asm volatile("v_mfma_f32_16x16x32_bf16 %0, %1, %2, %0" : "+v"(az[1][n]) : "v"(wf[s]), "v"(xf[j][2 * ks + 1]));
                SCHED0();
                if constexpr (s + ZD < 162) { h1 = trread(WF_P(s + ZD) + 2304); wf[s + ZD] = cat8(h0, h1); }
                SCHED0();
            SEND
        }
        __builtin_amdgcn_s_setprio(GPRIO);
        LDSW(M_RL + w * 4) = 9u;
        const u16 *hk = KARG(8), *w_out = KARG(10);
        const u16* dve = DVE ? KARG(9) + (cd < D ? cd & ~1 : 0) : nullptr;
        unsigned dde = 0;
        STAMP(4);
        {
        const int lane = vl >> 4, fr = lane & 15, g = lane >> 4, q4 = (lane >> 2) & 3, p4 = lane & 3, l3 = lane >> 3;
        u64 miss;
#define NEEDZ(mask) do { if ((miss & (mask)) != 0ull) { unsigned sp_ = 0; \
            do { miss = __builtin_amdgcn_ballot_w64(ldflag(rs, fz) != tag); } while ((miss & (mask)) != 0ull && ++sp_ < SPIN_CAP); } FENCE(); } while (0)
        u32x4 zp[9];
        hasconv = w < 2 || (w == 2 && b < 64);
        const unsigned fk = WS_FK + w * 512 + lane * 8;
        {
            float nmb[2], k1[2], k2[2];
            u32x4 kl[3];
#define SETUP() do { _Pragma("unroll") for (int m = 0; m < 2; ++m) { \
                LAS float* p = (LAS float*)(lds + L_ROWST + (16 * w + 8 * m + (fr >> 1)) * 16 + (fr & 1) * 4); \
                const float rd = p[2]; \
                nmb[m] = -p[0]; k1[m] = muls(rd, GC1); k2[m] = muls(muls(rd, rd), muls(rd, GC2)); } \
            if (!SUL) WAIT_V(0); \
            GCHECK(gc, sc + 16 * 12288); \
            *(LAS float*)(lds + L_CS + ((2 * w + (lane >> 5)) * 16 + fr) * 4) = half_sum(CSUM(gc, 0), CSUM(gc, 1)); \
            cs8 = CSUM(gc, 2); \
            *(LAS float*)(lds + L_CS + ((8 + w) * 16 + fr) * 4) = half_sum(cs8, cs8); \
            asm volatile("" : "+v"(k1[0]), "+v"(k1[1]), "+v"(k2[0]), "+v"(k2[1]), "+v"(nmb[0]), "+v"(nmb[1])); } while (0)
            if (SUL) WAIT_V(0); else SETUP();
#define WTS (WOS > 4 ? WOS + 4 : 8)
#define WOLD() do { _Pragma("unroll") for (int i = 0; i < 3; ++i) dma1n(i * 1024, mk_rsrc(w_out, b < 216 ? FH * D * 2 : 0), L0 + L_WSCR + w * 3072, vo[i], 0); } while (0)
            SPIN(M_RL, 9);
            SPIN(M_RS, 1);
            STAMP(5);
            if (A21 == 2) FLAG_ISSUE(fzv[0], fz);
            if (A21 == 3) { stageA(20); FLAG_ISSUE(fzv[0], fz); stageA(21); }
            else if (A21) { stageA(20); stageA(21); }
            if (WOS == -2) WOLD();
            if (SUL) SETUP();
            miss = ~0ull;
            if (A21 >= 2) asm volatile("s_waitcnt vmcnt(%3)\n v_cmp_ne_u64 %0, %1, %2" : "=s"(miss) : "s"(tag), "v"(fzv[0]), "n"(A21 == 2 ? 8 : 4) : "memory");
            NEEDZ(0xf0ull);
            stageB(20);
            if (B21E == 2) stageB(21);
            if (!A21) { stageA(20); stageA(21); }
            if (WOS == -1) WOLD();
            WAIT_L0();
            BAR();
            if (B21E == 1) stageB(21);
            f32x4 cs_[10];
#pragma unroll
            for (int i = 0; i < 10; ++i) cs_[i] = *(LAS f32x4*)(lds + L_CS + i * 64 + g * 16);
            bf16x4 wt[6];
            kl[0] = *(LAS u32x4*)(lds + KVS + w * 1024 + vl); kl[1] = *(LAS u32x4*)(lds + KVS + 4096 + w * 1024 + vl); kl[2] = *(LAS u32x4*)(lds + KVS + 8192 + vl);
            asm volatile("" : "+v"(kl[0]), "+v"(kl[1]), "+v"(kl[2]));
            asm volatile("" : "+v"(cs_[0]), "+v"(cs_[1]), "+v"(cs_[2]), "+v"(cs_[3]), "+v"(cs_[4]), "+v"(cs_[5]), "+v"(cs_[6]), "+v"(cs_[7]), "+v"(cs_[8]), "+v"(cs_[9]));
            const unsigned so12 = SPO(12), so8 = SPO(8);
#define PBV 0
#define PBN (PBV == 2 || PBV == 4 ? 6 : PBV == 5 ? 1 : 4)
#define PB(u) (((u) >= PB1 && (u) < PB1 + PBN) || ((u) >= PB2 && (u) < PB2 + PBN))
#define XE(u) (((u) == 6 ? 2 : (u) == 7) + ((u) == WOS ? 3 : (u) >= WTS && (u) < WTS + 3) + ((u) == HKE) + PB(u))
            unsigned vpf = PBV == 0 ? (lane >> 1 & 7) * (S * 2) + (lane & 1) * 128 + ((pj >> 2) << 15) : PBV == 1 ? (lane >> 1 & 7) * (S * 2) + (lane & 1) * 128
                : PBV == 2 ? ((lane >> 1) % 6) * (S * 2) + (lane & 1) * 128 : PBV == 3 ? (lane >> 1 & 1) * (S * 2) + (lane & 1) * 128 + ((pj >> 2) << 15)
                : PBV == 4 ? (lane >> 1 & 1) * (S * 2) + (lane & 1) * 128 + ((pj >> 2) << 15) : (lane >> 1 & 7) * (S * 2) + (lane & 1) * 128 + ((pj >> 2) << 15);
            asm volatile("" : "+v"(vpf));
#define PN(u) (((u) == 4 - PNE && (PNX & 1)) || ((u) == 12 - PNE && (PNX & 2)))
            unsigned vpn = PNE ? (lane >> 1) * (S * 2) + (lane & 1) * 128 : lane < 32 ? lane * (S * 2) + 128 : 0x7fffff00u;
            if (PNX) asm volatile("" : "+v"(vpn));
#define NW(u) ((u) == 0 ? 63 : (u) < 5 ? ((u) == 1 && B21E ? 4 : 9) + 3 * ((u) == WOS + 1) + PN((u) - 1) : (u) == 5 ? 14 + PN(4) : 18 + XE((u) - 1) + XE((u) - 2) + XE((u) - 3) + PN((u) - 1) + PN((u) - 2))
            SFOR(u, 0, 19)
                constexpr int n = 19 + u, t = n + 3, tb = u < 4 ? t - 1 : t;
                const unsigned la = lw + (t & 3) * SLOT, lb = lw + (tb & 3) * SLOT + SL_A, as = as0 + KIDX(t) * 128, zs = zs0 + KIDX(tb) * SL_B;
                SCHED0();
                asm volatile("s_waitcnt vmcnt(%0)\n s_barrier" :: "n"(NW(u)), "s"(la), "s"(as), "s"(zs) : "memory");
                if (u >= 1 && u < 5) { u64 nm_; asm volatile("v_cmp_ne_u64 %0, %1, %2" : "=s"(nm_) : "s"(tag), "v"(fzv[u & 1]) : "memory"); miss &= nm_; }
                if (u < 4) FLAG_ISSUE(fzv[(u + 1) & 1], fz);
                if (u == 4) { NEEDZ(~0ull); stageB(25); }
                if (u == WTS + 6 && w == 3 && lane == 0) stflag(ws + WS_FK + b * 8, tag);
                if (u == 16 && b == 0 && w == 3 && lane == 0) stflag(ws + WS_E, tag);
                if constexpr (u == 0) {
                    stageA(22);
                    if (!B21E) stageB(21);
                    if (WOS == 0) WOLD();
                } else {
                constexpr int q = u - 1, m = q & 1, nn = q >> 1;
                if (u == WOS + 2) {
#pragma unroll
                    for (int i = 0; i < 3; ++i) { LAS char* p = lds + L_WSCR + w * 3072 + (4 * g + q4) * 96 + i * 32 + p4 * 8; wt[2 * i] = trread(p); wt[2 * i + 1] = trread(p + 1536); }
                }
                if constexpr (PN(u)) pf4<0>(rsp, L0 + L_DUMP, vpn, as0 + __builtin_amdgcn_readlane(rho, u < 8 ? 16 : 32) * 256);
                const f32x4 cc = cs_[nn];
                f32x4 a = az[m][nn];
                float u0, u1, u2, u3;
                asm volatile("s_mov_b32 m0, %15\n v_fma_f32 %4, %8, %12, %4\n buffer_load_dwordx4 %16, %20, %21 offen lds\n"
                             "v_fma_f32 %5, %9, %12, %5\n v_fma_f32 %6, %10, %12, %6\n v_fma_f32 %7, %11, %12, %7\n"
                             "buffer_load_dwordx4 %17, %20, %21 offen offset:1024 lds\n"
                             "v_mul_f32 %0, %4, %4\n v_mul_f32 %1, %5, %5\n v_mul_f32 %2, %6, %6\n v_mul_f32 %3, %7, %7\n"
                             "buffer_load_dwordx4 %18, %20, %21 offen offset:2048 lds\n"
                             "v_fma_f32 %0, %0, %14, %13\n v_fma_f32 %1, %1, %14, %13\n v_fma_f32 %2, %2, %14, %13\n v_fma_f32 %3, %3, %14, %13\n"
                             "buffer_load_dwordx4 %19, %20, %21 offen offset:3072 lds\n"
                             "v_mul_f32 %0, %0, %4\n v_mul_f32 %1, %1, %5\n v_mul_f32 %2, %2, %6\n v_mul_f32 %3, %3, %7"
                             : "=&v"(u0), "=&v"(u1), "=&v"(u2), "=&v"(u3), "+v"(a[0]), "+v"(a[1]), "+v"(a[2]), "+v"(a[3])
                             : "v"(cc[0]), "v"(cc[1]), "v"(cc[2]), "v"(cc[3]), "v"(nmb[m]), "v"(k1[m]), "v"(k2[m]),
                               "s"(la), "v"(vA[0]), "v"(vA[1]), "v"(vA[2]), "v"(vA[3]), "s"(rsp), "s"(as) : "memory");
                if (u < 4) NEEDZ(3ull << (2 * (tb - 18)));
                asm volatile("s_mov_b32 m0, %8\n v_exp_f32 %0, %0\n buffer_load_dwordx4 %9, %10, %11 offen sc1 lds\n"
                             "v_exp_f32 %1, %1\n v_exp_f32 %2, %2\n v_exp_f32 %3, %3\n"
                             "buffer_load_dwordx4 %9, %10, %11 offen offset:1024 sc1 lds\n"
                             "v_add_f32 %0, 1.0, %0\n v_add_f32 %1, 1.0, %1\n v_add_f32 %2, 1.0, %2\n v_add_f32 %3, 1.0, %3\n"
                             "buffer_load_dwordx4 %9, %10, %11 offen offset:2048 sc1 lds\n"
                             "v_rcp_f32 %0, %0\n v_rcp_f32 %1, %1\n v_rcp_f32 %2, %2\n v_rcp_f32 %3, %3\n"
                             "buffer_load_dwordx4 %9, %10, %11 offen offset:3072 sc1 lds\n"
                             "v_mul_f32 %0, %0, %4\n v_mul_f32 %1, %1, %5\n v_mul_f32 %2, %2, %6\n v_mul_f32 %3, %3, %7\n"
                             "s_mov_b32 m0, %12\n s_mov_b32 exec_hi, 0\n buffer_load_dwordx4 %9, %10, %13 offen sc1 lds\n s_mov_b32 exec_hi, -1\n"
                             "v_cvt_pk_bf16_f32 %0, %0, %1\n v_cvt_pk_bf16_f32 %1, %2, %3"
                             : "+v"(u0), "+v"(u1), "+v"(u2), "+v"(u3)
                             : "v"(a[0]), "v"(a[1]), "v"(a[2]), "v"(a[3]), "s"(lb), "v"(vl), "s"(wr), "s"(zs), "s"(lh + (tb & 3) * SLOT + SL_A), "s"(zs + 16384 - w * 3584) : "memory");
                if (u == WOS) WOLD();
                if (u >= WTS && u < WTS + 3) st16c(rs, b < 216 ? vl : 0x7fffff00u, WS_WT + (b * 12 + w * 3 + u - WTS) * 1024, __builtin_bit_cast(u32x4, cat8(wt[2 * (u - WTS) % 6], wt[(2 * (u - WTS) + 1) % 6])));
                if (u == WOS + 2) asm volatile("" : "+v"(wt[0]), "+v"(wt[1]), "+v"(wt[2]), "+v"(wt[3]), "+v"(wt[4]), "+v"(wt[5]));
                if constexpr ((SPF & 2) && u >= SP2 && u < SP2 + 16) SPL(so12, u - SP2);
                if constexpr ((SPF & 16) && u >= 3 && u < 11) SPL(so8, u + 5);
                if constexpr ((SPF & 8) && u >= S8 && u < S8 + 16) { SPL(u < S8 + 8 ? so8 : so12, 2 * ((u - S8) & 7)); SPL(u < S8 + 8 ? so8 : so12, 2 * ((u - S8) & 7) + 1); }
                if constexpr (DVE && u == DVE) { unsigned& d_ = dde; asm volatile("s_load_dword %0, %1, 0x0 ; dve" : "+s"(d_) : "s"(dve) : "memory"); }
                if constexpr (u == HKE && u >= 6) PF_HK();
                if constexpr (PB(u)) { constexpr int k = u - (u < PB2 ? PB1 : PB2);
                    pf4<0>(rsp, L0 + L_DUMP, PBV < 2 ? vpf ^ (k << 15) : PBV == 2 ? vpf + (k == 5 ? 26 : k * 6) * (S * 2) : PBV == 3 ? vpf + k * 2 * (S * 2) : PBV == 4 ? vpf + (k < 4 ? k * 2 : (k - 4) * 2 + 8) * (S * 2) : vpf, as0 + __builtin_amdgcn_readlane(rho, u < PB2 ? 32 : 48) * 256); }
                if (u == 6 || u == 7) {
                    const unsigned so = WS_KVT + (KC * 72 * S + KR * 64) * 2, ko_ = l3 * (S * 2) + ((lane - (l3 >> 2) - 2 * w) & 7) * 16;
                    if (u == 6) st16c(rs, ko_, so + w * (8 * S * 2), kl[0]);
                    if (u == 6) st16c(rs, ko_, so + (w + 4) * (8 * S * 2), kl[1]);
                    if (u == 7) st16c(rs, w == 0 ? ko_ : 0x7fffff00u, so + 64 * S * 2, kl[2]);
                }
                if (q == 15) { cs_[8][0] = adds(cs_[8][0], cs_[9][0]); cs_[8][1] = adds(cs_[8][1], cs_[9][1]); cs_[8][2] = adds(cs_[8][2], cs_[9][2]); cs_[8][3] = adds(cs_[8][3], cs_[9][3]); }
                const int n4 = nn - (nn > 4), zq = nn == 4 ? 8 : m * 4 + (n4 >> 1), h = nn == 4 ? m : n4 & 1;
                zp[zq][h * 2] = __float_as_uint(u0); zp[zq][h * 2 + 1] = __float_as_uint(u1);
                asm volatile("" : "+v"(zp[zq]));
                }
            SEND
        }
        SCHED0();
        if (!SPF) STAMP(6);
#define TST(i) do { } while (0)
#pragma unroll 1
        for (int n = 38; n < 42; ++n) {
            WAIT_V(18);
            BAR();
            stageA(n + 3); stageB(n + 3);
        }
        asm volatile("s_waitcnt lgkmcnt(0)" : "+s"(sd), "+s"(dde) :: "memory");
        FLAG_ISSUE(fzv[0], fk);
#pragma unroll 1
        for (int n = 42; n < 44; ++n) {
            WAIT_V(19);
            BAR();
            stageA(n + 3); stageB(n + 3);
        }
        WAIT_V(18);
        asm volatile("" : "+v"(fzv[0]));
        if (!allset(fzv[0], tag)) spin_flags(rs, fk, tag);
        FENCE();
        TAILS(); TAILS2();
        const u16* dv = DVE ? nullptr : KARG(9);
        if (!DVE) asm volatile("" : "+s"(dv));
        ConvIn cin;
        BAR();
        TST(44);
        stageA(47); stageB(47);
        WAIT_V(18);
        BAR();
        TST(45);
        stageA(48); stageB(48);
        WAIT_V(18);
        BAR();
        TST(46);
        stageA(49); stageB(49);
        WAIT_V(18);
        BAR();
        TST(47);
        const unsigned lv = vl, lv4 = (lane >> 3 ^ ct) & 1 ? 0x7fffff00u : lv;
#define ZPUT(nv, late) do { SCHED0(); TST(49); \
        if (late) { WAIT_V(nv); BAR(); TST(48); } \
        _Pragma("unroll") for (int q = 0; q < 8; ++q) *(LAS u32x4*)(lds + ZS + w * 8192 + q * 1024 + lane * 16) = zp[q]; \
        *(LAS u32x4*)(lds + Z8OFF(w) + lane * 16) = zp[8]; \
        FENCE(); if (ZLATE) LDSW(M_RL + w * 4) = 10u; \
        SCHED0(); TST(50); if (!(late)) { WAIT_V(nv); WAIT_L0(); TST(51); BAR(); TST(48); } } while (0)
        if (hasconv) {
            conv_load(cin, hk, dv, wr, cd, lane);
            if (DVE) cin.dd = dde;
            ZPUT(12, ZLATE >= 1);
            __builtin_amdgcn_s_setprio(VPRIO);
            G3_LDBA(0);
            SCHED0();
            WAIT_V(5);
            CONV_PIN(cin);
            if (w == 2) SPIN(M_RC, 3);
            STAMP(7);
            conv_stage(cin, lds + redo, lane, [&](auto i_) __attribute__((always_inline)) { constexpr int k_ = decltype(i_)::v + 1; auto& bq_ = bq; { auto& bq = bq_; G3_LDBA(k_); } });
            __builtin_amdgcn_s_setprio(CRP);
            SCHED0();
            STAMP(8);
            conv_run(cin.dd, rs, lds + redo, cd, lane);
            STAMP(9);
            FP_ISSUE(); G3_DMA1(redo, sB, sB4);
            WAIT_V(9);
            if (lane == 0) stflag(ws + WS_FY + cd * 8, tag);
            XST(29);
            G3_DMA2(redo, sB, sB4);
            STAMP(20);
            FP_CHECK(19);
        } else {
            G3_LDBA(0);
            ZPUT(5, ZLATE >= 2);
            __builtin_amdgcn_s_setprio(DRP);
            G3_LDBA(1); G3_LDBA(2); G3_LDBA(3); G3_LDBA(4);
            SCHED0();
            STAMP(8);
            if (PCD) {
                SPIN(M_RC, 4);
                SFOR(i, 0, PCN)
                    if constexpr (i < 16) dma16c<i * 1024, (5 + i / 4) * KST + (i & 3) * 1024>(wr, L0 + redo, lane * 16, sB);
                    __builtin_amdgcn_s_sleep(PCD);
                SEND
            }
            SPIN(M_RC, RGW);
#pragma unroll 1
            for (int v = 5 - w; v < (b < 64 ? 4 : 6 - w); ++v) G3_DMA(redoff(0) + v * RED_W, sB + (v - 4 + BHIT - w + 2 * kx) * (9 * KST), sB4 + (v - 4 + BHIT - w + 2 * kx) * (9 * KST));
            if (PCD) {
                SFOR(i, PCN, 16)
                    dma16c<i * 1024, (5 + i / 4) * KST + (i & 3) * 1024>(wr, L0 + redo, lane * 16, sB);
                SEND
                dma16c<16384, 0>(wr, L0 + redo, G3_HO + (lane < 32 ? 5 : 6) * KST, sB4); dma16c<17408, 0>(wr, L0 + redo, G3_HO + (lane < 32 ? 7 : 8) * KST, sB4);
            } else G3_DMA(redo, sB, sB4);
            WAIT_V(PCD ? 18 - PCN : 18);
            LDSW(M_FL + w * 4) = 4u;
            if (XPF && w == 3) {
                pf4<0>(rx, L0 + L_DUMP, lane * (D * 2), rg * (64 * D * 2) + ct * 144); pf4<140>(rx, L0 + L_DUMP, lane * (D * 2), rg * (64 * D * 2) + ct * 144);
                pf4c<WS_ST1>(wr, L0 + L_DUMP, lane * 8, rg * 512);
            }
            for (int i_ = 0; i_ < PSL; ++i_) __builtin_amdgcn_s_sleep(16);
            STAMP(20);
            FP_ISSUE(); FP_CHECK(0);
        }
        }
    } else {
        asm volatile(".Lmega_comp:");
        unsigned k8_;
        if (K8P == 2) asm volatile("s_load_dword %0, %1, 0x40" : "=s"(k8_) : "s"(__builtin_amdgcn_kernarg_segment_ptr()) : "memory");
        if (tid < 16) *(LAS unsigned*)(lds + L_MISC + tid * 4) = 0u;
        if (EPF != 3) {
            const char* pc; unsigned len, len2, back;
            asm volatile("s_getpc_b64 %0\n.Lpf_here%=:\n s_mov_b32 %1, .Lmega_tail+1024-.Lmega_head\n s_mov_b32 %2, .Lpf_here%=-.Lmega_head\n s_mov_b32 %3, .Lmega_cend-.Lmega_head" : "=s"(pc), "=s"(len), "=s"(back), "=s"(len2));
            const u32x4 rc = mk_rsrc(pc - back, len > len2 ? len : len2);
            if (wid < CPFW) pf4<0>(rc, L0 + L_DUMP, ((r * CPFW + wid) & 7) * 8192 + lane * 128, 0);
        }
        if (WPA == -3) PF_WIN();
        u32x4 zp[9];
        unsigned bsw[4];
        const int wm = w & 1, wn = w >> 1;
#define BSLD() do { _Pragma("unroll") for (int m = 0; m < 4; ++m) asm volatile("buffer_load_ushort %0, %1, %2, %3 offen" : "=v"(bsw[m]) : "v"(fr * 2), "s"(mk_rsrc(b_sp, S * 2)), "s"((pi * 128 + 64 * wm + 16 * ((m + 2 * wn) & 3)) * 2) : "memory"); } while (0)
        if (EVB < 2) BSLD();
        if (SKP == 1 && w == 2) pf4<0>(mk_rsrc(KARG(7), D * 8), L0 + L_DUMP, lane < 6 ? ((KC * 576) & ~127) + lane * 128 : 0x7fffff00u, 0);
#pragma unroll
        for (int i = 0; i < 5; ++i) if (!EVB || EVB >= 4) SET_VB(i);
        SET_VSP();
        const unsigned as0 = (pi * 128 + 32 * w) * (S * 2);
        asm volatile("" : "+v"(vB[0]), "+v"(vB[1]), "+v"(vB[2]), "+v"(vB[3]), "+v"(vB[4]), "+v"(vA[0]), "+v"(vA[1]), "+v"(vA[2]), "+v"(vA[3]));
        unsigned ok[3], ov[3], kd8, kvo, krd;
        {
#pragma unroll
            for (int i = 0; i < 3; ++i) {
                const unsigned c = i * 64 + lane, rr = c / 9, o = (KR * 64 + 16 * w + rr) * (D * 2) + KC * 144 + (c - rr * 9) * 16 - i * 1024;
                ok[i] = i == 2 && rr >= 19 ? 0x7fffff00u : o - 3 * (D * 2);
                ov[i] = i == 2 && rr >= 16 ? 0x7fffff00u : o;
            }
            const int ln = lane < 36 ? lane : 35, sg = ln >= 18, kd = ln - sg * 18;
            kvo = L0 + KVS + kd * 512 + ((2 * w + sg + kd) & 7) * 16; kd8 = kd * 32 + KC * 576; krd = sg * 1152 + kd * 8;
            asm volatile("" : "+v"(ok[0]), "+v"(ok[1]), "+v"(ok[2]), "+v"(ov[0]), "+v"(ov[1]), "+v"(ov[2]), "+v"(kd8), "+v"(kvo), "+v"(krd));
        }
        if (EVB >= 2) { ARGS_READY(); BSLD(); }
        const u32x4 rsp = mk_rsrc(w_sp, S * S * 2);
        u32x4 sk[2], p2[4];
        u64 sdc = 0;
        {
            f32x4 az[2][9];
#pragma unroll
            for (int n = 0; n < 9; ++n) az[0][n] = az[1][n] = zero4;
            if (ZPIN) { PIN9(az[0]); PIN9(az[1]); }
            unsigned e0 = L0 + (32 * w + fr) * 128 + ((g ^ (fr >> 1)) << 4), e1 = e0 ^ 64, pw = L0 + SL_A + bb;
            unsigned ea[2][3] = {{e0, e1, pw}, {e0 + 2 * SLOT, e1 + 2 * SLOT, pw + 2 * SLOT}};
            if (AU4) { asm volatile("" : "+v"(ea[0][0]), "+v"(ea[0][1]), "+v"(ea[0][2]), "+v"(ea[1][0]), "+v"(ea[1][1]), "+v"(ea[1][2])); e0 = ea[0][0]; e1 = ea[0][1]; pw = ea[0][2]; }
            bf16x8 ax[2], bx[9], ay[2], by[9];
#define ZRD_X() do { ax[0] = RDP(e0, 0); ax[1] = RDP(e1, 2048); _Pragma("unroll") for (int n = 0; n < 9; ++n) bx[n] = TRP(pw, n * 32); } while (0)
#define ZRD_Y() do { ay[0] = RDP(e1, 0); ay[1] = RDP(e0, 2048); _Pragma("unroll") for (int n = 0; n < 9; ++n) by[n] = TRP(pw, n * 32 + 9216); } while (0)
#define ZMM(A, B, n0_, n1_) do { _Pragma("unroll") for (int n = n0_; n < n1_; ++n) { az[0][n] = mfma16(A[0], B[n], az[0][n]); az[1][n] = mfma16(A[1], B[n], az[1][n]); } } while (0)
#define ZSCHED(rest) do { _Pragma("unroll") for (int i_ = 0; i_ < 6; ++i_) { SGB(0x008, 1); SGB(0x100, 2); } _Pragma("unroll") for (int i_ = 0; i_ < 8; ++i_) { SGB(0x008, 1); SGB(0x100, 1); } if (rest) SGB(0x008, rest); SCHED0(); } while (0)
            XST(26);
            if (WPA == -4) PF_WIN();
            if (K8P == 0) asm volatile("" :: "s"(KARG(8)));
            if ((TLT & 1) && w == 2) pf4<0>(mk_rsrc(KARG(10), FH * D * 2), L0 + L_DUMP, lane < 6 ? (b / 27) * (288 * D * 2) + lane * 65536 : 0x7fffff00u, 0);
            if ((TLT & 2) && w == 2) pf4<0>(rsp, L0 + L_DUMP, lane < 9 ? pi * (128 * S * 2) + lane * 65536 : 0x7fffff00u, 0);
            if ((TLT & 4) && w == 3) { const unsigned o_ = lane < 3 ? KR * 64 * (D * 2) + lane * 32768 : 0x7fffff00u; pf4<0>(mk_rsrc(KARG(5), SZ_X), L0 + L_DUMP, o_, 0); pf4<0>(mk_rsrc(KARG(6), SZ_X), L0 + L_DUMP, o_, 0); }
            if ((TLT & 8) && w == 3) pf4<0>(mk_rsrc(KARG(8), SZ_X), L0 + L_DUMP, lane == 0 ? 2 * b * (S * 2) : lane == 1 ? (512 + b) * (S * 2) : 0x7fffff00u, 0);
            if (K8P == 2) asm volatile("s_waitcnt lgkmcnt(0)" : "+s"(k8_) :: "memory");
            XST(28);
            if (!TGC) TAG_READY();
            XST(27);
            __builtin_amdgcn_s_setprio(CPRIO);
            BAR();
            STAMP(1);
            ZRD_X();
            WAIT_LB0();
#pragma unroll 1
            for (int t = 0; t < 8; ++t) {
                const int dl = (t & 3) == 3 ? -3 * SLOT : SLOT;
                ZRD_Y();
                ZMM(ax, bx, 0, 7);
                ZSCHED(0);
                ADV(e0, dl); ADV(e1, dl); ADV(pw, dl);
                ZMM(ax, bx, 7, 9); ZMM(ay, by, 0, 2);
                SCHED0();
                WAIT_LB0();
                BAR();
                ZRD_X();
                ZMM(ay, by, 2, 9);
                ZSCHED(0);
            }
            ZRD_Y();
            ZMM(ax, bx, 0, 7);
            ZSCHED(0);
            ZMM(ax, bx, 7, 9); ZMM(ay, by, 0, 2);
            SCHED0();
            WAIT_LB0();
            BAR();
            f32x4 st[4], cq[3];
#pragma unroll
            for (int j = 0; j < 4; ++j) st[j] = *(LAS f32x4*)(lds + L_ROWST + (16 * w + 4 * g + j) * 16);
#pragma unroll
            for (int j = 0; j < 3; ++j) cq[j] = *(LAS f32x4*)(lds + L_CS + fr * 48 + j * 16);
            ZMM(ay, by, 2, 9);
            SCHED_IL(7, 14);
            STAMP(2);
            if (TGC) { asm volatile("" : "+v"(st[0]), "+v"(st[1]), "+v"(st[2]), "+v"(st[3]), "+v"(cq[0]), "+v"(cq[1]), "+v"(cq[2]));
                asm volatile("s_load_dwordx2 %0, %1, 0x0" : "=s"(tag) : "s"(ws + WS_E) : "memory"); SCHED0(); }
            f32x2 sum[4], sq[4], k1[4], k2[4];
            cq[2][0] += cq[2][1];
#pragma unroll
            for (int j = 0; j < 4; ++j) { const f32x2 rd = hi2(st[j]); k1[j] = rd * GC1; k2[j] = rd * rd * rd * GC2; }
            {
                f32x2 zv[9][4], te[2][4];
#pragma unroll
                for (int n = 0; n < 9; ++n)
#pragma unroll
                    for (int j = 0; j < 4; ++j) zv[n][j] = (j & 1) ? hi2(az[j >> 1][n]) : lo2(az[j >> 1][n]);
#define J4(x) do { _Pragma("unroll") for (int j = 0; j < 4; ++j) { x; } SCHED0(); } while (0)
#define G_ARG(n) do { const float cc = -(((n) & 1) ? __uint_as_float(movs(__float_as_uint(cq[(n) >> 2][(n) & 3]))) : cq[(n) >> 2][(n) & 3]); \
                J4(zv[n][j] = fma2(lo2(st[j]), (f32x2){cc, cc}, zv[n][j])); J4(te[(n) & 1][j] = zv[n][j] * zv[n][j]); } while (0)
#define G_AR2(n) do { J4(te[(n) & 1][j] = fma2(te[(n) & 1][j], k2[j], k1[j])); J4(te[(n) & 1][j] *= zv[n][j]); } while (0)
#define G_SUM(n) do { J4(sum[j] += zv[n][j]); J4(sq[j] = fma2(zv[n][j], zv[n][j], sq[j])); } while (0)
#define WINOFF(i_) (as0 + ((pj >> 2) << 15) + (pi ^ ((((i_) ^ (pj & 3)) == 7 || ((i_) ^ (pj & 3)) == 8) ? (i_) ^ (pj & 3) ^ 15 : (i_) ^ (pj & 3))) * 256)
                const unsigned sg0 = WINOFF(4), sg1 = WINOFF(8);
#define SPC(k_) do { if (SPG && (k_) < SPG) { asm volatile("s_buffer_load_dwordx2 %0, %1, %2 offset:%3 ; spc" : "+s"(sdc) : "s"(rsp), "s"((k_) < 16 ? sg0 : sg1), "n"(((k_) & 7) * 4096 + ((k_) >> 3 & 1) * 128 + 60) : "memory"); SCHED0(); } } while (0)
                SCHED0();
                G_ARG(0); G_AR2(0);
#pragma unroll
                for (int n = 0; n < 9; ++n) {
                    f32x2* t = te[n & 1];
                    SPC(SGR * n);
                    J4((t[j] = (f32x2){__builtin_amdgcn_exp2f(t[j].x), __builtin_amdgcn_exp2f(t[j].y)}));
                    if (SGR > 2) SPC(SGR * n + 2);
                    if (n == 1) J4(sq[j] = zv[0][j] * zv[0][j]; sum[j] = zv[0][j]);
                    if (n > 1) G_SUM(n - 1);
                    J4((t[j] += (f32x2){1.0f, 1.0f}));
                    if (n < 8) G_ARG(n + 1);
                    SPC(SGR * n + 1);
                    J4((t[j] = (f32x2){__builtin_amdgcn_rcpf(t[j].x), __builtin_amdgcn_rcpf(t[j].y)}));
                    if (SGR > 3) SPC(SGR * n + 3);
                    if (n < 8) G_AR2(n + 1);
                    J4(zv[n][j] *= t[j]);
                    asm volatile("" : "+v"(zv[n][0]), "+v"(zv[n][1]), "+v"(zv[n][2]), "+v"(zv[n][3]));
                }
                G_SUM(8);
#pragma unroll
                for (int n = 0; n < 9; ++n)
#pragma unroll
                    for (int m = 0; m < 2; ++m) az[m][n] = (f32x4){zv[n][2 * m].x, zv[n][2 * m].y, zv[n][2 * m + 1].x, zv[n][2 * m + 1].y};
            }
#pragma unroll
            for (int j = 0; j < 4; ++j) { const f32x2 rd = hi2(st[j]); sum[j] *= rd; sq[j] *= rd * rd; asm volatile("" : "+v"(sum[j]), "+v"(sq[j])); }
            SCHED0();
            if (TGC) { asm volatile("s_waitcnt lgkmcnt(0)" : "+s"(tag) :: "memory"); tag += TAG_INC; }
            LDSW(M_RC + w * 4) = 1u;
            SCHED0();
            float r0 = sum[0].x, r1 = sq[0].x, r2 = sum[0].y, r3 = sq[0].y, r4 = sum[1].x, r5 = sq[1].x, r6 = sum[1].y, r7 = sq[1].y;
            asm volatile(
                "s_nop 1\n"
                "v_add_f32_dpp %0, %0, %0 row_mirror row_mask:0xf bank_mask:0x3\n v_add_f32_dpp %1, %1, %1 row_mirror row_mask:0xf bank_mask:0x3\n"
                "v_add_f32_dpp %2, %2, %2 row_mirror row_mask:0xf bank_mask:0x3\n v_add_f32_dpp %3, %3, %3 row_mirror row_mask:0xf bank_mask:0x3\n"
                "v_add_f32_dpp %4, %4, %4 row_mirror row_mask:0xf bank_mask:0x3\n v_add_f32_dpp %5, %5, %5 row_mirror row_mask:0xf bank_mask:0x3\n"
                "v_add_f32_dpp %6, %6, %6 row_mirror row_mask:0xf bank_mask:0x3\n v_add_f32_dpp %7, %7, %7 row_mirror row_mask:0xf bank_mask:0x3\n"
                "v_add_f32_dpp %0, %8, %8 row_mirror row_mask:0xf bank_mask:0xc\n v_add_f32_dpp %1, %9, %9 row_mirror row_mask:0xf bank_mask:0xc\n"
                "v_add_f32_dpp %2, %10, %10 row_mirror row_mask:0xf bank_mask:0xc\n v_add_f32_dpp %3, %11, %11 row_mirror row_mask:0xf bank_mask:0xc\n"
                "v_add_f32_dpp %4, %12, %12 row_mirror row_mask:0xf bank_mask:0xc\n v_add_f32_dpp %5, %13, %13 row_mirror row_mask:0xf bank_mask:0xc\n"
                "v_add_f32_dpp %6, %14, %14 row_mirror row_mask:0xf bank_mask:0xc\n v_add_f32_dpp %7, %15, %15 row_mirror row_mask:0xf bank_mask:0xc\n"
                "v_add_f32_dpp %0, %0, %0 row_half_mirror row_mask:0xf bank_mask:0x5\n v_add_f32_dpp %1, %1, %1 row_half_mirror row_mask:0xf bank_mask:0x5\n"
                "v_add_f32_dpp %2, %2, %2 row_half_mirror row_mask:0xf bank_mask:0x5\n v_add_f32_dpp %3, %3, %3 row_half_mirror row_mask:0xf bank_mask:0x5\n"
                "v_add_f32_dpp %0, %4, %4 row_half_mirror row_mask:0xf bank_mask:0xa\n v_add_f32_dpp %1, %5, %5 row_half_mirror row_mask:0xf bank_mask:0xa\n"
                "v_add_f32_dpp %2, %6, %6 row_half_mirror row_mask:0xf bank_mask:0xa\n v_add_f32_dpp %3, %7, %7 row_half_mirror row_mask:0xf bank_mask:0xa\n"
                "v_add_f32_dpp %0, %0, %0 quad_perm:[1,0,3,2] row_mask:0xf bank_mask:0xf\n v_add_f32_dpp %1, %1, %1 quad_perm:[1,0,3,2] row_mask:0xf bank_mask:0xf\n"
                "v_add_f32_dpp %2, %2, %2 quad_perm:[1,0,3,2] row_mask:0xf bank_mask:0xf\n v_add_f32_dpp %3, %3, %3 quad_perm:[1,0,3,2] row_mask:0xf bank_mask:0xf\n"
                "v_add_f32_dpp %0, %0, %0 quad_perm:[2,3,0,1] row_mask:0xf bank_mask:0xf\n v_add_f32_dpp %1, %1, %1 quad_perm:[2,3,0,1] row_mask:0xf bank_mask:0xf\n"
                "v_add_f32_dpp %2, %2, %2 quad_perm:[2,3,0,1] row_mask:0xf bank_mask:0xf\n v_add_f32_dpp %3, %3, %3 quad_perm:[2,3,0,1] row_mask:0xf bank_mask:0xf\n"
                : "+v"(r0), "+v"(r1), "+v"(r2), "+v"(r3), "+v"(r4), "+v"(r5), "+v"(r6), "+v"(r7)
                : "v"(sum[2].x), "v"(sq[2].x), "v"(sum[2].y), "v"(sq[2].y), "v"(sum[3].x), "v"(sq[3].x), "v"(sum[3].y), "v"(sq[3].y));
            const unsigned tag32 = (unsigned)tag, so2 = WS_ST2 + pi * 16384 + w * 256;
            st16c(rs, p4 == 0 ? lane * 4 : 0x7fffff00u, so2 + pj * 1024, rec4(r0, r1, r2, r3, tag32));
            SCHED0();
            STAMP(3);
            {
                u32x4 pv[4];
                const u32x4 rwi = mk_rsrc(w_in, D * F * 2);
                const u16 *kin = KARG(5), *vin = KARG(6), *shk = KARG(7);
                const unsigned po = g * 1024 + fr * 16;
#define GATH2(p_) do { _Pragma("unroll") for (int i = 0; i < 4; ++i) ALD("x4", p_[i], po, wr, so2 + i * 4096, " sc1"); } while (0)
#define GBAD2(p_) (__builtin_amdgcn_ballot_w64((((p_[0][3] ^ tag32) | (p_[1][3] ^ tag32)) | ((p_[2][3] ^ tag32) | (p_[3][3] ^ tag32))) != 0u) != 0ull)
#define PIN4(p_) asm volatile("" : "+v"(p_[0]), "+v"(p_[1]), "+v"(p_[2]), "+v"(p_[3]))
                if (G1P == 0) GATH2(pv);
                SPIN(M_RL, 1);
                if (SPG) asm volatile("s_waitcnt lgkmcnt(0) ; spc end" : "+s"(sdc) :: "memory");
                if (G1P == 1) GATH2(pv);
                DMA_W1(7);
                if (G1P == 2) GATH2(pv);
#define KVLD() do { _Pragma("unroll") for (int i = 0; i < 3; ++i) dma1(i * 1024, mk_rsrc(vin, SZ_X), L0 + L_WSCR + w * 3072, ov[i], 0); \
                    asm volatile("buffer_load_dwordx4 %0, %2, %3, 0 offen\n buffer_load_dwordx4 %1, %2, %3, 0 offen offset:16" : "=&v"(sk[0]), "=&v"(sk[1]) : "v"(kd8), "s"(mk_rsrc(shk, D * 8)) : "memory"); } while (0)
#define KKLD() do { _Pragma("unroll") for (int i = 0; i < 3; ++i) dma1(i * 1024, mk_rsrc(kin, SZ_X), lw, ok[i], 0); } while (0)
                if (KS1 == 0) KVLD();
                SPIN(M_RL, 2);
                if (G1P == 4) GATH2(p2);
                DMA_W1(8);
                if (G1P != 4) GATH2(p2);
                if (KS1 == 1) KVLD();
                KKLD();
                bool again = true;
                if (G1P < 3) { WAIT_V(G1P < 2 ? 22 : 17); PIN4(pv); again = GBAD2(pv); }
                if (again) {
                    WAIT_V((KS1 == 1 ? 8 : 3) + (G1P == 4 ? 5 : 0));
                    if (G1P != 4) LDSW(M_RC + w * 4) = 2u;
                    unsigned spins = 0;
                    for (;;) {
                        PIN4(p2);
                        if (!GBAD2(p2) || ++spins > SPIN_CAP) break;
                        if (GSL) __builtin_amdgcn_s_sleep(GSL);
                        GATH2(p2);
                        WAIT_V(0);
                        if (G1P == 4) LDSW(M_RC + w * 4) = 2u;
                    }
#pragma unroll
                    for (int i = 0; i < 4; ++i) pv[i] = p2[i];
                }
                STAMP(5);
                if (WPA == 21) { WAIT_V(0); PF_WIN(); }
                const f32x2 c = rec_consts<4>(pv, 1.0f / FH);
                LAS float* p = (LAS float*)(lds + L_ZST + w * 256 + fr * 16 + (lane >> 5) * 4);
                p[0] = muls(c.y, *(LAS float*)(lds + L_ROWST + (16 * w + fr) * 16 + 8 + (lane >> 5) * 4)); p[2] = muls(-c.x, c.y);
            }
            FENCE();
            const unsigned zdst = WS_Z2N + ((pj * 32 + pi * 2 + (w >> 1)) * 18 + (w & 1)) * 1024;
#pragma unroll
            for (int j = 0; j < 4; ++j) st[j] = *(LAS f32x4*)(lds + L_ZST + w * 256 + (4 * g + j) * 16);
#pragma unroll
            for (int n = 0; n < 9; ++n) {
#pragma unroll
                for (int j = 0; j < 4; ++j) { const f32x4 a = az[j >> 1][n]; zp[n][j] = norm2s((j & 1) ? hi2(a) : lo2(a), st[j]); }
                st16c(rs, lane * 16, zdst + n * 2048, zp[n]);
                SCHED0();
            }
        }
        STAMP(6);
        {
            u32x2 kw[11], vw[8];
            if (WPA == 24) PF_WIN();
            WAIT_V(9 + (WPA == 21 || WPA == 24));
            LDSW(M_RC + w * 4) = 2u;
            asm volatile("" : "+v"(sk[0]), "+v"(sk[1]), "+v"(p2[0]), "+v"(p2[1]), "+v"(p2[2]), "+v"(p2[3]));
#pragma unroll
            for (int i = 0; i < 11; ++i) kw[i] = *(LAS u32x2*)(lds + w * 4096 + krd + i * 144);
#pragma unroll
            for (int i = 0; i < 8; ++i) vw[i] = *(LAS u32x2*)(lds + L_WSCR + w * 3072 + krd + i * 144);
            SCHED0();
            SPIN(M_RL, 6);
#pragma unroll
            for (int n = 0; n < 9; ++n) *(LAS u32x4*)(lds + (w < 2 ? T18 : T19) + SL_A + n * 2048 + (w & 1) * 1024 + lane * 16) = zp[n];
            SCHED0();
#define HALF(w_, c) (((c) & 1) ? bfhi(w_) : bflo(w_))
#define KVC(c) do { SCHED0(); asm volatile("" : "+v"(sk[c >> 1])); \
                const float h[4] = {bflo(sk[c >> 1][(c & 1) * 2]), bfhi(sk[c >> 1][(c & 1) * 2]), bflo(sk[c >> 1][(c & 1) * 2 + 1]), bfhi(sk[c >> 1][(c & 1) * 2 + 1])}; \
                float kk[11], o[8]; \
                _Pragma("unroll") for (int i = 0; i < 11; ++i) kk[i] = HALF(kw[i][c >> 1], c); \
                _Pragma("unroll") for (int i = 0; i < 8; i += 2) tap4d(o[i], o[i + 1], kk + i, h, HALF(vw[i][c >> 1], c), HALF(vw[i + 1][c >> 1], c)); \
                *(LAS u32x4*)(size_t)(kvo + c * 128) = (u32x4){pk2(o[7], o[6]), pk2(o[5], o[4]), pk2(o[3], o[2]), pk2(o[1], o[0])}; SCHED0(); } while (0)
            asm volatile("" : "+v"(kw[0]), "+v"(kw[1]), "+v"(kw[2]), "+v"(kw[3]), "+v"(kw[4]), "+v"(kw[5]), "+v"(kw[6]), "+v"(kw[7]), "+v"(kw[8]), "+v"(kw[9]), "+v"(kw[10]));
            asm volatile("" : "+v"(vw[0]), "+v"(vw[1]), "+v"(vw[2]), "+v"(vw[3]), "+v"(vw[4]), "+v"(vw[5]), "+v"(vw[6]), "+v"(vw[7]));
            if (FE == 2) { dma4v(rsp, lw + T18, vA, as0 + pi * 256); dma4v(rsp, lw + T19, vA, as0 + pi * 256 + 128); }
            if (WPA == 22) PF_WIN();
            if (RSE) LDSW(M_RS + w * 4) = 1u;
            WAIT_V(FE == 2 ? 8 + (WPA == 22 || WPA == 24) : 0);
            if (lane == 0) stflag(ws + WS_FZ + ((pj * 16 + pi) * 4 + w) * 8, tag);
            LDSW(M_RS + w * 4) = 1u;
            if (WPA == 23) PF_WIN();
            STAMP(7);
            if (FE == 1) { dma4v(rsp, lw + T18, vA, as0 + pi * 256); dma4v(rsp, lw + T19, vA, as0 + pi * 256 + 128); }
            __builtin_amdgcn_s_setprio(KPRIO);
            if (NWA) SPIN(M_RS, 1); else SPIN(M_RL, 7);
            KVC(0); KVC(1); KVC(2); KVC(3);
            __builtin_amdgcn_s_setprio(CPRIO);
        }
        STAMP(4);
        asm volatile("" : "+v"(bsw[0]), "+v"(bsw[1]), "+v"(bsw[2]), "+v"(bsw[3]));
        f32x4 ac[4][4], a4[2];
#pragma unroll
        for (int m = 0; m < 4; ++m) {
            const float bs = bflo(bsw[m]);
#pragma unroll
            for (int l = 0; l < 4; ++l) ac[m][l] = (f32x4){bs, bs, bs, bs};
            if (m < 2) a4[m] = (f32x4){bs, bs, bs, bs};
            asm volatile("" : "+v"(ac[m][0]), "+v"(ac[m][1]), "+v"(ac[m][2]), "+v"(ac[m][3]));
        }
        asm volatile("" : "+v"(a4[0]), "+v"(a4[1]));
        const int swz = (g ^ (fr >> 1)) << 4;
        unsigned za[3] = {L0 + ZS + (16 * wm + 10 * wn) * 1024 + lane * 16, L0 + ZS + (16 * wm + 8 - 6 * wn) * 1024 + lane * 16, L0 + Z8OFF(2 * wm + wn) + lane * 16};
        asm volatile("" : "+v"(za[0]), "+v"(za[1]), "+v"(za[2]));
        unsigned ra[2][6];
        ra[0][0] = L0 + (64 * wm + 32 * wn + fr) * 128 + swz; ra[0][1] = L0 + (64 * wm + 32 * (1 - wn) + fr) * 128 + swz; ra[0][2] = ra[0][0] ^ 64; ra[0][3] = ra[0][1] ^ 64;
        ra[0][4] = L0 + SL_A + 5 * wn * 2048 + lane * 16; ra[0][5] = L0 + SL_A + 4 * 2048 + lane * 16;
#pragma unroll
        for (int i = 0; i < 6; ++i) { ra[1][i] = ra[0][i] + 2 * SLOT; asm volatile("" : "+v"(ra[0][i]), "+v"(ra[1][i])); }
        bf16x8 ax[4], bx[5], ay[4], by[5];
#define MMA_L(B, A, l) do { _Pragma("unroll") for (int m = 0; m < 4; ++m) ac[m][l] = mfma16(B[l], A[m], ac[m][l]); } while (0)
#define MMA_4(B, A) do { a4[0] = mfma16(B[4], A[0], a4[0]); a4[1] = mfma16(B[4], A[1], a4[1]); } while (0)
#define RT(i, at, o) RDP(ra[(at) >= 2 * SLOT][i], (at) - ((at) >= 2 * SLOT ? 2 * SLOT : 0) + (o))
#define RDX_B(at) do { bx[0] = RT(4, at, 0); bx[1] = RT(4, at, 2048); bx[2] = RT(4, at, 4096); bx[3] = RT(4, at, 6144); } while (0)
#define RDX_A(at) do { ax[0] = RT(0, at, 0); ax[1] = RT(0, at, 2048); ax[2] = RT(1, at, 0); ax[3] = RT(1, at, 2048); bx[4] = RT(5, at, 0); } while (0)
#define RDY_(at) do { by[0] = RT(4, at, 1024); ay[0] = RT(2, at, 0); ay[1] = RT(2, at, 2048); ay[2] = RT(3, at, 0); ay[3] = RT(3, at, 2048); \
        by[1] = RT(4, at, 3072); by[2] = RT(4, at, 5120); by[3] = RT(4, at, 7168); by[4] = RT(5, at, 1024); } while (0)
#define RDY() RDY_(SLOT)
        SCHED0();
        STAMP(8);
        WAIT_V(FE == 1 ? 4 : 5 + (WPA == 22 || WPA == 23));
        WAIT_L0();
        BAR();
        STAMP(9);
        RDX_B(T18);
        RDX_A(T18);
        SCHED0();
#define CST_A() do { } while (0)
#define CST_B() do { } while (0)
#define CST_C(t) do { } while (0)
#define CSTEP(t, at, nx, sync) do { CST_A(); \
            RDY_(at); \
            MMA_L(bx, ax, 0); MMA_L(bx, ax, 1); MMA_L(bx, ax, 2); MMA_L(bx, ax, 3); \
            SCHED_IL(9, 16); \
            sync; CST_B(); \
            RDX_B(nx); \
            MMA_4(bx, ax); MMA_L(by, ay, 0); \
            SCHED_IL(4, 6); \
            asm volatile("" : "+v"(a4[0]), "+v"(a4[1])); \
            RDX_A(nx); \
            MMA_L(by, ay, 1); MMA_L(by, ay, 2); MMA_L(by, ay, 3); MMA_4(by, ay); \
            SCHED_IL(5, 14); CST_C(t); } while (0)
        CSTEP(18, T18, T19, __builtin_amdgcn_s_waitcnt(0x0070); BAR());
        CSTEP(19, T19, 0, WAIT_LB0(); BAR());
#pragma unroll 1
        for (int t = 20; t < 48; t += 4) {
            CSTEP(t, 0, SLOT, WAIT_LB0(); BAR());
            CSTEP(t + 1, SLOT, 2 * SLOT, WAIT_LB0(); BAR());
            CSTEP(t + 2, 2 * SLOT, 3 * SLOT, WAIT_LB0(); BAR());
            CSTEP(t + 3, 3 * SLOT, 0, WAIT_LB0(); BAR());
        }
        CSTEP(48, 0, SLOT, LDSW(M_RC + w * 4) = 3u; SCHED0());
        unsigned fz1 = LDSW(M_RL + p4 * 4);
        STAMP(10);
        RDY();
        if (ZLATE) {
            MMA_L(bx, ax, 0); MMA_L(bx, ax, 1);
            SGB(0x100, 1); SCHED_IL(8, 8);
            wcheck(fz1, L0 + M_RL + p4 * 4, 10);
        }
#pragma unroll
        for (int q = 0; q < 8; ++q) zp[q] = *(LAS u32x4*)(size_t)(za[q >> 2] + (q >> 1 & 1) * 4096 + (q & 1) * 1024);
        zp[8] = *(LAS u32x4*)(size_t)za[2];
        if (!ZLATE) { MMA_L(bx, ax, 0); MMA_L(bx, ax, 1); }
        MMA_L(bx, ax, 2); MMA_L(bx, ax, 3); MMA_4(bx, ax);
        if (ZLATE) SCHED_IL(9, 10); else SCHED_IL(18, 18);
        LDSW(M_RC + w * 4) = 4u;
        SCHED0();
        MMA_L(by, ay, 0); MMA_L(by, ay, 1); MMA_L(by, ay, 2); MMA_L(by, ay, 3); MMA_4(by, ay);
        SCHED0();
        STAMP(11);
        {
            const int kap0 = 9 * (pj >> 1) + 4 * (pj & 1) + 2 * wn, kap4 = 9 * (pj >> 1) + 8;
#pragma unroll
            for (int lp = 0; lp < 2; ++lp)
#pragma unroll
                for (int m = 0; m < 4; ++m) {
                    const unsigned dst = WS_P + (pi * 8 + 4 * wm + ((m + 2 * wn) & 3)) * (72 * 1024);
                    const u32x4 z = zp[m * 2 + lp];
                    const f32x4 a = ac[m][2 * lp], c = ac[m][2 * lp + 1];
                    st16c(rs, PSTO(lane * 16), dst + (kap0 + lp) * 1024, (u32x4){mulz(z[0], lo2(a)), mulz(z[1], hi2(a)), mulz(z[2], lo2(c)), mulz(z[3], hi2(c))});
                    SCHED0();
                }
#pragma unroll
            for (int m = 0; m < 2; ++m)
                st8c(rs, PSTO((lane >> 5) * 256 + fr * 16 + (g & 1) * 8), WS_P + (pi * 8 + 4 * wm + 2 * wn + m) * (72 * 1024) + kap4 * 1024 + (pj & 1) * 512, (u32x2){mulz(zp[8][2 * m], lo2(a4[m])), mulz(zp[8][2 * m + 1], hi2(a4[m]))});
        }
        SCHED0();
        if (CVD || RGW == 5) LDSW(M_RC + w * 4) = 5u;
        STAMP(12);
        TAILS(); TAILS2();
        const unsigned lv = lane * 16, lv4 = (lane >> 3 ^ ct) & 1 ? 0x7fffff00u : lv;
        G3_LDBA(0); if (CPRE > 1) G3_LDBA(1);
        SCHED0();
        WAIT_V(5 * CPRE);
        if (lane == 0) stflag(ws + WS_FP + ((pi * 16 + pj) * 4 + w) * (8 * FPS), tag);
        __builtin_amdgcn_s_setprio(0);
        STAMP(13);
        SCHED0();
        if (CPRE < 2) G3_LDBA(1);
        G3_LDBA(2); G3_LDBA(3); G3_LDBA(4);
        SCHED0();
        if (EFP) {
            if (w < 2) { SPIN(M_RC, 4); if (EFP == 2) G3_DMA1(redo, sB, sB4); FP_ISSUE(); if (EFP == 1) G3_DMA1(redo, sB, sB4); G3_DMA2(redo, sB, sB4); STAMP(20); FP_CHECK(EFP == 1 ? 18 : 9); }
            else { FP_ISSUE(); STAMP(20); FP_CHECK(0); }
        } else {
        if (w < 2) { SPIN(M_RC, 4); G3_DMA(redo, sB, sB4); }
        FP_ISSUE();
        STAMP(20);
        FP_CHECK(0);
        }
        if (!EFP) WAIT_VB(0);
        asm volatile(".Lmega_cend:");
    }
    {
        const int lane = opqv(tid) & 63, fr = lane & 15, g = lane >> 4;
        TAILS(); TAILS2();
        f32x4 acc[4][5];
        if (LPRIO) { if (wid >= 4) __builtin_amdgcn_s_setprio(LPRIO); } else if (CRP || DRP) __builtin_amdgcn_s_setprio(0);
        STAMP(21);
#define OUTPOS() const int lane = opqv(tid) & 63, fr = lane & 15, g = lane >> 4, tA = 64 * rg + 16 * wid + fr, dA = 16 * n30 + 4 * g, dH = 16 * n34 + 8 * (ct & 1) + 4 * (g & 1)
        unsigned fy;
        u32x4 fyv, stv4;
#define LOADY() do { const int ln_ = opqv(tid) & 63; unsigned t_; \
        asm volatile("s_mov_b32 m0, %1\n s_add_i32 %0, %4, 0x1fc00\n buffer_load_dwordx4 %2, %3, %4 offen sc1 lds\n buffer_load_dwordx4 %2, %3, %0 offen offset:1024 sc1 lds\n" \
                     "s_add_i32 %0, %4, 0x3f800\n s_mov_b64 exec, 0xffff\n buffer_load_dwordx4 %2, %3, %0 offen offset:2048 sc1 lds\n s_mov_b64 exec, -1" \
                     : "=&s"(t_) : "s"(L0 + redo + wid * 4608), "v"((ln_ >> 1) * (S * 2) + (ln_ & 1) * 16), "s"(wr), "s"(RFL(WS_YT + (72 * ct * S + 64 * rg + 16 * wid) * 2)) : "scc", "memory"); } while (0)
        LAS char* red = lds + redo;
#define PUT(mm) do { _Pragma("unroll") for (int l = 0; l < 4; ++l) *(LAS f32x4*)(red + MG(mm) * 4608 + l * 1024 + lane * 16) = acc[mm][l]; } while (0)
#define PUTH(mm) (*(LAS f32x4*)(red + MG(mm) * 4608 + 4096 + (lane & 31) * 16) = acc[mm][4])
        {
            bf16x8 aq[9][4];
            unsigned fw;
            const int fwo = wid_ == 2 || (wid_ == 3 && b_ < 64) ? M_FL + 12 : wid_ == 3 ? M_FL + 8 : M_RC;
            if (LW && wid >= 4) wcheck(0u, L0 + M_RC + (LW == 1 ? (lane & 3) : (wid & 3)) * 4, 5);
#define PF_P() pf4c<0>(wr, L0 + L_DUMP, lane < 32 ? (lane >> 3) * (72 * 1024) + (lane & 7) * 128 : 0x7fffff00u, sA + (ct + 1) * 1024)
            if (PPF == 1) { PF_P(); SCHED0(); }
            G3_LDA(0);
            if (PPF == 2) { SCHED0(); PF_P(); SCHED0(); }
            if (LDD > 1) G3_LDA(1); if (LDD > 2) G3_LDA(2); if (LDD > 3) G3_LDA(3);
            SCHED0();
            if (PPF == 3) { PF_P(); SCHED0(); }
            if (LPRIO && LPE < 0) __builtin_amdgcn_s_setprio(0);
            SCHED0();
#pragma unroll
            for (int i = 0; i < 9; ++i) {
                if (i == 8) {
                    asm volatile("" : "+v"(aq[8][0]), "+v"(aq[8][1]), "+v"(aq[8][2]), "+v"(aq[8][3]), "+v"(bq[8][0]), "+v"(bq[8][1]), "+v"(bq[8][2]), "+v"(bq[8][3]), "+v"(bq[8][4]) :: "memory");
                    if (wid < 4) {
                        const unsigned ln = opqv(tid) & 63;
                        fy = WS_FY + 576 * ct + (ln < 35 ? ln : 35) * 16; fyv = ld16c(rs, fy, 0);
                        stv4 = ld16c(rs, (ln & 14) * 8, WS_ST1 + (64 * rg + 16 * wid) * 8);
                        unsigned xo[3];
#pragma unroll
                        for (int j = 0; j < 3; ++j) { const unsigned c = ln + 64 * j; xo[j] = c * 16 + (c * 57 >> 9) * (D * 2 - 144) - j * 1024; }
                        asm volatile("s_mov_b32 m0, %0\n s_nop 0\n buffer_load_dwordx4 %1, %4, %5 offen lds\n buffer_load_dwordx4 %2, %4, %5 offen offset:1024 lds\n"
                                     "s_mov_b64 exec, 0xffff\n buffer_load_dwordx4 %3, %4, %5 offen offset:2048 lds\n s_mov_b64 exec, -1"
                                     :: "s"(L0 + redo + wid * 4608 + 2304), "v"(xo[0]), "v"(xo[1]), "v"(xo[2]), "s"(mk_rsrc(x, SZ_X)), "s"(RFL((64 * rg + 16 * wid) * (D * 2) + 144 * ct)) : "memory");
                    }
                    SCHED0();
                }
#pragma unroll
                for (int m = 0; m < 4; ++m) {
#pragma unroll
                    for (int l = 0; l < 5; ++l) acc[m][l] = mfma16(bq[i][l], aq[i][m], i == 0 ? zero4 : acc[m][l]);
                    if (WIL && i == 8 && m > 0) { PUT(m - 1); _Pragma("unroll") for (int l = 0; l < 4; ++l) { SGB(0x008, 1); SGB(0x200, 1); } SGB(0x008, 1); }
                    if (WIL && i == 8) SCHED0();
#define G3_LDA1(i, m) (aq[i][m] = __builtin_bit_cast(bf16x8, ld16c(rs, lane * 16, sA + MG(m) * (72 * 1024) + KAP(i) * 1024)))
                    if (LDI == 1 && i + LDD < 9) { SCHED0(); G3_LDA1(i + LDD, m); SCHED0(); }
                    if (LDI == 2 && i == 0) { SCHED0(); G3_LDA1(4, m); SCHED0(); }
                    if (LDI == 2 && (i == 1 || i == 2)) { G3_LDA1(2 * i + 3 + (m >> 1), (2 * m) & 3); G3_LDA1(2 * i + 3 + (m >> 1), (2 * m + 1) & 3);
                        SGB(0x008, 2); SGB(0x020, 1); SGB(0x008, 3); SGB(0x020, 1); SCHED0(); }
                    if (LDI == 3 && i >= 1 && i < 4 && (i < 3 || m < 2)) { G3_LDA1(2 * i + 2 + (m >> 1), (2 * m) & 3); G3_LDA1(2 * i + 2 + (m >> 1), (2 * m + 1) & 3);
                        SGB(0x008, 1); SGB(0x020, 1); SGB(0x008, 2); SGB(0x020, 1); SGB(0x008, 2); SCHED0(); }
                }
                SCHED0();
                XST(30 + i);
                if (!LDI && 2 * i + 4 < 9) G3_LDA(2 * i + 4);
                if (!LDI && 2 * i + 5 < 9) G3_LDA(2 * i + 5);
                if (LW && i == LWI) { SCHED0(); if (wid < 4) LDSW(M_RC + wid * 4) = 5u; SCHED0(); }
                if (LPRIO && LPE == i) __builtin_amdgcn_s_setprio(0);
                if (i == 2) fw = LDSW(fwo);
                if (i == 3) wcheck(fw, L0 + fwo, 4);
                if (i >= 3 && i + 2 < 9) G3_RDB(i + 2);
                SCHED0();
            }
        }
        STAMP(22);
        if (!WIL) { PUT(0); PUT(1); PUT(2); }
        if ((g >> 1) == (ct & 1)) { PUTH(0); PUTH(1); PUTH(2); }
        if (wid < 4) {
            STAMP(23);
            f32x4* o = acc[3];
            const u16* out = KARG(11);
            FENCE();
            LDSW(M_RC + wid * 4) = 6u;
            asm volatile("s_waitcnt vmcnt(0)" : "+v"(fyv), "+v"(stv4) :: "memory");
            OUTPOS();
            u32x2 xv[5];
#pragma unroll
            for (int u = 0; u < 5; ++u) xv[u] = *(LAS u32x2*)(red + wid * 4608 + 2304 + fr * 144 + (u < 4 ? (ct & 1) * 16 + u * 32 + g * 8 : ((ct & 1) ? 0 : 128) + (g & 1) * 8));
            for (unsigned sp = 0; sp < SPIN_CAP; ++sp) {
                if (__builtin_amdgcn_ballot_w64(((fyv[0] ^ (unsigned)tag) | (fyv[1] ^ (unsigned)(tag >> 32)) | (fyv[2] ^ (unsigned)tag) | (fyv[3] ^ (unsigned)(tag >> 32))) != 0u) == 0ull) break;
                FENCE();
                fyv = ld16c(rs, fy, 0);
            }
            FENCE();
            LOADY();
            XST(39);
            const float mb = __uint_as_float((fr & 1) ? stv4[1] : stv4[0]), rd = __uint_as_float((fr & 1) ? stv4[3] : stv4[2]);
            f32x4 xx[5], sr[5];
#pragma unroll
            for (int u = 0; u < 5; ++u)
#pragma unroll
                for (int h = 0; h < 2; ++h) {
                    const float x0 = bflo(xv[u][h]), x1 = bfhi(xv[u][h]);
                    unsigned q = pk2(adds(x0, -mb), adds(x1, -mb));
                    q = pk2(muls(bflo(q), rd), muls(bfhi(q), rd));
                    xx[u][2 * h] = x0; xx[u][2 * h + 1] = x1;
                    sr[u][2 * h] = muls(bflo(q), rd); sr[u][2 * h + 1] = muls(bfhi(q), rd);
                }
            unsigned r0 = L0 + wid * 4608 + lane * 16, r7 = r0 + redoff(7), h0 = r0 + 4096 - (ct & 1) * 512, h7 = h0 + redoff(7);
            asm volatile("" : "+v"(r0), "+v"(r7), "+v"(h0), "+v"(h7));
#define PART(v, u) (*(LAS f32x4*)(size_t)((v) == 6 ? ((u) < 4 ? r0 + (redoff(6) + (u) * 1024) : h0 + redoff(6)) : (u) < 4 ? r7 + (redoff(v) - redoff(7) + (u) * 1024) : h7 + (redoff(v) - redoff(7))))
            unsigned oo = W16 ? (tA * D + 16 * n30 + 16 * (g & 1) + 8 * (g >> 1)) * 2 : (tA * D + dA) * 2, oh = (g >> 1) == (ct & 1) ? (tA * D + dH) * 2 : 0x7fffff00u;
            float ke = muls(rd, -1.4426950408889634f);
            SCHED0();
            wcheck(0u, L0 + M_RC + (lane & 3) * 4, 6);
#pragma unroll
            for (int v = 1; v < 4; ++v) {
                const unsigned ro = redoff(0) + ((wid + v) & 3) * RED_W, a0 = r0 + ro, a4 = h0 + ro;
                f32x4 pp[5];
#pragma unroll
                for (int u = 0; u < 5; ++u) pp[u] = *(LAS f32x4*)(size_t)(u < 4 ? a0 + u * 1024 : a4);
#pragma unroll
                for (int u = 0; u < 5; ++u)
#pragma unroll
                    for (int rr = 0; rr < 4; ++rr) o[u][rr] = adds(o[u][rr], pp[u][rr]);
            }
            SCHED0();
            WAIT_V(0);
            {
                LAS char* yb = red + wid * 4608 + lane * 8;
#pragma unroll
                for (int u = 0; u < 5; ++u) {
                    const u32x2 yw = __builtin_bit_cast(u32x2, trread(u < 4 ? yb + (ct & 1) * 256 + u * 512 : yb + ((ct & 1) ? -256 : 2048)));
#pragma unroll
                    for (int h = 0; h < 2; ++h) { sr[u][2 * h] = muls(sr[u][2 * h], bflo(yw[h])); sr[u][2 * h + 1] = muls(sr[u][2 * h + 1], bfhi(yw[h])); }
                }
            }
#pragma unroll
            for (int u = 0; u < 5; ++u) asm volatile("" : "+v"(o[u]), "+v"(xx[u]), "+v"(sr[u]));
            asm volatile("" : "+v"(oo), "+v"(oh), "+v"(ke));
            XST(40);
            asm volatile("s_barrier\n.Lmega_tail:" ::: "memory");
            STAMP(24);
            f32x4 pp[5][4];
            u32x2 rs2[5];
#pragma unroll
            for (int u = 0; u < 5; ++u)
#pragma unroll
                for (int v = 0; v < 4; ++v) { pp[u][v] = PART(v + 4, u); SCHED0(); }
#pragma unroll
            for (int u = 0; u < 5; ++u) {
                const f32x4 z = o[u] + ((pp[u][0] + pp[u][1]) + (pp[u][2] + pp[u][3]));
                u32x2& res = rs2[u];
#pragma unroll
                for (int h = 0; h < 2; ++h) {
                    const f32x2 one = {1.0f, 1.0f};
                    f32x2 zz = h ? hi2(z) : lo2(z);
                    const f32x2 s2 = h ? hi2(sr[u]) : lo2(sr[u]), x2 = h ? hi2(xx[u]) : lo2(xx[u]);
                    const f32x2 e = zz * (f32x2){ke, ke};
                    const f32x2 dn = (f32x2){__builtin_amdgcn_exp2f(e.x), __builtin_amdgcn_exp2f(e.y)} + one;
                    res[h] = pk2v(fma2(zz * s2, (f32x2){__builtin_amdgcn_rcpf(dn.x), __builtin_amdgcn_rcpf(dn.y)}, x2));
                }
                if (!W16 || u == 4) __builtin_amdgcn_raw_buffer_store_b64(res, in_rsrc(out, SZ_X), u < 4 ? oo + 32 * u : oh, 0, OAUX);
                else if (u & 1) {
                    const u32x2 s0 = __builtin_amdgcn_permlane16_swap(rs2[u - 1][0], res[0], false, false), s1 = __builtin_amdgcn_permlane16_swap(rs2[u - 1][1], res[1], false, false);
                    const u32x4 q = {s0[0], s1[0], s0[1], s1[1]};
                    __builtin_amdgcn_raw_buffer_store_b128(q, in_rsrc(out, SZ_X), oo + 32 * (u - 1), 0, OAUX);
                    asm volatile("s_nop 1" :: "v"(q));
                }
            }
        } else {
            PUT(3);
            if ((g >> 1) == (ct & 1)) PUTH(3);
            STAMP(23);
            asm volatile("s_waitcnt lgkmcnt(0)\n s_barrier" ::: "memory");
            STAMP(24);
        }
    }
}

extern "C" void kernel_launch(void* const* d_in, const int* in_sizes, int n_in,
                              void* d_out, int out_size, void* d_ws, size_t ws_size,
                              hipStream_t stream) {
    (void)hipFuncSetAttribute((const void*)mega, hipFuncAttributeMaxDynamicSharedMemorySize, LDS_SZ);
    mega<<<256, 512, LDS_SZ, stream>>>((const u16*)d_in[0], (const u16*)d_in[7], (char*)d_ws, (const u16*)d_in[8], (const u16*)d_in[9], (const u16*)d_in[1], (const u16*)d_in[2],
                                       (const u16*)d_in[3], (const u16*)d_in[4], (const u16*)d_in[5], (const u16*)d_in[10], (u16*)d_out);
}
```
